```python
import torch

def _softmax(x, axis=-1):
    return torch.softmax(x, dim=axis)
def _gelu(x):
    return torch.nn.functional.gelu(x, approximate="tanh")
def _silu(x):
    return torch.nn.functional.silu(x)
def _sigmoid(x):
    return torch.sigmoid(x)
def _layernorm(x, axis=-1, eps=1e-5):
    m = x.mean(dim=axis, keepdim=True)
    v = x.var(dim=axis, keepdim=True, unbiased=False)
    return (x - m) / torch.sqrt(v + eps)
def _scatter_add(tgt, idx, src, axis=0):
    return tgt.clone().index_add_(axis, idx, src)
def _segment_sum(src, seg, n):
    out = torch.zeros((n,) + src.shape[1:], dtype=src.dtype)
    return out.index_add_(0, seg, src)
def _pad(x, pw):
    flat = tuple(v for pair in reversed(pw) for v in pair)
    return torch.nn.functional.pad(x, flat)

kernel_name = 'standalone__dense_activation_silu__prompt'

def setup_inputs():

    _w = (lambda a: (lambda t: t.to(torch.bfloat16) if t.is_floating_point() else t)(torch.as_tensor(a)))
    B, S, D, F = 1, 2048, 576, 4608
    return {
        "x": _w((lambda *s: torch.randn(*s, dtype=torch.bfloat16) * 0.1)(B, S, D)),
        "w1": _w((lambda *s: torch.randn(*s, dtype=torch.bfloat16) * 3.2)(D, F) * (D ** -0.5)),
        "w2": _w((lambda *s: torch.randn(*s, dtype=torch.bfloat16) * 3.2)(F, D) * (F ** -0.5)),
    }

def reference(x, w1, w2):
    h = torch.einsum("bsd,df->bsf", x, w1)
    h = _silu(h)
    out = torch.einsum("bsf,fd->bsd", h, w2)
    return out
```

```cpp
#include <hip/hip_runtime.h>
#include <hip/hip_bf16.h>
typedef __attribute__((ext_vector_type(8))) short bf16x8;
typedef __attribute__((ext_vector_type(4))) float f32x4;
typedef unsigned short u16;

__device__ __forceinline__ u16 f2bf(float f){ __hip_bfloat16 b = __float2bfloat16(f); return *(u16*)&b; }

template<int ACT>
__global__ void gemm_naive(const u16* __restrict__ A, const u16* __restrict__ B, u16* __restrict__ C, int M, int N, int K){
  int wave = (blockIdx.x * blockDim.x + threadIdx.x) >> 6;
  int lane = threadIdx.x & 63;
  int tn = wave % (N/16), tm = wave / (N/16);
  if (tm >= M/16) return;
  f32x4 acc = {0,0,0,0};
  int r = lane & 15, q = lane >> 4;
  for (int k0 = 0; k0 < K; k0 += 32) {
    bf16x8 a = *(const bf16x8*)(A + (size_t)(tm*16 + r)*K + k0 + q*8);
    bf16x8 b;
    for (int j = 0; j < 8; ++j) b[j] = (short)B[(size_t)(k0 + q*8 + j)*N + tn*16 + r];
    acc = __builtin_amdgcn_mfma_f32_16x16x32_bf16(a, b, acc, 0,0,0);
  }
  for (int j = 0; j < 4; ++j) {
    float v = acc[j];
    if (ACT) v = v / (1.f + __expf(-v));
    C[(size_t)(tm*16 + q*4 + j)*N + tn*16 + r] = f2bf(v);
  }
}

extern "C" void kernel_launch(void* const* d_in, const int* in_sizes, int n_in,
                              void* d_out, int out_size, void* d_ws, size_t ws_size,
                              hipStream_t stream) {
  const u16* x = (const u16*)d_in[0];
  const u16* w1 = (const u16*)d_in[1];
  const u16* w2 = (const u16*)d_in[2];
  u16* h = (u16*)d_ws;
  u16* out = (u16*)d_out;
  const int M = 2048, D = 576, F = 4608;
  gemm_naive<1><<<(M/16)*(F/16)/4, 256, 0, stream>>>(x, w1, h, M, F, D);
  gemm_naive<0><<<(M/16)*(D/16)/4, 256, 0, stream>>>(h, w2, out, M, D, F);
}
```

```cpp
#include <hip/hip_runtime.h>

__global__ __launch_bounds__(512, 2) __attribute__((aligned(4096))) void fused_mlp_kernel(const void* x, const void* w1, const void* w2, void* out, void* ws) {
  unsigned bid = blockIdx.x;
  unsigned wv = threadIdx.x;
  asm volatile(
    "s_load_dwordx8 s[40:47], %1, 0x0\n\t"
    "s_load_dwordx2 s[48:49], %1, 0x20\n\t"
    "s_getpc_b64 s[80:81]\n\t"
    "LPC_%=:\n\t"
    "s_lshl_b32 s82, %2, 8\n\t"
    "s_and_b32 s82, s82, 0xf800\n\t"
    "v_lshlrev_b32 v11, 2, v0\n\t"
    "v_and_b32 v11, 0x780, v11\n\t"
    "v_or_b32 v11, s82, v11\n\t"
    "v_min_u32 v11, (LEND_%=-LPC_%=-4)&0xfffffc, v11\n\t"
    "global_load_dword v11, v11, s[80:81]\n\t"
    "v_readfirstlane_b32 s89, v0\n\t"
    "s_mov_b32 s88, %2\n\t"
    "s_nop 2\n\t"
    "s_lshr_b32 s89, s89, 6\n\t"
    "v_mbcnt_lo_u32_b32 v15, -1, 0\n\t"
    "v_mbcnt_hi_u32_b32 v15, -1, v15\n\t"
    "s_and_b32 s91, s88, 3\n\t"
    "s_bfe_u32 s90, s88, 0x20003\n\t"
    "s_lshr_b32 s75, s88, 5\n\t"
    "s_bfe_u32 s76, s88, 0x10002\n\t"
    "s_lshl_b32 s76, s76, 3\n\t"
    "s_add_u32 s92, s75, s76\n\t"
    "s_lshl_b32 s36, s89, 10\n\t"
    "s_mul_i32 s37, s92, 147456\n\t"
    "s_mul_i32 s38, s91, 2304\n\t"
    "s_mul_i32 s74, s90, 640\n\t"
    "s_add_u32 s38, s38, s74\n\t"
    "s_add_u32 s74, s90, s75\n\t"
    "s_lshl_b32 s74, s74, 2\n\t"
    "s_mov_b32 s76, 0x64206420\n\t"
    "s_mov_b32 s77, 0x64206420\n\t"
    "s_lshr_b64 s[76:77], s[76:77], s74\n\t"
    "s_and_b32 s74, s76, 15\n\t"
    "s_lshl_b32 s9, s74, 7\n\t"
    "s_add_u32 s9, s9, s37\n\t"
    "s_add_u32 s10, s37, 1152\n\t"
    "s_mul_i32 s0, s74, 589824\n\t"
    "s_add_u32 s0, s0, s38\n\t"
    "s_add_u32 s38, s38, 5308416\n\t"
    "v_lshrrev_b32 v12, 3, v15\n\t"
    "s_lshl_b32 s74, s89, 3\n\t"
    "v_add_u32 v12, s74, v12\n\t"
    "v_and_b32 v13, 7, v15\n\t"
    "v_bfe_u32 v14, v12, 1, 3\n\t"
    "v_xor_b32 v14, v14, v13\n\t"
    "v_lshlrev_b32 v14, 4, v14\n\t"
    "s_lshl_b32 s74, s91, 3\n\t"
    "s_and_b32 s74, s74, 16\n\t"
    "v_xor_b32 v216, s74, v12\n\t"
    "v_mul_u32_u24 v216, 1152, v216\n\t"
    "v_add_u32 v216, v216, v14\n\t"
    "s_mul_i32 s74, s89, 27\n\t"
    "v_min_u32 v16, 26, v15\n\t"
    "v_add_u32 v16, s74, v16\n\t"
    "v_min_u32 v16, 215, v16\n\t"
    "v_mul_u32_u24 v18, 3641, v16\n\t"
    "v_lshrrev_b32 v18, 16, v18\n\t"
    "v_mul_u32_u24 v18, 6912, v18\n\t"
    "v_lshlrev_b32 v16, 7, v16\n\t"
    "v_add_u32 v18, v18, v16\n\t"
    "s_mul_i32 s74, s89, 6\n\t"
    "v_min_u32 v19, 5, v15\n\t"
    "v_add_u32 v19, s74, v19\n\t"
    "v_min_u32 v19, 47, v19\n\t"
    "v_lshrrev_b32 v18, 2, v19\n\t"
    "v_mul_u32_u24 v18, 6656, v18\n\t"
    "v_mul_u32_u24 v19, 640, v19\n\t"
    "v_add_u32 v19, v19, v18\n\t"
    "s_mul_i32 s74, s89, 9\n\t"
    "v_min_u32 v18, 8, v15\n\t"
    "v_add_u32 v18, s74, v18\n\t"
    "v_min_u32 v18, 71, v18\n\t"
    "v_mul_u32_u24 v14, 10923, v18\n\t"
    "v_lshrrev_b32 v14, 16, v14\n\t"
    "v_mul_u32_u24 v14, 7296, v14\n\t"
    "v_and_b32 v17, 1, v18\n\t"
    "v_lshl_add_u32 v14, v17, 7, v14\n\t"
    "v_lshrrev_b32 v18, 1, v18\n\t"
    "v_mul_u32_u24 v18, 640, v18\n\t"
    "v_add_u32 v18, v18, v14\n\t"
    "s_lshr_b32 s74, s88, 2\n\t"
    "s_lshr_b32 s75, s88, 5\n\t"
    "s_lshl_b32 s75, s75, 1\n\t"
    "s_sub_u32 s74, s74, s75\n\t"
    "s_mul_i32 s11, s74, 27648\n\t"
    "s_mul_i32 s75, s91, 1327104\n\t"
    "s_add_u32 s11, s11, s75\n\t"
    "s_mul_i32 s61, s74, 110592\n\t"
    "s_mul_i32 s75, s91, 2304\n\t"
    "s_add_u32 s61, s61, s75\n\t"
    "s_lshl2_add_u32 s74, s90, s91\n\t"
    "s_lshl3_add_u32 s74, s74, s89\n\t"
    "s_mul_i32 s62, s74, 1152\n\t"
    "s_add_u32 s62, s62, s37\n\t"
    "s_waitcnt lgkmcnt(0)\n\t"
    "s_mov_b64 s[16:17], s[40:41]\n\t"
    "s_and_b32 s17, s17, 0xffff\n\t"
    "s_mov_b32 s18, 0x240000\n\t"
    "s_mov_b32 s19, 0x00020000\n\t"
    "s_mov_b64 s[20:21], s[42:43]\n\t"
    "s_and_b32 s21, s21, 0xffff\n\t"
    "s_mov_b32 s22, 0x510000\n\t"
    "s_mov_b32 s23, 0x00020000\n\t"
    "s_mov_b64 s[28:29], s[48:49]\n\t"
    "s_mov_b64 s[24:25], s[44:45]\n\t"
    "s_mov_b64 s[32:33], s[46:47]\n\t"
    "s_add_u32 m0, s36, 147456\n\t"
    "s_add_u32 s85, s9, 73728\n\t"
    "buffer_load_dwordx4 v216, s[16:19], s9 offen lds\n\t"
    "s_add_u32 m0, s36, 155648\n\t"
    "s_add_u32 s9, s9, 128\n\t"
    "buffer_load_dwordx4 v216, s[16:19], s85 offen lds\n\t"
    "s_cmp_eq_u32 s9, s10\n\t"
    "s_cselect_b32 s9, s37, s9\n\t"
    "v_and_b32 v14, 2, v12\n\t"
    "v_bfe_u32 v4, v12, 3, 1\n\t"
    "v_lshl_or_b32 v14, v4, 2, v14\n\t"
    "v_xor_b32 v14, v14, v13\n\t"
    "v_lshlrev_b32 v14, 4, v14\n\t"
    "v_mul_u32_u24 v4, 9216, v12\n\t"
    "v_add_u32 v4, v4, v14\n\t"
    "s_add_u32 m0, s36, 0\n\t"
    "s_add_u32 s85, s0, 0x0\n\t"
    "buffer_load_dwordx4 v4, s[20:23], s85 offen lds\n\t"
    "s_add_u32 m0, s36, 8192\n\t"
    "s_add_u32 s85, s0, 0x80\n\t"
    "buffer_load_dwordx4 v4, s[20:23], s85 offen lds\n\t"
    "s_add_u32 s1, s0, 589824\n\t"
    "s_sub_u32 s84, s1, 5308416\n\t"
    "s_cmp_ge_u32 s1, s38\n\t"
    "s_cselect_b32 s1, s84, s1\n\t"
    "s_add_u32 s2, s1, 589824\n\t"
    "s_sub_u32 s84, s2, 5308416\n\t"
    "s_cmp_ge_u32 s2, s38\n\t"
    "s_cselect_b32 s2, s84, s2\n\t"
    "s_add_u32 s3, s2, 589824\n\t"
    "s_sub_u32 s84, s3, 5308416\n\t"
    "s_cmp_ge_u32 s3, s38\n\t"
    "s_cselect_b32 s3, s84, s3\n\t"
    "s_add_u32 s4, s3, 589824\n\t"
    "s_sub_u32 s84, s4, 5308416\n\t"
    "s_cmp_ge_u32 s4, s38\n\t"
    "s_cselect_b32 s4, s84, s4\n\t"
    "s_add_u32 s5, s4, 589824\n\t"
    "s_sub_u32 s84, s5, 5308416\n\t"
    "s_cmp_ge_u32 s5, s38\n\t"
    "s_cselect_b32 s5, s84, s5\n\t"
    "s_add_u32 s6, s5, 589824\n\t"
    "s_sub_u32 s84, s6, 5308416\n\t"
    "s_cmp_ge_u32 s6, s38\n\t"
    "s_cselect_b32 s6, s84, s6\n\t"
    "s_add_u32 s7, s6, 589824\n\t"
    "s_sub_u32 s84, s7, 5308416\n\t"
    "s_cmp_ge_u32 s7, s38\n\t"
    "s_cselect_b32 s7, s84, s7\n\t"
    "s_add_u32 s8, s7, 589824\n\t"
    "s_sub_u32 s84, s8, 5308416\n\t"
    "s_cmp_ge_u32 s8, s38\n\t"
    "s_cselect_b32 s8, s84, s8\n\t"
    "v_and_b32 v14, 6, v12\n\t"
    "v_xor_b32 v14, v14, v13\n\t"
    "v_lshlrev_b32 v14, 4, v14\n\t"
    "v_mul_u32_u24 v5, 1152, v12\n\t"
    "v_add_u32 v5, v5, v14\n\t"
    "s_and_b32 s29, s29, 0xffff\n\t"
    "s_mov_b32 s30, 0x2c00000\n\t"
    "s_mov_b32 s31, 0x00020000\n\t"
    "s_and_b32 s25, s25, 0xffff\n\t"
    "s_mov_b32 s26, 0x510000\n\t"
    "s_mov_b32 s27, 0x00020000\n\t"
    "s_and_b32 s33, s33, 0xffff\n\t"
    "s_mov_b32 s34, 0x240000\n\t"
    "s_mov_b32 s35, 0x00020000\n\t"
    "s_and_b32 s93, s89, 3\n\t"
    "s_lshr_b32 s94, s89, 2\n\t"
    "v_lshlrev_b32 v6, 4, v15\n\t"
    "v_lshlrev_b32 v7, 2, v15\n\t"
    "v_bfe_u32 v12, v15, 3, 2\n\t"
    "s_lshl_b32 s74, s94, 1\n\t"
    "v_xor_b32 v12, s74, v12\n\t"
    "v_lshlrev_b32 v12, 5, v12\n\t"
    "v_and_b32 v13, 3, v15\n\t"
    "v_lshl_add_u32 v12, v13, 3, v12\n\t"
    "v_bfe_u32 v13, v15, 2, 2\n\t"
    "v_lshl_add_u32 v12, v13, 7, v12\n\t"
    "v_lshrrev_b32 v13, 4, v15\n\t"
    "v_lshl_add_u32 v0, v13, 10, v12\n\t"
    "v_xor_b32 v1, 32, v0\n\t"
    "s_lshl_b32 s74, s94, 12\n\t"
    "v_lshlrev_b32 v3, 9, v13\n\t"
    "v_sub_u32 v3, s74, v3\n\t"
    "v_bfe_u32 v14, v15, 1, 3\n\t"
    "v_xor_b32 v14, v14, v13\n\t"
    "v_and_b32 v12, 15, v15\n\t"
    "s_lshl_b32 s74, s93, 12\n\t"
    "v_lshl_add_u32 v220, v12, 7, s74\n\t"
    "v_lshl_add_u32 v220, v14, 4, v220\n\t"
    "v_xor_b32 v221, 64, v220\n\t"
    "v_mul_u32_u24 v9, 1152, v12\n\t"
    "v_lshl_add_u32 v9, v13, 3, v9\n\t"
    "s_lshl_b32 s74, s91, 6\n\t"
    "s_and_b32 s74, s74, 64\n\t"
    "s_mul_i32 s75, s94, 96\n\t"
    "s_xor_b32 s74, s74, s75\n\t"
    "v_add_u32 v3, v0, v3\n\t"
    "v_xor_b32 v3, s74, v3\n\t"
    "v_sub_u32 v3, v3, v0\n\t"
    "s_andn2_b32 s74, s88, 0x18\n\t"
    "s_andn2_b32 s75, s88, 3\n\t"
    "v_and_b32 v12, 3, v15\n\t"
    "v_lshl_or_b32 v13, v12, 3, s74\n\t"
    "v_or_b32 v12, s75, v12\n\t"
    "v_cmp_gt_u32 vcc, 4, v15\n\t"
    "s_nop 1\n\t"
    "v_cndmask_b32 v12, v12, v13, vcc\n\t"
    "v_lshlrev_b32 v12, 7, v12\n\t"
    "s_mov_b32 s74, 0x2a00000\n\t"
    "buffer_load_dword v8, v12, s[28:31], s74 offen sc1\n\t"
    "s_cmp_eq_u32 s90, 3\n\t"
    "s_cbranch_scc1 LTYPEB_%=\n\t"
    "s_mov_b32 s96, s90\n\t"
    "s_add_u32 s97, s90, 1\n\t"
    "s_cmp_eq_u32 s97, 3\n\t"
    "s_cselect_b32 s97, 0, s97\n\t"
    "s_add_u32 s98, s97, 1\n\t"
    "s_cmp_eq_u32 s98, 3\n\t"
    "s_cselect_b32 s98, 0, s98\n\t"
    "s_mov_b32 s99, 3\n\t"
    "s_mul_i32 s74, s91, 1327104\n\t"
    "s_lshl_b32 s75, s90, 8\n\t"
    "s_add_u32 s74, s74, s75\n\t"
    "s_lshl_b32 s75, s91, 6\n\t"
    "s_and_b32 s75, s75, 128\n\t"
    "s_xor_b32 s76, s75, 128\n\t"
    "s_add_u32 s74, s74, s76\n\t"
    "s_sub_u32 s100, s75, s76\n\t"
    "s_sub_u32 s101, 256, s76\n\t"
    "s_mul_i32 s75, s92, 72\n\t"
    "s_mul_i32 s76, s91, 18\n\t"
    "s_add_u32 s75, s75, s76\n\t"
    "s_lshl_b32 s75, s75, 3\n\t"
    "s_add_u32 s75, s75, s89\n\t"
    "s_mul_i32 s75, s75, 2304\n\t"
    "s_mul_i32 s76, s96, 368640\n\t"
    "s_add_u32 s39, s74, s76\n\t"
    "s_mul_i32 s76, s96, 92160\n\t"
    "s_add_u32 s43, s75, s76\n\t"
    "s_mul_i32 s76, s97, 368640\n\t"
    "s_add_u32 s40, s74, s76\n\t"
    "s_mul_i32 s76, s97, 92160\n\t"
    "s_add_u32 s44, s75, s76\n\t"
    "s_mul_i32 s76, s98, 368640\n\t"
    "s_add_u32 s41, s74, s76\n\t"
    "s_mul_i32 s76, s98, 92160\n\t"
    "s_add_u32 s45, s75, s76\n\t"
    "s_mul_i32 s76, s99, 368640\n\t"
    "s_add_u32 s42, s74, s76\n\t"
    "s_mul_i32 s76, s99, 92160\n\t"
    "s_add_u32 s46, s75, s76\n\t"
    "s_mul_i32 s76, s90, 92160\n\t"
    "s_add_u32 s47, s75, s76\n\t"
    "s_lshl_b32 s74, s92, 2\n\t"
    "s_add_u32 s74, s74, s90\n\t"
    "s_lshl_b32 s74, s74, 2\n\t"
    "s_mul_i32 s75, s89, 3072\n\t"
    "s_add_u32 s75, s75, 0x1600000\n\t"
    "s_add_u32 s76, s74, s91\n\t"
    "s_mul_i32 s76, s76, 73728\n\t"
    "s_add_u32 s64, s76, s75\n\t"
    "s_xor_b32 s76, s91, 1\n\t"
    "s_add_u32 s76, s76, s74\n\t"
    "s_mul_i32 s76, s76, 73728\n\t"
    "s_add_u32 s76, s76, s75\n\t"
    "s_add_u32 s69, s76, 0\n\t"
    "s_xor_b32 s76, s91, 2\n\t"
    "s_add_u32 s76, s76, s74\n\t"
    "s_mul_i32 s76, s76, 73728\n\t"
    "s_add_u32 s76, s76, s75\n\t"
    "s_add_u32 s70, s76, 24576\n\t"
    "s_xor_b32 s76, s91, 3\n\t"
    "s_add_u32 s76, s76, s74\n\t"
    "s_mul_i32 s76, s76, 73728\n\t"
    "s_add_u32 s76, s76, s75\n\t"
    "s_add_u32 s71, s76, 49152\n\t"
    "s_lshl_b32 s74, s92, 2\n\t"
    "s_add_u32 s74, s74, s93\n\t"
    "s_lshl_b32 s74, s74, 1\n\t"
    "s_lshr_b32 s75, s91, 1\n\t"
    "s_add_u32 s74, s74, s75\n\t"
    "s_mul_i32 s74, s74, 18432\n\t"
    "s_lshl_b32 s75, s90, 8\n\t"
    "s_add_u32 s74, s74, s75\n\t"
    "s_lshl_b32 s75, s91, 6\n\t"
    "s_add_u32 s74, s74, s75\n\t"
    "s_lshl_b32 s75, s94, 5\n\t"
    "s_add_u32 s62, s74, s75\n\t"
    "s_mul_i32 s74, s100, -144\n\t"
    "s_add_u32 s67, s62, s74\n\t"
    "s_sub_u32 s74, s101, s100\n\t"
    "s_add_u32 s68, s62, s74\n\t"
    "s_lshl_b32 s74, s88, 7\n\t"
    "s_add_u32 s95, s74, 0x2a00000\n\t"
    "s_mov_b32 s63, 0x8000\n\t"
    "s_mov_b32 s72, 0x0c0c0400\n\t"
    "s_mov_b32 s73, 0x04000c0c\n\t"
    "s_add_u32 m0, s36, 131072\n\t"
    "s_add_u32 s85, s9, 73728\n\t"
    "buffer_load_dwordx4 v216, s[16:19], s9 offen lds\n\t"
    "s_add_u32 m0, s36, 139264\n\t"
    "s_add_u32 s9, s9, 128\n\t"
    "buffer_load_dwordx4 v216, s[16:19], s85 offen lds\n\t"
    "s_cmp_eq_u32 s9, s10\n\t"
    "s_cselect_b32 s9, s37, s9\n\t"
    "s_add_u32 m0, s36, 16384\n\t"
    "s_add_u32 s85, s1, 0x0\n\t"
    "buffer_load_dwordx4 v4, s[20:23], s85 offen lds\n\t"
    "s_add_u32 m0, s36, 24576\n\t"
    "s_add_u32 s85, s1, 0x80\n\t"
    "buffer_load_dwordx4 v4, s[20:23], s85 offen lds\n\t"
    "s_mov_b64 exec, 0\n\t"
    "s_cmp_eq_u32 s89, 0\n\t"
    "s_cbranch_scc1 LIPA0x0_%=\n\t"
    "s_cmp_eq_u32 s89, 1\n\t"
    "s_cbranch_scc1 LIPA1x0_%=\n\t"
    "s_cmp_eq_u32 s89, 2\n\t"
    "s_cbranch_scc1 LIPA2x0_%=\n\t"
    "s_cmp_eq_u32 s89, 3\n\t"
    "s_cbranch_scc1 LIPA3x0_%=\n\t"
    "s_cmp_eq_u32 s89, 4\n\t"
    "s_cbranch_scc1 LIPA4x0_%=\n\t"
    "s_cmp_eq_u32 s89, 5\n\t"
    "s_cbranch_scc1 LIPRA_%=\n\t"
    "s_cmp_eq_u32 s89, 6\n\t"
    "s_cbranch_scc1 LIPRA_%=\n\t"
    "s_cmp_eq_u32 s89, 7\n\t"
    "s_cbranch_scc1 LIPRA_%=\n\t"
    "LIPRA_%=:\n\t"
    "s_mov_b64 exec, -1\n\t"
    "s_waitcnt vmcnt(6)\n\t"
    "s_barrier\n\t"
    "v_add_u32 v220, 0x18000, v220\n\t"
    "ds_read_b128 v[40:43], v220 offset:49152\n\t"
    "ds_read_b128 v[44:47], v220 offset:51200\n\t"
    "v_add_u32 v221, 0x18000, v221\n\t"
    "ds_read_b128 v[48:51], v221 offset:49152\n\t"
    "ds_read_b128 v[52:55], v221 offset:51200\n\t"
    "ds_read_b64_tr_b16 v[24:25], v0 offset:0\n\t"
    "ds_read_b64_tr_b16 v[26:27], v0 offset:512\n\t"
    "ds_read_b64_tr_b16 v[28:29], v1 offset:0\n\t"
    "ds_read_b64_tr_b16 v[30:31], v1 offset:512\n\t"
    "ds_read_b64_tr_b16 v[32:33], v0 offset:4096\n\t"
    "ds_read_b64_tr_b16 v[34:35], v0 offset:4608\n\t"
    "ds_read_b64_tr_b16 v[36:37], v1 offset:4096\n\t"
    "ds_read_b64_tr_b16 v[38:39], v1 offset:4608\n\t"
    "s_waitcnt vmcnt(5)\n\t"
    "s_barrier\n\t"
    "s_setprio 1\n\t"
    "s_waitcnt lgkmcnt(6)\n\t"
    "v_mfma_f32_16x16x32_bf16 v[224:227], v[24:27], v[40:43], 0\n\t"
    "v_mfma_f32_16x16x32_bf16 v[232:235], v[24:27], v[44:47], 0\n\t"
    "ds_read_b64_tr_b16 v[24:25], v0 offset:8192\n\t"
    "s_waitcnt lgkmcnt(5)\n\t"
    "v_mfma_f32_16x16x32_bf16 v[228:231], v[28:31], v[40:43], 0\n\t"
    "ds_read_b64_tr_b16 v[26:27], v0 offset:8704\n\t"
    "s_add_u32 m0, s36, 32768\n\t"
    "s_add_u32 s85, s9, 73728\n\t"
    "buffer_load_dwordx4 v216, s[16:19], s9 offen lds\n\t"
    "s_add_u32 m0, s36, 40960\n\t"
    "s_add_u32 s9, s9, 128\n\t"
    "buffer_load_dwordx4 v216, s[16:19], s85 offen lds\n\t"
    "s_cmp_eq_u32 s9, s10\n\t"
    "s_cselect_b32 s9, s37, s9\n\t"
    "v_mfma_f32_16x16x32_bf16 v[236:239], v[28:31], v[44:47], 0\n\t"
    "ds_read_b64_tr_b16 v[28:29], v1 offset:8192\n\t"
    "s_setprio 0\n\t"
    "s_waitcnt lgkmcnt(5)\n\t"
    "v_mfma_f32_16x16x32_bf16 v[224:227], v[32:35], v[48:51], v[224:227]\n\t"
    "ds_read_b64_tr_b16 v[30:31], v1 offset:8704\n\t"
    "v_mfma_f32_16x16x32_bf16 v[232:235], v[32:35], v[52:55], v[232:235]\n\t"
    "ds_read_b64_tr_b16 v[32:33], v0 offset:12288\n\t"
    "ds_read_b64_tr_b16 v[34:35], v0 offset:12800\n\t"
    "s_waitcnt lgkmcnt(6)\n\t"
    "v_mfma_f32_16x16x32_bf16 v[228:231], v[36:39], v[48:51], v[228:231]\n\t"
    "s_add_u32 m0, s36, 49152\n\t"
    "s_add_u32 s85, s2, 0x0\n\t"
    "buffer_load_dwordx4 v4, s[20:23], s85 offen lds\n\t"
    "v_mfma_f32_16x16x32_bf16 v[236:239], v[36:39], v[52:55], v[236:239]\n\t"
    "ds_read_b64_tr_b16 v[36:37], v1 offset:12288\n\t"
    "s_waitcnt vmcnt(4)\n\t"
    "s_barrier\n\t"
    "s_setprio 1\n\t"
    "s_waitcnt lgkmcnt(5)\n\t"
    "v_mfma_f32_16x16x32_bf16 v[240:243], v[24:27], v[40:43], 0\n\t"
    "ds_read_b64_tr_b16 v[38:39], v1 offset:12800\n\t"
    "v_mfma_f32_16x16x32_bf16 v[248:251], v[24:27], v[44:47], 0\n\t"
    "ds_read_b128 v[56:59], v220 offset:32768\n\t"
    "ds_read_b64_tr_b16 v[24:25], v0 offset:16384\n\t"
    "s_waitcnt lgkmcnt(6)\n\t"
    "v_mfma_f32_16x16x32_bf16 v[244:247], v[28:31], v[40:43], 0\n\t"
    "s_add_u32 m0, s36, 57344\n\t"
    "s_add_u32 s85, s2, 0x80\n\t"
    "buffer_load_dwordx4 v4, s[20:23], s85 offen lds\n\t"
    "ds_read_b64_tr_b16 v[26:27], v0 offset:16896\n\t"
    "v_mfma_f32_16x16x32_bf16 v[252:255], v[28:31], v[44:47], 0\n\t"
    "ds_read_b128 v[60:63], v220 offset:34816\n\t"
    "ds_read_b64_tr_b16 v[28:29], v1 offset:16384\n\t"
    "s_setprio 0\n\t"
    "s_waitcnt lgkmcnt(7)\n\t"
    "v_mfma_f32_16x16x32_bf16 v[240:243], v[32:35], v[48:51], v[240:243]\n\t"
    "s_add_u32 m0, s36, 65536\n\t"
    "s_add_u32 s85, s9, 73728\n\t"
    "buffer_load_dwordx4 v216, s[16:19], s9 offen lds\n\t"
    "s_add_u32 m0, s36, 73728\n\t"
    "s_add_u32 s9, s9, 128\n\t"
    "buffer_load_dwordx4 v216, s[16:19], s85 offen lds\n\t"
    "s_cmp_eq_u32 s9, s10\n\t"
    "s_cselect_b32 s9, s37, s9\n\t"
    "ds_read_b64_tr_b16 v[30:31], v1 offset:16896\n\t"
    "v_mfma_f32_16x16x32_bf16 v[248:251], v[32:35], v[52:55], v[248:251]\n\t"
    "ds_read_b128 v[64:67], v221 offset:32768\n\t"
    "ds_read_b64_tr_b16 v[32:33], v0 offset:20480\n\t"
    "s_waitcnt lgkmcnt(8)\n\t"
    "v_mfma_f32_16x16x32_bf16 v[244:247], v[36:39], v[48:51], v[244:247]\n\t"
    "ds_read_b64_tr_b16 v[34:35], v0 offset:20992\n\t"
    "v_mfma_f32_16x16x32_bf16 v[252:255], v[36:39], v[52:55], v[252:255]\n\t"
    "ds_read_b128 v[68:71], v221 offset:34816\n\t"
    "ds_read_b64_tr_b16 v[36:37], v1 offset:20480\n\t"
    "s_waitcnt vmcnt(6)\n\t"
    "s_barrier\n\t"
    "s_setprio 1\n\t"
    "s_waitcnt lgkmcnt(8)\n\t"
    "v_mfma_f32_16x16x32_bf16 v[224:227], v[24:27], v[56:59], v[224:227]\n\t"
    "ds_read_b64_tr_b16 v[38:39], v1 offset:20992\n\t"
    "s_waitcnt lgkmcnt(8)\n\t"
    "v_mfma_f32_16x16x32_bf16 v[232:235], v[24:27], v[60:63], v[232:235]\n\t"
    "ds_read_b64_tr_b16 v[24:25], v0 offset:24576\n\t"
    "s_waitcnt lgkmcnt(7)\n\t"
    "v_mfma_f32_16x16x32_bf16 v[228:231], v[28:31], v[56:59], v[228:231]\n\t"
    "ds_read_b64_tr_b16 v[26:27], v0 offset:25088\n\t"
    "s_add_u32 m0, s36, 81920\n\t"
    "s_add_u32 s85, s3, 0x0\n\t"
    "buffer_load_dwordx4 v4, s[20:23], s85 offen lds\n\t"
    "v_mfma_f32_16x16x32_bf16 v[236:239], v[28:31], v[60:63], v[236:239]\n\t"
    "ds_read_b64_tr_b16 v[28:29], v1 offset:24576\n\t"
    "s_setprio 0\n\t"
    "s_waitcnt lgkmcnt(6)\n\t"
    "v_mfma_f32_16x16x32_bf16 v[224:227], v[32:35], v[64:67], v[224:227]\n\t"
    "ds_read_b64_tr_b16 v[30:31], v1 offset:25088\n\t"
    "s_add_u32 m0, s36, 90112\n\t"
    "s_add_u32 s85, s3, 0x80\n\t"
    "buffer_load_dwordx4 v4, s[20:23], s85 offen lds\n\t"
    "s_waitcnt lgkmcnt(6)\n\t"
    "v_mfma_f32_16x16x32_bf16 v[232:235], v[32:35], v[68:71], v[232:235]\n\t"
    "ds_read_b64_tr_b16 v[32:33], v0 offset:28672\n\t"
    "ds_read_b64_tr_b16 v[34:35], v0 offset:29184\n\t"
    "s_waitcnt lgkmcnt(6)\n\t"
    "v_mfma_f32_16x16x32_bf16 v[228:231], v[36:39], v[64:67], v[228:231]\n\t"
    "v_mfma_f32_16x16x32_bf16 v[236:239], v[36:39], v[68:71], v[236:239]\n\t"
    "ds_read_b64_tr_b16 v[36:37], v1 offset:28672\n\t"
    "s_waitcnt vmcnt(5)\n\t"
    "s_barrier\n\t"
    "s_setprio 1\n\t"
    "s_waitcnt lgkmcnt(5)\n\t"
    "v_mfma_f32_16x16x32_bf16 v[240:243], v[24:27], v[56:59], v[240:243]\n\t"
    "ds_read_b64_tr_b16 v[38:39], v1 offset:29184\n\t"
    "v_mfma_f32_16x16x32_bf16 v[248:251], v[24:27], v[60:63], v[248:251]\n\t"
    "ds_read_b64_tr_b16 v[24:25], v0 offset:49152\n\t"
    "v_add_u32 v220, 0xffff0000, v220\n\t"
    "ds_read_b128 v[72:75], v220 offset:0\n\t"
    "s_waitcnt lgkmcnt(6)\n\t"
    "v_mfma_f32_16x16x32_bf16 v[244:247], v[28:31], v[56:59], v[244:247]\n\t"
    "ds_read_b64_tr_b16 v[26:27], v0 offset:49664\n\t"
    "s_add_u32 m0, s36, 0\n\t"
    "s_add_u32 s85, s9, 73728\n\t"
    "buffer_load_dwordx4 v216, s[16:19], s9 offen lds\n\t"
    "s_add_u32 m0, s36, 8192\n\t"
    "s_add_u32 s9, s9, 128\n\t"
    "buffer_load_dwordx4 v216, s[16:19], s85 offen lds\n\t"
    "s_cmp_eq_u32 s9, s10\n\t"
    "s_cselect_b32 s9, s37, s9\n\t"
    "v_mfma_f32_16x16x32_bf16 v[252:255], v[28:31], v[60:63], v[252:255]\n\t"
    "ds_read_b64_tr_b16 v[28:29], v1 offset:49152\n\t"
    "ds_read_b128 v[76:79], v220 offset:2048\n\t"
    "s_setprio 0\n\t"
    "s_waitcnt lgkmcnt(7)\n\t"
    "v_mfma_f32_16x16x32_bf16 v[240:243], v[32:35], v[64:67], v[240:243]\n\t"
    "ds_read_b64_tr_b16 v[30:31], v1 offset:49664\n\t"
    "v_mfma_f32_16x16x32_bf16 v[248:251], v[32:35], v[68:71], v[248:251]\n\t"
    "ds_read_b64_tr_b16 v[32:33], v0 offset:53248\n\t"
    "v_add_u32 v221, 0xffff0000, v221\n\t"
    "ds_read_b128 v[80:83], v221 offset:0\n\t"
    "s_waitcnt lgkmcnt(8)\n\t"
    "v_mfma_f32_16x16x32_bf16 v[244:247], v[36:39], v[64:67], v[244:247]\n\t"
    "ds_read_b64_tr_b16 v[34:35], v0 offset:53760\n\t"
    "s_add_u32 m0, s36, 98304\n\t"
    "s_add_u32 s85, s4, 0x0\n\t"
    "buffer_load_dwordx4 v4, s[20:23], s85 offen lds\n\t"
    "v_mfma_f32_16x16x32_bf16 v[252:255], v[36:39], v[68:71], v[252:255]\n\t"
    "ds_read_b64_tr_b16 v[36:37], v1 offset:53248\n\t"
    "ds_read_b128 v[84:87], v221 offset:2048\n\t"
    "s_waitcnt vmcnt(7)\n\t"
    "s_barrier\n\t"
    "s_setprio 1\n\t"
    "s_waitcnt lgkmcnt(8)\n\t"
    "v_mfma_f32_16x16x32_bf16 v[224:227], v[24:27], v[72:75], v[224:227]\n\t"
    "ds_read_b64_tr_b16 v[38:39], v1 offset:53760\n\t"
    "s_waitcnt lgkmcnt(7)\n\t"
    "v_mfma_f32_16x16x32_bf16 v[232:235], v[24:27], v[76:79], v[232:235]\n\t"
    "ds_read_b64_tr_b16 v[24:25], v0 offset:57344\n\t"
    "s_waitcnt lgkmcnt(7)\n\t"
    "v_mfma_f32_16x16x32_bf16 v[228:231], v[28:31], v[72:75], v[228:231]\n\t"
    "ds_read_b64_tr_b16 v[26:27], v0 offset:57856\n\t"
    "s_add_u32 m0, s36, 106496\n\t"
    "s_add_u32 s85, s4, 0x80\n\t"
    "buffer_load_dwordx4 v4, s[20:23], s85 offen lds\n\t"
    "v_mfma_f32_16x16x32_bf16 v[236:239], v[28:31], v[76:79], v[236:239]\n\t"
    "ds_read_b64_tr_b16 v[28:29], v1 offset:57344\n\t"
    "s_setprio 0\n\t"
    "s_waitcnt lgkmcnt(6)\n\t"
    "v_mfma_f32_16x16x32_bf16 v[224:227], v[32:35], v[80:83], v[224:227]\n\t"
    "ds_read_b64_tr_b16 v[30:31], v1 offset:57856\n\t"
    "s_add_u32 m0, s36, 16384\n\t"
    "s_add_u32 s85, s9, 73728\n\t"
    "buffer_load_dwordx4 v216, s[16:19], s9 offen lds\n\t"
    "s_add_u32 m0, s36, 24576\n\t"
    "s_add_u32 s9, s9, 128\n\t"
    "buffer_load_dwordx4 v216, s[16:19], s85 offen lds\n\t"
    "s_cmp_eq_u32 s9, s10\n\t"
    "s_cselect_b32 s9, s37, s9\n\t"
    "s_waitcnt lgkmcnt(5)\n\t"
    "v_mfma_f32_16x16x32_bf16 v[232:235], v[32:35], v[84:87], v[232:235]\n\t"
    "ds_read_b64_tr_b16 v[32:33], v0 offset:61440\n\t"
    "s_waitcnt lgkmcnt(5)\n\t"
    "v_mfma_f32_16x16x32_bf16 v[228:231], v[36:39], v[80:83], v[228:231]\n\t"
    "ds_read_b64_tr_b16 v[34:35], v0 offset:61952\n\t"
    "v_mfma_f32_16x16x32_bf16 v[236:239], v[36:39], v[84:87], v[236:239]\n\t"
    "ds_read_b64_tr_b16 v[36:37], v1 offset:61440\n\t"
    "s_waitcnt vmcnt(7)\n\t"
    "s_barrier\n\t"
    "s_setprio 1\n\t"
    "s_waitcnt lgkmcnt(5)\n\t"
    "v_mfma_f32_16x16x32_bf16 v[240:243], v[24:27], v[72:75], v[240:243]\n\t"
    "ds_read_b64_tr_b16 v[38:39], v1 offset:61952\n\t"
    "v_mfma_f32_16x16x32_bf16 v[248:251], v[24:27], v[76:79], v[248:251]\n\t"
    "v_add_u32 v0, 0x14000, v0\n\t"
    "ds_read_b64_tr_b16 v[24:25], v0 offset:0\n\t"
    "ds_read_b128 v[88:91], v220 offset:32768\n\t"
    "s_waitcnt lgkmcnt(6)\n\t"
    "v_mfma_f32_16x16x32_bf16 v[244:247], v[28:31], v[72:75], v[244:247]\n\t"
    "ds_read_b64_tr_b16 v[26:27], v0 offset:512\n\t"
    "s_add_u32 m0, s36, 114688\n\t"
    "s_add_u32 s85, s5, 0x0\n\t"
    "buffer_load_dwordx4 v4, s[20:23], s85 offen lds\n\t"
    "v_mfma_f32_16x16x32_bf16 v[252:255], v[28:31], v[76:79], v[252:255]\n\t"
    "v_add_u32 v1, 0x14000, v1\n\t"
    "ds_read_b64_tr_b16 v[28:29], v1 offset:0\n\t"
    "ds_read_b128 v[92:95], v220 offset:34816\n\t"
    "s_setprio 0\n\t"
    "s_waitcnt lgkmcnt(7)\n\t"
    "v_mfma_f32_16x16x32_bf16 v[240:243], v[32:35], v[80:83], v[240:243]\n\t"
    "ds_read_b64_tr_b16 v[30:31], v1 offset:512\n\t"
    "s_add_u32 m0, s36, 122880\n\t"
    "s_add_u32 s85, s5, 0x80\n\t"
    "buffer_load_dwordx4 v4, s[20:23], s85 offen lds\n\t"
    "v_mfma_f32_16x16x32_bf16 v[248:251], v[32:35], v[84:87], v[248:251]\n\t"
    "ds_read_b64_tr_b16 v[32:33], v0 offset:4096\n\t"
    "ds_read_b64_tr_b16 v[34:35], v0 offset:4608\n\t"
    "ds_read_b128 v[96:99], v221 offset:32768\n\t"
    "s_waitcnt lgkmcnt(9)\n\t"
    "v_mfma_f32_16x16x32_bf16 v[244:247], v[36:39], v[80:83], v[244:247]\n\t"
    "v_mfma_f32_16x16x32_bf16 v[252:255], v[36:39], v[84:87], v[252:255]\n\t"
    "ds_read_b64_tr_b16 v[36:37], v1 offset:4096\n\t"
    "ds_read_b64_tr_b16 v[38:39], v1 offset:4608\n\t"
    "ds_read_b128 v[100:103], v221 offset:34816\n\t"
    "s_waitcnt vmcnt(8)\n\t"
    "s_barrier\n\t"
    "s_setprio 1\n\t"
    "s_waitcnt lgkmcnt(9)\n\t"
    "v_mfma_f32_16x16x32_bf16 v[224:227], v[24:27], v[88:91], v[224:227]\n\t"
    "s_waitcnt lgkmcnt(7)\n\t"
    "v_mfma_f32_16x16x32_bf16 v[232:235], v[24:27], v[92:95], v[232:235]\n\t"
    "ds_read_b64_tr_b16 v[24:25], v0 offset:8192\n\t"
    "s_waitcnt lgkmcnt(7)\n\t"
    "v_mfma_f32_16x16x32_bf16 v[228:231], v[28:31], v[88:91], v[228:231]\n\t"
    "ds_read_b64_tr_b16 v[26:27], v0 offset:8704\n\t"
    "s_add_u32 m0, s36, 32768\n\t"
    "s_add_u32 s85, s9, 73728\n\t"
    "buffer_load_dwordx4 v216, s[16:19], s9 offen lds\n\t"
    "s_add_u32 m0, s36, 40960\n\t"
    "s_add_u32 s9, s9, 128\n\t"
    "buffer_load_dwordx4 v216, s[16:19], s85 offen lds\n\t"
    "s_cmp_eq_u32 s9, s10\n\t"
    "s_cselect_b32 s9, s37, s9\n\t"
    "v_mfma_f32_16x16x32_bf16 v[236:239], v[28:31], v[92:95], v[236:239]\n\t"
    "ds_read_b64_tr_b16 v[28:29], v1 offset:8192\n\t"
    "s_setprio 0\n\t"
    "s_waitcnt lgkmcnt(6)\n\t"
    "v_mfma_f32_16x16x32_bf16 v[224:227], v[32:35], v[96:99], v[224:227]\n\t"
    "ds_read_b64_tr_b16 v[30:31], v1 offset:8704\n\t"
    "s_waitcnt lgkmcnt(4)\n\t"
    "v_mfma_f32_16x16x32_bf16 v[232:235], v[32:35], v[100:103], v[232:235]\n\t"
    "ds_read_b64_tr_b16 v[32:33], v0 offset:12288\n\t"
    "v_mfma_f32_16x16x32_bf16 v[228:231], v[36:39], v[96:99], v[228:231]\n\t"
    "ds_read_b64_tr_b16 v[34:35], v0 offset:12800\n\t"
    "s_add_u32 m0, s36, 131072\n\t"
    "s_add_u32 s85, s6, 0x0\n\t"
    "buffer_load_dwordx4 v4, s[20:23], s85 offen lds\n\t"
    "v_mfma_f32_16x16x32_bf16 v[236:239], v[36:39], v[100:103], v[236:239]\n\t"
    "ds_read_b64_tr_b16 v[36:37], v1 offset:12288\n\t"
    "buffer_load_dword v17, v19, s[20:23], s61 offen offset:256\n\t"
    "s_waitcnt vmcnt(9)\n\t"
    "s_barrier\n\t"
    "s_setprio 1\n\t"
    "s_waitcnt lgkmcnt(5)\n\t"
    "v_mfma_f32_16x16x32_bf16 v[240:243], v[24:27], v[88:91], v[240:243]\n\t"
    "ds_read_b64_tr_b16 v[38:39], v1 offset:12800\n\t"
    "v_mfma_f32_16x16x32_bf16 v[248:251], v[24:27], v[92:95], v[248:251]\n\t"
    "ds_read_b64_tr_b16 v[24:25], v0 offset:16384\n\t"
    "v_add_u32 v220, 0xffff8000, v220\n\t"
    "ds_read_b128 v[104:107], v220 offset:0\n\t"
    "s_waitcnt lgkmcnt(6)\n\t"
    "v_mfma_f32_16x16x32_bf16 v[244:247], v[28:31], v[88:91], v[244:247]\n\t"
    "ds_read_b64_tr_b16 v[26:27], v0 offset:16896\n\t"
    "s_add_u32 m0, s36, 139264\n\t"
    "s_add_u32 s85, s6, 0x80\n\t"
    "buffer_load_dwordx4 v4, s[20:23], s85 offen lds\n\t"
    "v_mfma_f32_16x16x32_bf16 v[252:255], v[28:31], v[92:95], v[252:255]\n\t"
    "ds_read_b64_tr_b16 v[28:29], v1 offset:16384\n\t"
    "ds_read_b128 v[108:111], v220 offset:2048\n\t"
    "s_setprio 0\n\t"
    "s_waitcnt lgkmcnt(7)\n\t"
    "v_mfma_f32_16x16x32_bf16 v[240:243], v[32:35], v[96:99], v[240:243]\n\t"
    "ds_read_b64_tr_b16 v[30:31], v1 offset:16896\n\t"
    "s_add_u32 m0, s36, 49152\n\t"
    "s_add_u32 s85, s9, 73728\n\t"
    "buffer_load_dwordx4 v216, s[16:19], s9 offen lds\n\t"
    "s_add_u32 m0, s36, 57344\n\t"
    "s_add_u32 s9, s9, 128\n\t"
    "buffer_load_dwordx4 v216, s[16:19], s85 offen lds\n\t"
    "s_cmp_eq_u32 s9, s10\n\t"
    "s_cselect_b32 s9, s37, s9\n\t"
    "v_mfma_f32_16x16x32_bf16 v[248:251], v[32:35], v[100:103], v[248:251]\n\t"
    "ds_read_b64_tr_b16 v[32:33], v0 offset:20480\n\t"
    "v_add_u32 v221, 0xffff8000, v221\n\t"
    "ds_read_b128 v[112:115], v221 offset:0\n\t"
    "s_waitcnt lgkmcnt(8)\n\t"
    "v_mfma_f32_16x16x32_bf16 v[244:247], v[36:39], v[96:99], v[244:247]\n\t"
    "ds_read_b64_tr_b16 v[34:35], v0 offset:20992\n\t"
    "v_mfma_f32_16x16x32_bf16 v[252:255], v[36:39], v[100:103], v[252:255]\n\t"
    "ds_read_b64_tr_b16 v[36:37], v1 offset:20480\n\t"
    "ds_read_b128 v[116:119], v221 offset:2048\n\t"
    "s_waitcnt vmcnt(11)\n\t"
    "s_barrier\n\t"
    "s_setprio 1\n\t"
    "s_waitcnt lgkmcnt(8)\n\t"
    "v_mfma_f32_16x16x32_bf16 v[224:227], v[24:27], v[104:107], v[224:227]\n\t"
    "ds_read_b64_tr_b16 v[38:39], v1 offset:20992\n\t"
    "s_waitcnt lgkmcnt(7)\n\t"
    "v_mfma_f32_16x16x32_bf16 v[232:235], v[24:27], v[108:111], v[232:235]\n\t"
    "ds_read_b64_tr_b16 v[24:25], v0 offset:24576\n\t"
    "ds_read_b64_tr_b16 v[26:27], v0 offset:25088\n\t"
    "s_waitcnt lgkmcnt(8)\n\t"
    "LIPA0x0_%=:\n\t"
    "s_cbranch_execz LIPRA_%=\n\t"
    "v_mfma_f32_16x16x32_bf16 v[228:231], v[28:31], v[104:107], v[228:231]\n\t"
    "s_add_u32 m0, s36, 147456\n\t"
    "s_add_u32 s85, s7, 0x0\n\t"
    "buffer_load_dwordx4 v4, s[20:23], s85 offen lds\n\t"
    "v_mfma_f32_16x16x32_bf16 v[236:239], v[28:31], v[108:111], v[236:239]\n\t"
    "ds_read_b64_tr_b16 v[28:29], v1 offset:24576\n\t"
    "s_setprio 0\n\t"
    "s_waitcnt lgkmcnt(6)\n\t"
    "v_mfma_f32_16x16x32_bf16 v[224:227], v[32:35], v[112:115], v[224:227]\n\t"
    "ds_read_b64_tr_b16 v[30:31], v1 offset:25088\n\t"
    "s_add_u32 m0, s36, 155648\n\t"
    "s_add_u32 s85, s7, 0x80\n\t"
    "buffer_load_dwordx4 v4, s[20:23], s85 offen lds\n\t"
    "s_waitcnt lgkmcnt(5)\n\t"
    "v_mfma_f32_16x16x32_bf16 v[232:235], v[32:35], v[116:119], v[232:235]\n\t"
    "ds_read_b64_tr_b16 v[32:33], v0 offset:28672\n\t"
    "s_waitcnt lgkmcnt(5)\n\t"
    "v_mfma_f32_16x16x32_bf16 v[228:231], v[36:39], v[112:115], v[228:231]\n\t"
    "ds_read_b64_tr_b16 v[34:35], v0 offset:29184\n\t"
    "v_mfma_f32_16x16x32_bf16 v[236:239], v[36:39], v[116:119], v[236:239]\n\t"
    "ds_read_b64_tr_b16 v[36:37], v1 offset:28672\n\t"
    "s_waitcnt vmcnt(10)\n\t"
    "s_barrier\n\t"
    "s_setprio 1\n\t"
    "s_waitcnt lgkmcnt(5)\n\t"
    "v_mfma_f32_16x16x32_bf16 v[240:243], v[24:27], v[104:107], v[240:243]\n\t"
    "ds_read_b64_tr_b16 v[38:39], v1 offset:29184\n\t"
    "v_mfma_f32_16x16x32_bf16 v[248:251], v[24:27], v[108:111], v[248:251]\n\t"
    "ds_read_b64_tr_b16 v[24:25], v0 offset:32768\n\t"
    "ds_read_b128 v[120:123], v220 offset:16384\n\t"
    "s_waitcnt lgkmcnt(6)\n\t"
    "v_mfma_f32_16x16x32_bf16 v[244:247], v[28:31], v[104:107], v[244:247]\n\t"
    "ds_read_b64_tr_b16 v[26:27], v0 offset:33280\n\t"
    "s_add_u32 m0, s36, 65536\n\t"
    "s_add_u32 s85, s9, 73728\n\t"
    "buffer_load_dwordx4 v216, s[16:19], s9 offen lds\n\t"
    "s_add_u32 m0, s36, 73728\n\t"
    "s_add_u32 s9, s9, 128\n\t"
    "buffer_load_dwordx4 v216, s[16:19], s85 offen lds\n\t"
    "s_cmp_eq_u32 s9, s10\n\t"
    "s_cselect_b32 s9, s37, s9\n\t"
    "v_mfma_f32_16x16x32_bf16 v[252:255], v[28:31], v[108:111], v[252:255]\n\t"
    "ds_read_b64_tr_b16 v[28:29], v1 offset:32768\n\t"
    "ds_read_b128 v[124:127], v220 offset:18432\n\t"
    "v_readlane_b32 s48, v8, s96\n\t"
    "s_nop 0\n\t"
    "s_mul_i32 s48, s48, 0x9e3779b1\n\t"
    "s_add_u32 s48, s48, 0x7f4a7c15\n\t"
    "s_setprio 0\n\t"
    "s_waitcnt lgkmcnt(7)\n\t"
    "v_mfma_f32_16x16x32_bf16 v[240:243], v[32:35], v[112:115], v[240:243]\n\t"
    "ds_read_b64_tr_b16 v[30:31], v1 offset:33280\n\t"
    "v_mfma_f32_16x16x32_bf16 v[248:251], v[32:35], v[116:119], v[248:251]\n\t"
    "ds_read_b64_tr_b16 v[32:33], v0 offset:36864\n\t"
    "ds_read_b64_tr_b16 v[34:35], v0 offset:37376\n\t"
    "ds_read_b128 v[128:131], v221 offset:16384\n\t"
    "s_waitcnt lgkmcnt(9)\n\t"
    "v_mfma_f32_16x16x32_bf16 v[244:247], v[36:39], v[112:115], v[244:247]\n\t"
    "s_add_u32 m0, s36, 0\n\t"
    "s_add_u32 s85, s8, 0x0\n\t"
    "buffer_load_dwordx4 v4, s[20:23], s85 offen lds\n\t"
    "v_mfma_f32_16x16x32_bf16 v[252:255], v[36:39], v[116:119], v[252:255]\n\t"
    "ds_read_b64_tr_b16 v[36:37], v1 offset:36864\n\t"
    "ds_read_b128 v[132:135], v221 offset:18432\n\t"
    "v_readlane_b32 s49, v8, s97\n\t"
    "s_nop 0\n\t"
    "s_mul_i32 s49, s49, 0x9e3779b1\n\t"
    "s_add_u32 s49, s49, 0x7f4a7c15\n\t"
    "s_waitcnt vmcnt(12)\n\t"
    "s_barrier\n\t"
    "s_setprio 1\n\t"
    "s_waitcnt lgkmcnt(8)\n\t"
    "v_mfma_f32_16x16x32_bf16 v[224:227], v[24:27], v[120:123], v[224:227]\n\t"
    "ds_read_b64_tr_b16 v[38:39], v1 offset:37376\n\t"
    "s_waitcnt lgkmcnt(7)\n\t"
    "v_mfma_f32_16x16x32_bf16 v[232:235], v[24:27], v[124:127], v[232:235]\n\t"
    "ds_read_b64_tr_b16 v[24:25], v0 offset:40960\n\t"
    "s_waitcnt lgkmcnt(7)\n\t"
    "v_mfma_f32_16x16x32_bf16 v[228:231], v[28:31], v[120:123], v[228:231]\n\t"
    "ds_read_b64_tr_b16 v[26:27], v0 offset:41472\n\t"
    "s_add_u32 m0, s36, 8192\n\t"
    "s_add_u32 s85, s8, 0x80\n\t"
    "buffer_load_dwordx4 v4, s[20:23], s85 offen lds\n\t"
    "v_mfma_f32_16x16x32_bf16 v[236:239], v[28:31], v[124:127], v[236:239]\n\t"
    "ds_read_b64_tr_b16 v[28:29], v1 offset:40960\n\t"
    "v_readlane_b32 s50, v8, s98\n\t"
    "s_nop 0\n\t"
    "s_mul_i32 s50, s50, 0x9e3779b1\n\t"
    "s_add_u32 s50, s50, 0x7f4a7c15\n\t"
    "s_setprio 0\n\t"
    "s_waitcnt lgkmcnt(6)\n\t"
    "v_mfma_f32_16x16x32_bf16 v[224:227], v[32:35], v[128:131], v[224:227]\n\t"
    "ds_read_b64_tr_b16 v[30:31], v1 offset:41472\n\t"
    "s_add_u32 m0, s36, 81920\n\t"
    "s_add_u32 s85, s0, 0x100\n\t"
    "buffer_load_dwordx4 v4, s[20:23], s85 offen lds\n\t"
    "s_waitcnt lgkmcnt(5)\n\t"
    "v_mfma_f32_16x16x32_bf16 v[232:235], v[32:35], v[132:135], v[232:235]\n\t"
    "ds_read_b64_tr_b16 v[32:33], v0 offset:45056\n\t"
    "s_waitcnt lgkmcnt(5)\n\t"
    "v_mfma_f32_16x16x32_bf16 v[228:231], v[36:39], v[128:131], v[228:231]\n\t"
    "ds_read_b64_tr_b16 v[34:35], v0 offset:45568\n\t"
    "s_add_u32 m0, s36, 90112\n\t"
    "s_add_u32 s85, s1, 0x100\n\t"
    "buffer_load_dwordx4 v4, s[20:23], s85 offen lds\n\t"
    "v_mfma_f32_16x16x32_bf16 v[236:239], v[36:39], v[132:135], v[236:239]\n\t"
    "ds_read_b64_tr_b16 v[36:37], v1 offset:45056\n\t"
    "v_readlane_b32 s51, v8, s99\n\t"
    "s_nop 0\n\t"
    "s_mul_i32 s51, s51, 0x9e3779b1\n\t"
    "s_add_u32 s51, s51, 0x7f4a7c15\n\t"
    "s_waitcnt vmcnt(12)\n\t"
    "s_barrier\n\t"
    "s_setprio 1\n\t"
    "s_waitcnt lgkmcnt(5)\n\t"
    "v_mfma_f32_16x16x32_bf16 v[240:243], v[24:27], v[120:123], v[240:243]\n\t"
    "ds_read_b64_tr_b16 v[38:39], v1 offset:45568\n\t"
    "v_mfma_f32_16x16x32_bf16 v[248:251], v[24:27], v[124:127], v[248:251]\n\t"
    "ds_read_b64_tr_b16 v[24:25], v0 offset:49152\n\t"
    "ds_read_b128 v[136:139], v220 offset:32768\n\t"
    "s_waitcnt lgkmcnt(6)\n\t"
    "v_mfma_f32_16x16x32_bf16 v[244:247], v[28:31], v[120:123], v[244:247]\n\t"
    "s_add_u32 m0, s36, 98304\n\t"
    "s_add_u32 s85, s2, 0x100\n\t"
    "buffer_load_dwordx4 v4, s[20:23], s85 offen lds\n\t"
    "ds_read_b64_tr_b16 v[26:27], v0 offset:49664\n\t"
    "v_mfma_f32_16x16x32_bf16 v[252:255], v[28:31], v[124:127], v[252:255]\n\t"
    "ds_read_b128 v[140:143], v220 offset:34816\n\t"
    "v_readlane_b32 s56, v8, s90\n\t"
    "s_nop 0\n\t"
    "s_mul_i32 s55, s56, 0x9e3779b1\n\t"
    "s_add_u32 s55, s55, 0x7f4a7c15\n\t"
    "s_and_b32 s74, s56, 0xff\n\t"
    "s_mul_i32 s74, s74, 0x01010101\n\t"
    "s_xor_b32 s74, s74, 0xd4935211\n\t"
    "s_bfe_u32 s57, s74, 0x80000\n\t"
    "s_bfe_u32 s58, s74, 0x80008\n\t"
    "s_bfe_u32 s59, s74, 0x80010\n\t"
    "s_bfe_u32 s60, s74, 0x80018\n\t"
    "ds_read_b64_tr_b16 v[28:29], v1 offset:49152\n\t"
    "s_setprio 0\n\t"
    "s_waitcnt lgkmcnt(7)\n\t"
    "v_mfma_f32_16x16x32_bf16 v[240:243], v[32:35], v[128:131], v[240:243]\n\t"
    "ds_read_b64_tr_b16 v[30:31], v1 offset:49664\n\t"
    "s_add_u32 m0, s36, 106496\n\t"
    "s_add_u32 s85, s3, 0x100\n\t"
    "buffer_load_dwordx4 v4, s[20:23], s85 offen lds\n\t"
    "v_mfma_f32_16x16x32_bf16 v[248:251], v[32:35], v[132:135], v[248:251]\n\t"
    "ds_read_b128 v[144:147], v221 offset:32768\n\t"
    "ds_read_b64_tr_b16 v[32:33], v0 offset:53248\n\t"
    "s_waitcnt lgkmcnt(8)\n\t"
    "v_mfma_f32_16x16x32_bf16 v[244:247], v[36:39], v[128:131], v[244:247]\n\t"
    "ds_read_b64_tr_b16 v[34:35], v0 offset:53760\n\t"
    "s_add_u32 m0, s36, 114688\n\t"
    "s_add_u32 s85, s4, 0x100\n\t"
    "buffer_load_dwordx4 v4, s[20:23], s85 offen lds\n\t"
    "v_mfma_f32_16x16x32_bf16 v[252:255], v[36:39], v[132:135], v[252:255]\n\t"
    "ds_read_b64_tr_b16 v[36:37], v1 offset:53248\n\t"
    "ds_read_b128 v[148:151], v221 offset:34816\n\t"
    "s_xor_b32 s74, s91, 5\n\t"
    "v_readlane_b32 s52, v8, s74\n\t"
    "s_nop 0\n\t"
    "s_and_b32 s52, s52, 0xff\n\t"
    "s_mul_i32 s52, s52, 0x01010101\n\t"
    "s_xor_b32 s52, s52, 0xd4935211\n\t"
    "s_waitcnt vmcnt(13)\n\t"
    "s_barrier\n\t"
    "s_setprio 1\n\t"
    "s_waitcnt lgkmcnt(8)\n\t"
    "v_mfma_f32_16x16x32_bf16 v[224:227], v[24:27], v[136:139], v[224:227]\n\t"
    "ds_read_b64_tr_b16 v[38:39], v1 offset:53760\n\t"
    "s_waitcnt lgkmcnt(8)\n\t"
    "v_mfma_f32_16x16x32_bf16 v[232:235], v[24:27], v[140:143], v[232:235]\n\t"
    "ds_read_b64_tr_b16 v[24:25], v0 offset:57344\n\t"
    "ds_read_b64_tr_b16 v[26:27], v0 offset:57856\n\t"
    "s_waitcnt lgkmcnt(8)\n\t"
    "v_mfma_f32_16x16x32_bf16 v[228:231], v[28:31], v[136:139], v[228:231]\n\t"
    "s_add_u32 m0, s36, 16384\n\t"
    "s_add_u32 s85, s5, 0x100\n\t"
    "buffer_load_dwordx4 v4, s[20:23], s85 offen lds\n\t"
    "v_mfma_f32_16x16x32_bf16 v[236:239], v[28:31], v[140:143], v[236:239]\n\t"
    "ds_read_b64_tr_b16 v[28:29], v1 offset:57344\n\t"
    "ds_read_b64_tr_b16 v[30:31], v1 offset:57856\n\t"
    "s_xor_b32 s74, s91, 6\n\t"
    "v_readlane_b32 s53, v8, s74\n\t"
    "s_nop 0\n\t"
    "s_and_b32 s53, s53, 0xff\n\t"
    "s_mul_i32 s53, s53, 0x01010101\n\t"
    "s_xor_b32 s53, s53, 0xd4935211\n\t"
    "s_setprio 0\n\t"
    "s_waitcnt lgkmcnt(7)\n\t"
    "v_mfma_f32_16x16x32_bf16 v[224:227], v[32:35], v[144:147], v[224:227]\n\t"
    "s_add_u32 m0, s36, 24576\n\t"
    "s_add_u32 s85, s6, 0x100\n\t"
    "buffer_load_dwordx4 v4, s[20:23], s85 offen lds\n\t"
    "s_waitcnt lgkmcnt(5)\n\t"
    "v_mfma_f32_16x16x32_bf16 v[232:235], v[32:35], v[148:151], v[232:235]\n\t"
    "ds_read_b64_tr_b16 v[32:33], v0 offset:61440\n\t"
    "ds_read_b64_tr_b16 v[34:35], v0 offset:61952\n\t"
    "s_waitcnt lgkmcnt(6)\n\t"
    "v_mfma_f32_16x16x32_bf16 v[228:231], v[36:39], v[144:147], v[228:231]\n\t"
    "s_add_u32 m0, s36, 122880\n\t"
    "s_add_u32 s85, s7, 0x100\n\t"
    "buffer_load_dwordx4 v4, s[20:23], s85 offen lds\n\t"
    "v_mfma_f32_16x16x32_bf16 v[236:239], v[36:39], v[148:151], v[236:239]\n\t"
    "ds_read_b64_tr_b16 v[36:37], v1 offset:61440\n\t"
    "s_xor_b32 s74, s91, 7\n\t"
    "v_readlane_b32 s54, v8, s74\n\t"
    "s_nop 0\n\t"
    "s_and_b32 s54, s54, 0xff\n\t"
    "s_mul_i32 s54, s54, 0x01010101\n\t"
    "s_xor_b32 s54, s54, 0xd4935211\n\t"
    "s_waitcnt vmcnt(13)\n\t"
    "s_barrier\n\t"
    "s_setprio 1\n\t"
    "s_waitcnt lgkmcnt(5)\n\t"
    "v_mfma_f32_16x16x32_bf16 v[240:243], v[24:27], v[136:139], v[240:243]\n\t"
    "ds_read_b64_tr_b16 v[38:39], v1 offset:61952\n\t"
    "v_mfma_f32_16x16x32_bf16 v[248:251], v[24:27], v[140:143], v[248:251]\n\t"
    "v_add_u32 v0, 0x4000, v0\n\t"
    "ds_read_b64_tr_b16 v[24:25], v0 offset:49152\n\t"
    "ds_read_b64_tr_b16 v[26:27], v0 offset:49664\n\t"
    "ds_read_b128 v[152:155], v220 offset:49152\n\t"
    "s_waitcnt lgkmcnt(7)\n\t"
    "v_mfma_f32_16x16x32_bf16 v[244:247], v[28:31], v[136:139], v[244:247]\n\t"
    "s_add_u32 m0, s36, 131072\n\t"
    "s_add_u32 s85, s8, 0x100\n\t"
    "buffer_load_dwordx4 v4, s[20:23], s85 offen lds\n\t"
    "v_mfma_f32_16x16x32_bf16 v[252:255], v[28:31], v[140:143], v[252:255]\n\t"
    "v_add_u32 v1, 0x4000, v1\n\t"
    "ds_read_b64_tr_b16 v[28:29], v1 offset:49152\n\t"
    "ds_read_b64_tr_b16 v[30:31], v1 offset:49664\n\t"
    "ds_read_b128 v[156:159], v220 offset:51200\n\t"
    "s_setprio 0\n\t"
    "s_waitcnt lgkmcnt(8)\n\t"
    "v_mfma_f32_16x16x32_bf16 v[240:243], v[32:35], v[144:147], v[240:243]\n\t"
    "s_add_u32 m0, s36, 32768\n\t"
    "s_add_u32 s85, s0, 0x180\n\t"
    "buffer_load_dwordx4 v4, s[20:23], s85 offen lds\n\t"
    "v_mfma_f32_16x16x32_bf16 v[248:251], v[32:35], v[148:151], v[248:251]\n\t"
    "ds_read_b64_tr_b16 v[32:33], v0 offset:53248\n\t"
    "ds_read_b64_tr_b16 v[34:35], v0 offset:53760\n\t"
    "ds_read_b128 v[160:163], v221 offset:49152\n\t"
    "s_waitcnt lgkmcnt(9)\n\t"
    "v_mfma_f32_16x16x32_bf16 v[244:247], v[36:39], v[144:147], v[244:247]\n\t"
    "s_add_u32 m0, s36, 40960\n\t"
    "s_add_u32 s85, s1, 0x180\n\t"
    "buffer_load_dwordx4 v4, s[20:23], s85 offen lds\n\t"
    "v_mfma_f32_16x16x32_bf16 v[252:255], v[36:39], v[148:151], v[252:255]\n\t"
    "ds_read_b64_tr_b16 v[36:37], v1 offset:53248\n\t"
    "ds_read_b128 v[164:167], v221 offset:51200\n\t"
    "buffer_load_dword v17, v18, s[20:23], s61 offen offset:384\n\t"
    "s_mov_b64 exec, 0xfc7e3f\n\t"
    "buffer_load_dword v17, v16, s[24:27], s11 offen\n\t"
    "s_mov_b64 exec, -1\n\t"
    "s_waitcnt vmcnt(17)\n\t"
    "s_barrier\n\t"
    "s_setprio 1\n\t"
    "s_waitcnt lgkmcnt(8)\n\t"
    "v_mfma_f32_16x16x32_bf16 v[224:227], v[24:27], v[152:155], v[224:227]\n\t"
    "ds_read_b64_tr_b16 v[38:39], v1 offset:53760\n\t"
    "s_waitcnt lgkmcnt(6)\n\t"
    "v_mfma_f32_16x16x32_bf16 v[232:235], v[24:27], v[156:159], v[232:235]\n\t"
    "ds_read_b64_tr_b16 v[24:25], v0 offset:57344\n\t"
    "v_mfma_f32_16x16x32_bf16 v[228:231], v[28:31], v[152:155], v[228:231]\n\t"
    "ds_read_b64_tr_b16 v[26:27], v0 offset:57856\n\t"
    "v_mfma_f32_16x16x32_bf16 v[236:239], v[28:31], v[156:159], v[236:239]\n\t"
    "ds_read_b64_tr_b16 v[28:29], v1 offset:57344\n\t"
    "s_setprio 0\n\t"
    "s_waitcnt lgkmcnt(6)\n\t"
    "v_mfma_f32_16x16x32_bf16 v[224:227], v[32:35], v[160:163], v[224:227]\n\t"
    "ds_read_b64_tr_b16 v[30:31], v1 offset:57856\n\t"
    "s_waitcnt lgkmcnt(5)\n\t"
    "v_mfma_f32_16x16x32_bf16 v[232:235], v[32:35], v[164:167], v[232:235]\n\t"
    "ds_read_b64_tr_b16 v[32:33], v0 offset:61440\n\t"
    "s_waitcnt lgkmcnt(5)\n\t"
    "v_mfma_f32_16x16x32_bf16 v[228:231], v[36:39], v[160:163], v[228:231]\n\t"
    "ds_read_b64_tr_b16 v[34:35], v0 offset:61952\n\t"
    "v_mfma_f32_16x16x32_bf16 v[236:239], v[36:39], v[164:167], v[236:239]\n\t"
    "ds_read_b64_tr_b16 v[36:37], v1 offset:61440\n\t"
    "s_waitcnt vmcnt(14)\n\t"
    "s_barrier\n\t"
    "s_setprio 3\n\t"
    "s_waitcnt lgkmcnt(5)\n\t"
    "v_mfma_f32_16x16x32_bf16 v[240:243], v[24:27], v[152:155], v[240:243]\n\t"
    "ds_read_b64_tr_b16 v[38:39], v1 offset:61952\n\t"
    "v_mfma_f32_16x16x32_bf16 v[248:251], v[24:27], v[156:159], v[248:251]\n\t"
    "v_add_u32 v220, 0x10000, v220\n\t"
    "ds_read_b128 v[168:171], v220 offset:0\n\t"
    "v_add_u32 v0, 0xfffe8000, v0\n\t"
    "ds_read_b64_tr_b16 v[24:25], v0 offset:0\n\t"
    "s_setprio 2\n\t"
    "s_waitcnt lgkmcnt(6)\n\t"
    "v_mfma_f32_16x16x32_bf16 v[244:247], v[28:31], v[152:155], v[244:247]\n\t"
    "ds_read_b64_tr_b16 v[26:27], v0 offset:512\n\t"
    "v_mfma_f32_16x16x32_bf16 v[252:255], v[28:31], v[156:159], v[252:255]\n\t"
    "ds_read_b128 v[172:175], v220 offset:2048\n\t"
    "v_add_u32 v1, 0xfffe8000, v1\n\t"
    "ds_read_b64_tr_b16 v[28:29], v1 offset:0\n\t"
    "s_setprio 1\n\t"
    "s_waitcnt lgkmcnt(7)\n\t"
    "v_mfma_f32_16x16x32_bf16 v[240:243], v[32:35], v[160:163], v[240:243]\n\t"
    "ds_read_b64_tr_b16 v[30:31], v1 offset:512\n\t"
    "v_mfma_f32_16x16x32_bf16 v[248:251], v[32:35], v[164:167], v[248:251]\n\t"
    "ds_read_b64_tr_b16 v[32:33], v0 offset:4096\n\t"
    "v_add_u32 v221, 0x10000, v221\n\t"
    "ds_read_b128 v[176:179], v221 offset:0\n\t"
    "s_setprio 0\n\t"
    "s_waitcnt lgkmcnt(8)\n\t"
    "v_mfma_f32_16x16x32_bf16 v[244:247], v[36:39], v[160:163], v[244:247]\n\t"
    "ds_read_b64_tr_b16 v[34:35], v0 offset:4608\n\t"
    "v_mfma_f32_16x16x32_bf16 v[252:255], v[36:39], v[164:167], v[252:255]\n\t"
    "ds_read_b64_tr_b16 v[36:37], v1 offset:4096\n\t"
    "ds_read_b128 v[180:183], v221 offset:2048\n\t"
    "s_waitcnt vmcnt(11)\n\t"
    "s_barrier\n\t"
    "s_setprio 3\n\t"
    "s_waitcnt lgkmcnt(8)\n\t"
    "v_mfma_f32_16x16x32_bf16 v[224:227], v[24:27], v[168:171], v[224:227]\n\t"
    "ds_read_b64_tr_b16 v[38:39], v1 offset:4608\n\t"
    "s_waitcnt lgkmcnt(8)\n\t"
    "v_mfma_f32_16x16x32_bf16 v[232:235], v[24:27], v[172:175], v[232:235]\n\t"
    "ds_read_b64_tr_b16 v[24:25], v0 offset:8192\n\t"
    "s_waitcnt lgkmcnt(7)\n\t"
    "v_mfma_f32_16x16x32_bf16 v[228:231], v[28:31], v[168:171], v[228:231]\n\t"
    "ds_read_b64_tr_b16 v[26:27], v0 offset:8704\n\t"
    "v_mfma_f32_16x16x32_bf16 v[236:239], v[28:31], v[172:175], v[236:239]\n\t"
    "ds_read_b64_tr_b16 v[28:29], v1 offset:8192\n\t"
    "s_waitcnt lgkmcnt(6)\n\t"
    "v_mfma_f32_16x16x32_bf16 v[224:227], v[32:35], v[176:179], v[224:227]\n\t"
    "ds_read_b64_tr_b16 v[30:31], v1 offset:8704\n\t"
    "s_waitcnt lgkmcnt(5)\n\t"
    "v_mfma_f32_16x16x32_bf16 v[232:235], v[32:35], v[180:183], v[232:235]\n\t"
    "ds_read_b64_tr_b16 v[32:33], v0 offset:12288\n\t"
    "s_waitcnt lgkmcnt(5)\n\t"
    "v_mfma_f32_16x16x32_bf16 v[228:231], v[36:39], v[176:179], v[228:231]\n\t"
    "ds_read_b64_tr_b16 v[34:35], v0 offset:12800\n\t"
    "v_mfma_f32_16x16x32_bf16 v[236:239], v[36:39], v[180:183], v[236:239]\n\t"
    "ds_read_b64_tr_b16 v[36:37], v1 offset:12288\n\t"
    "s_waitcnt lgkmcnt(5)\n\t"
    "v_mfma_f32_16x16x32_bf16 v[240:243], v[24:27], v[168:171], v[240:243]\n\t"
    "ds_read_b64_tr_b16 v[38:39], v1 offset:12800\n\t"
    "v_mul_f32 v12, 0xbfb8aa3b, v224\n\t"
    "v_mul_f32 v13, 0xbfb8aa3b, v225\n\t"
    "v_exp_f32 v12, v12\n\t"
    "v_mfma_f32_16x16x32_bf16 v[248:251], v[24:27], v[172:175], v[248:251]\n\t"
    "v_add_u32 v0, 0x14000, v0\n\t"
    "ds_read_b64_tr_b16 v[24:25], v0 offset:0\n\t"
    "v_exp_f32 v13, v13\n\t"
    "v_add_f32 v12, 1.0, v12\n\t"
    "v_add_f32 v13, 1.0, v13\n\t"
    "s_waitcnt lgkmcnt(5)\n\t"
    "v_mfma_f32_16x16x32_bf16 v[244:247], v[28:31], v[168:171], v[244:247]\n\t"
    "ds_read_b64_tr_b16 v[26:27], v0 offset:512\n\t"
    "v_rcp_f32 v12, v12\n\t"
    "v_rcp_f32 v13, v13\n\t"
    "v_mfma_f32_16x16x32_bf16 v[252:255], v[28:31], v[172:175], v[252:255]\n\t"
    "v_add_u32 v1, 0x14000, v1\n\t"
    "ds_read_b64_tr_b16 v[28:29], v1 offset:0\n\t"
    "v_mul_f32 v12, v224, v12\n\t"
    "v_mul_f32 v13, v225, v13\n\t"
    "v_cvt_pk_bf16_f32 v184, v12, v13\n\t"
    "s_setprio 2\n\t"
    "s_waitcnt lgkmcnt(5)\n\t"
    "v_mfma_f32_16x16x32_bf16 v[240:243], v[32:35], v[176:179], v[240:243]\n\t"
    "ds_read_b64_tr_b16 v[30:31], v1 offset:512\n\t"
    "v_mul_f32 v14, 0xbfb8aa3b, v226\n\t"
    "v_mul_f32 v15, 0xbfb8aa3b, v227\n\t"
    "v_exp_f32 v14, v14\n\t"
    "v_mfma_f32_16x16x32_bf16 v[248:251], v[32:35], v[180:183], v[248:251]\n\t"
    "ds_read_b64_tr_b16 v[32:33], v0 offset:4096\n\t"
    "v_exp_f32 v15, v15\n\t"
    "v_add_f32 v14, 1.0, v14\n\t"
    "v_add_f32 v15, 1.0, v15\n\t"
    "s_waitcnt lgkmcnt(5)\n\t"
    "v_mfma_f32_16x16x32_bf16 v[244:247], v[36:39], v[176:179], v[244:247]\n\t"
    "ds_read_b64_tr_b16 v[34:35], v0 offset:4608\n\t"
    "v_rcp_f32 v14, v14\n\t"
    "v_rcp_f32 v15, v15\n\t"
    "v_mfma_f32_16x16x32_bf16 v[252:255], v[36:39], v[180:183], v[252:255]\n\t"
    "ds_read_b64_tr_b16 v[36:37], v1 offset:4096\n\t"
    "v_mul_f32 v14, v226, v14\n\t"
    "v_mul_f32 v15, v227, v15\n\t"
    "v_cvt_pk_bf16_f32 v185, v14, v15\n\t"
    "s_setprio 1\n\t"
    "s_waitcnt lgkmcnt(5)\n\t"
    "v_mfma_f32_16x16x32_bf16 v[200:203], v[24:27], v[40:43], 0\n\t"
    "ds_read_b64_tr_b16 v[38:39], v1 offset:4608\n\t"
    "v_mul_f32 v12, 0xbfb8aa3b, v228\n\t"
    "v_mul_f32 v13, 0xbfb8aa3b, v229\n\t"
    "v_exp_f32 v12, v12\n\t"
    "v_mfma_f32_16x16x32_bf16 v[208:211], v[24:27], v[44:47], 0\n\t"
    "ds_read_b64_tr_b16 v[24:25], v0 offset:8192\n\t"
    "v_exp_f32 v13, v13\n\t"
    "v_add_f32 v12, 1.0, v12\n\t"
    "v_add_f32 v13, 1.0, v13\n\t"
    "s_waitcnt lgkmcnt(5)\n\t"
    "v_mfma_f32_16x16x32_bf16 v[204:207], v[28:31], v[40:43], 0\n\t"
    "ds_read_b64_tr_b16 v[26:27], v0 offset:8704\n\t"
    "v_rcp_f32 v12, v12\n\t"
    "v_rcp_f32 v13, v13\n\t"
    "v_mfma_f32_16x16x32_bf16 v[212:215], v[28:31], v[44:47], 0\n\t"
    "ds_read_b64_tr_b16 v[28:29], v1 offset:8192\n\t"
    "v_mul_f32 v12, v228, v12\n\t"
    "v_mul_f32 v13, v229, v13\n\t"
    "v_cvt_pk_bf16_f32 v186, v12, v13\n\t"
    "s_setprio 0\n\t"
    "s_waitcnt lgkmcnt(5)\n\t"
    "v_mfma_f32_16x16x32_bf16 v[200:203], v[32:35], v[48:51], v[200:203]\n\t"
    "ds_read_b64_tr_b16 v[30:31], v1 offset:8704\n\t"
    "v_mul_f32 v14, 0xbfb8aa3b, v230\n\t"
    "v_mul_f32 v15, 0xbfb8aa3b, v231\n\t"
    "v_exp_f32 v14, v14\n\t"
    "v_mfma_f32_16x16x32_bf16 v[208:211], v[32:35], v[52:55], v[208:211]\n\t"
    "ds_read_b64_tr_b16 v[32:33], v0 offset:12288\n\t"
    "v_exp_f32 v15, v15\n\t"
    "v_add_f32 v14, 1.0, v14\n\t"
    "v_add_f32 v15, 1.0, v15\n\t"
    "s_waitcnt lgkmcnt(5)\n\t"
    "v_mfma_f32_16x16x32_bf16 v[204:207], v[36:39], v[48:51], v[204:207]\n\t"
    "ds_read_b64_tr_b16 v[34:35], v0 offset:12800\n\t"
    "v_rcp_f32 v14, v14\n\t"
    "v_rcp_f32 v15, v15\n\t"
    "v_mfma_f32_16x16x32_bf16 v[212:215], v[36:39], v[52:55], v[212:215]\n\t"
    "ds_read_b64_tr_b16 v[36:37], v1 offset:12288\n\t"
    "v_mul_f32 v14, v230, v14\n\t"
    "v_mul_f32 v15, v231, v15\n\t"
    "v_cvt_pk_bf16_f32 v187, v14, v15\n\t"
    "s_waitcnt vmcnt(8)\n\t"
    "s_barrier\n\t"
    "s_setprio 3\n\t"
    "s_waitcnt lgkmcnt(5)\n\t"
    "v_mfma_f32_16x16x32_bf16 v[200:203], v[24:27], v[56:59], v[200:203]\n\t"
    "ds_read_b64_tr_b16 v[38:39], v1 offset:12800\n\t"
    "s_add_u32 s85, s47, 0x0\n\t"
    "buffer_store_dwordx4 v[184:187], v6, s[28:31], s85 offen sc1\n\t"
    "v_add3_u32 v20, v184, v185, v186\n\t"
    "v_add_u32 v20, v20, v187\n\t"
    "v_mul_f32 v12, 0xbfb8aa3b, v232\n\t"
    "v_mul_f32 v13, 0xbfb8aa3b, v233\n\t"
    "v_exp_f32 v12, v12\n\t"
    "v_mfma_f32_16x16x32_bf16 v[208:211], v[24:27], v[60:63], v[208:211]\n\t"
    "ds_read_b64_tr_b16 v[24:25], v0 offset:16384\n\t"
    "ds_read_b64_tr_b16 v[26:27], v0 offset:16896\n\t"
    "v_exp_f32 v13, v13\n\t"
    "v_add_f32 v12, 1.0, v12\n\t"
    "v_add_f32 v13, 1.0, v13\n\t"
    "s_waitcnt lgkmcnt(6)\n\t"
    "v_mfma_f32_16x16x32_bf16 v[204:207], v[28:31], v[56:59], v[204:207]\n\t"
    "v_rcp_f32 v12, v12\n\t"
    "v_rcp_f32 v13, v13\n\t"
    "v_mfma_f32_16x16x32_bf16 v[212:215], v[28:31], v[60:63], v[212:215]\n\t"
    "ds_read_b64_tr_b16 v[28:29], v1 offset:16384\n\t"
    "ds_read_b64_tr_b16 v[30:31], v1 offset:16896\n\t"
    "v_mul_f32 v12, v232, v12\n\t"
    "v_mul_f32 v13, v233, v13\n\t"
    "v_cvt_pk_bf16_f32 v188, v12, v13\n\t"
    "s_waitcnt lgkmcnt(6)\n\t"
    "v_mfma_f32_16x16x32_bf16 v[200:203], v[32:35], v[64:67], v[200:203]\n\t"
    "v_mul_f32 v14, 0xbfb8aa3b, v234\n\t"
    "v_mul_f32 v15, 0xbfb8aa3b, v235\n\t"
    "v_exp_f32 v14, v14\n\t"
    "v_mfma_f32_16x16x32_bf16 v[208:211], v[32:35], v[68:71], v[208:211]\n\t"
    "ds_read_b64_tr_b16 v[32:33], v0 offset:20480\n\t"
    "ds_read_b64_tr_b16 v[34:35], v0 offset:20992\n\t"
    "v_exp_f32 v15, v15\n\t"
    "v_add_f32 v14, 1.0, v14\n\t"
    "v_add_f32 v15, 1.0, v15\n\t"
    "s_waitcnt lgkmcnt(6)\n\t"
    "v_mfma_f32_16x16x32_bf16 v[204:207], v[36:39], v[64:67], v[204:207]\n\t"
    "v_rcp_f32 v14, v14\n\t"
    "v_rcp_f32 v15, v15\n\t"
    "v_mfma_f32_16x16x32_bf16 v[212:215], v[36:39], v[68:71], v[212:215]\n\t"
    "ds_read_b64_tr_b16 v[36:37], v1 offset:20480\n\t"
    "ds_read_b64_tr_b16 v[38:39], v1 offset:20992\n\t"
    "v_mul_f32 v14, v234, v14\n\t"
    "v_mul_f32 v15, v235, v15\n\t"
    "v_cvt_pk_bf16_f32 v189, v14, v15\n\t"
    "s_waitcnt lgkmcnt(6)\n\t"
    "v_mfma_f32_16x16x32_bf16 v[200:203], v[24:27], v[72:75], v[200:203]\n\t"
    "v_mul_f32 v12, 0xbfb8aa3b, v236\n\t"
    "v_mul_f32 v13, 0xbfb8aa3b, v237\n\t"
    "v_exp_f32 v12, v12\n\t"
    "v_mfma_f32_16x16x32_bf16 v[208:211], v[24:27], v[76:79], v[208:211]\n\t"
    "ds_read_b64_tr_b16 v[24:25], v0 offset:24576\n\t"
    "ds_read_b64_tr_b16 v[26:27], v0 offset:25088\n\t"
    "v_exp_f32 v13, v13\n\t"
    "v_add_f32 v12, 1.0, v12\n\t"
    "v_add_f32 v13, 1.0, v13\n\t"
    "s_waitcnt lgkmcnt(6)\n\t"
    "v_mfma_f32_16x16x32_bf16 v[204:207], v[28:31], v[72:75], v[204:207]\n\t"
    "v_rcp_f32 v12, v12\n\t"
    "v_rcp_f32 v13, v13\n\t"
    "v_mfma_f32_16x16x32_bf16 v[212:215], v[28:31], v[76:79], v[212:215]\n\t"
    "ds_read_b64_tr_b16 v[28:29], v1 offset:24576\n\t"
    "ds_read_b64_tr_b16 v[30:31], v1 offset:25088\n\t"
    "v_mul_f32 v12, v236, v12\n\t"
    "v_mul_f32 v13, v237, v13\n\t"
    "v_cvt_pk_bf16_f32 v190, v12, v13\n\t"
    "s_setprio 2\n\t"
    "s_waitcnt lgkmcnt(6)\n\t"
    "v_mfma_f32_16x16x32_bf16 v[200:203], v[32:35], v[80:83], v[200:203]\n\t"
    "v_mul_f32 v14, 0xbfb8aa3b, v238\n\t"
    "v_mul_f32 v15, 0xbfb8aa3b, v239\n\t"
    "v_exp_f32 v14, v14\n\t"
    "v_mfma_f32_16x16x32_bf16 v[208:211], v[32:35], v[84:87], v[208:211]\n\t"
    "ds_read_b64_tr_b16 v[32:33], v0 offset:28672\n\t"
    "ds_read_b64_tr_b16 v[34:35], v0 offset:29184\n\t"
    "v_exp_f32 v15, v15\n\t"
    "v_add_f32 v14, 1.0, v14\n\t"
    "v_add_f32 v15, 1.0, v15\n\t"
    "s_waitcnt lgkmcnt(6)\n\t"
    "v_mfma_f32_16x16x32_bf16 v[204:207], v[36:39], v[80:83], v[204:207]\n\t"
    "v_rcp_f32 v14, v14\n\t"
    "v_rcp_f32 v15, v15\n\t"
    "v_mfma_f32_16x16x32_bf16 v[212:215], v[36:39], v[84:87], v[212:215]\n\t"
    "ds_read_b64_tr_b16 v[36:37], v1 offset:28672\n\t"
    "ds_read_b64_tr_b16 v[38:39], v1 offset:29184\n\t"
    "v_mul_f32 v14, v238, v14\n\t"
    "v_mul_f32 v15, v239, v15\n\t"
    "v_cvt_pk_bf16_f32 v191, v14, v15\n\t"
    "s_setprio 1\n\t"
    "s_waitcnt lgkmcnt(6)\n\t"
    "v_mfma_f32_16x16x32_bf16 v[200:203], v[24:27], v[88:91], v[200:203]\n\t"
    "s_add_u32 s85, s47, 0x0\n\t"
    "buffer_store_dwordx4 v[188:191], v6, s[28:31], s85 offen offset:1024 sc1\n\t"
    "v_add3_u32 v20, v20, v188, v189\n\t"
    "v_add3_u32 v20, v20, v190, v191\n\t"
    "v_sub_u32 v20, s55, v20\n\t"
    "buffer_store_dword v20, v7, s[28:31], s85 offen offset:2048 sc1\n\t"
    "v_mfma_f32_16x16x32_bf16 v[208:211], v[24:27], v[92:95], v[208:211]\n\t"
    "ds_read_b64_tr_b16 v[24:25], v0 offset:32768\n\t"
    "ds_read_b64_tr_b16 v[26:27], v0 offset:33280\n\t"
    "v_mul_f32 v12, 0xbfb8aa3b, v240\n\t"
    "v_mul_f32 v13, 0xbfb8aa3b, v241\n\t"
    "v_exp_f32 v12, v12\n\t"
    "s_waitcnt lgkmcnt(6)\n\t"
    "v_mfma_f32_16x16x32_bf16 v[204:207], v[28:31], v[88:91], v[204:207]\n\t"
    "s_add_u32 m0, s36, 49152\n\t"
    "s_add_u32 s85, s2, 0x180\n\t"
    "buffer_load_dwordx4 v4, s[20:23], s85 offen lds\n\t"
    "v_mfma_f32_16x16x32_bf16 v[212:215], v[28:31], v[92:95], v[212:215]\n\t"
    "ds_read_b64_tr_b16 v[28:29], v1 offset:32768\n\t"
    "ds_read_b64_tr_b16 v[30:31], v1 offset:33280\n\t"
    "v_exp_f32 v13, v13\n\t"
    "v_add_f32 v12, 1.0, v12\n\t"
    "v_add_f32 v13, 1.0, v13\n\t"
    "s_setprio 0\n\t"
    "s_waitcnt lgkmcnt(6)\n\t"
    "v_mfma_f32_16x16x32_bf16 v[200:203], v[32:35], v[96:99], v[200:203]\n\t"
    "s_add_u32 m0, s36, 57344\n\t"
    "s_add_u32 s85, s3, 0x180\n\t"
    "buffer_load_dwordx4 v4, s[20:23], s85 offen lds\n\t"
    "v_mfma_f32_16x16x32_bf16 v[208:211], v[32:35], v[100:103], v[208:211]\n\t"
    "ds_read_b64_tr_b16 v[32:33], v0 offset:36864\n\t"
    "v_rcp_f32 v12, v12\n\t"
    "v_rcp_f32 v13, v13\n\t"
    "s_waitcnt lgkmcnt(5)\n\t"
    "v_mfma_f32_16x16x32_bf16 v[204:207], v[36:39], v[96:99], v[204:207]\n\t"
    "ds_read_b64_tr_b16 v[34:35], v0 offset:37376\n\t"
    "v_mfma_f32_16x16x32_bf16 v[212:215], v[36:39], v[100:103], v[212:215]\n\t"
    "ds_read_b64_tr_b16 v[36:37], v1 offset:36864\n\t"
    "v_mul_f32 v12, v240, v12\n\t"
    "v_mul_f32 v13, v241, v13\n\t"
    "v_cvt_pk_bf16_f32 v192, v12, v13\n\t"
    "s_waitcnt vmcnt(10)\n\t"
    "s_barrier\n\t"
    "s_setprio 3\n\t"
    "s_waitcnt lgkmcnt(5)\n\t"
    "v_mfma_f32_16x16x32_bf16 v[200:203], v[24:27], v[104:107], v[200:203]\n\t"
    "ds_read_b64_tr_b16 v[38:39], v1 offset:37376\n\t"
    "v_mfma_f32_16x16x32_bf16 v[208:211], v[24:27], v[108:111], v[208:211]\n\t"
    "v_add_u32 v0, 0xffff0000, v0\n\t"
    "ds_read_b64_tr_b16 v[24:25], v0 offset:0\n\t"
    "ds_read_b64_tr_b16 v[26:27], v0 offset:512\n\t"
    "v_mul_f32 v12, 0xbfb8aa3b, v242\n\t"
    "v_mul_f32 v13, 0xbfb8aa3b, v243\n\t"
    "v_exp_f32 v12, v12\n\t"
    "s_waitcnt lgkmcnt(6)\n\t"
    "v_mfma_f32_16x16x32_bf16 v[204:207], v[28:31], v[104:107], v[204:207]\n\t"
    "s_add_u32 m0, s36, 65536\n\t"
    "s_add_u32 s85, s4, 0x180\n\t"
    "buffer_load_dwordx4 v4, s[20:23], s85 offen lds\n\t"
    "v_mfma_f32_16x16x32_bf16 v[212:215], v[28:31], v[108:111], v[212:215]\n\t"
    "v_add_u32 v1, 0xffff0000, v1\n\t"
    "ds_read_b64_tr_b16 v[28:29], v1 offset:0\n\t"
    "ds_read_b64_tr_b16 v[30:31], v1 offset:512\n\t"
    "v_exp_f32 v13, v13\n\t"
    "v_add_f32 v12, 1.0, v12\n\t"
    "v_add_f32 v13, 1.0, v13\n\t"
    "s_waitcnt lgkmcnt(6)\n\t"
    "v_mfma_f32_16x16x32_bf16 v[200:203], v[32:35], v[112:115], v[200:203]\n\t"
    "s_add_u32 m0, s36, 73728\n\t"
    "s_add_u32 s85, s5, 0x180\n\t"
    "buffer_load_dwordx4 v4, s[20:23], s85 offen lds\n\t"
    "v_mfma_f32_16x16x32_bf16 v[208:211], v[32:35], v[116:119], v[208:211]\n\t"
    "ds_read_b64_tr_b16 v[32:33], v0 offset:4096\n\t"
    "v_rcp_f32 v12, v12\n\t"
    "v_rcp_f32 v13, v13\n\t"
    "s_waitcnt lgkmcnt(5)\n\t"
    "v_mfma_f32_16x16x32_bf16 v[204:207], v[36:39], v[112:115], v[204:207]\n\t"
    "ds_read_b64_tr_b16 v[34:35], v0 offset:4608\n\t"
    "v_mfma_f32_16x16x32_bf16 v[212:215], v[36:39], v[116:119], v[212:215]\n\t"
    "ds_read_b64_tr_b16 v[36:37], v1 offset:4096\n\t"
    "v_mul_f32 v12, v242, v12\n\t"
    "v_mul_f32 v13, v243, v13\n\t"
    "v_cvt_pk_bf16_f32 v193, v12, v13\n\t"
    "s_waitcnt lgkmcnt(5)\n\t"
    "v_mfma_f32_16x16x32_bf16 v[200:203], v[24:27], v[120:123], v[200:203]\n\t"
    "ds_read_b64_tr_b16 v[38:39], v1 offset:4608\n\t"
    "v_mfma_f32_16x16x32_bf16 v[208:211], v[24:27], v[124:127], v[208:211]\n\t"
    "ds_read_b64_tr_b16 v[24:25], v0 offset:8192\n\t"
    "ds_read_b64_tr_b16 v[26:27], v0 offset:8704\n\t"
    "v_mul_f32 v12, 0xbfb8aa3b, v244\n\t"
    "v_mul_f32 v13, 0xbfb8aa3b, v245\n\t"
    "v_exp_f32 v12, v12\n\t"
    "s_waitcnt lgkmcnt(6)\n\t"
    "v_mfma_f32_16x16x32_bf16 v[204:207], v[28:31], v[120:123], v[204:207]\n\t"
    "s_add_u32 m0, s36, 81920\n\t"
    "s_add_u32 s85, s6, 0x180\n\t"
    "LIPA1x0_%=:\n\t"
    "s_cbranch_execz LIPRA_%=\n\t"
    "buffer_load_dwordx4 v4, s[20:23], s85 offen lds\n\t"
    "v_mfma_f32_16x16x32_bf16 v[212:215], v[28:31], v[124:127], v[212:215]\n\t"
    "ds_read_b64_tr_b16 v[28:29], v1 offset:8192\n\t"
    "ds_read_b64_tr_b16 v[30:31], v1 offset:8704\n\t"
    "v_exp_f32 v13, v13\n\t"
    "v_add_f32 v12, 1.0, v12\n\t"
    "v_add_f32 v13, 1.0, v13\n\t"
    "s_setprio 2\n\t"
    "s_waitcnt lgkmcnt(6)\n\t"
    "v_mfma_f32_16x16x32_bf16 v[200:203], v[32:35], v[128:131], v[200:203]\n\t"
    "s_add_u32 m0, s36, 90112\n\t"
    "s_add_u32 s85, s7, 0x180\n\t"
    "buffer_load_dwordx4 v4, s[20:23], s85 offen lds\n\t"
    "v_mfma_f32_16x16x32_bf16 v[208:211], v[32:35], v[132:135], v[208:211]\n\t"
    "ds_read_b64_tr_b16 v[32:33], v0 offset:12288\n\t"
    "v_rcp_f32 v12, v12\n\t"
    "v_rcp_f32 v13, v13\n\t"
    "s_waitcnt lgkmcnt(5)\n\t"
    "v_mfma_f32_16x16x32_bf16 v[204:207], v[36:39], v[128:131], v[204:207]\n\t"
    "ds_read_b64_tr_b16 v[34:35], v0 offset:12800\n\t"
    "v_mfma_f32_16x16x32_bf16 v[212:215], v[36:39], v[132:135], v[212:215]\n\t"
    "ds_read_b64_tr_b16 v[36:37], v1 offset:12288\n\t"
    "ds_read_b64_tr_b16 v[38:39], v1 offset:12800\n\t"
    "v_mul_f32 v12, v244, v12\n\t"
    "v_mul_f32 v13, v245, v13\n\t"
    "v_cvt_pk_bf16_f32 v194, v12, v13\n\t"
    "s_setprio 1\n\t"
    "s_waitcnt lgkmcnt(6)\n\t"
    "v_mfma_f32_16x16x32_bf16 v[200:203], v[24:27], v[136:139], v[200:203]\n\t"
    "v_mfma_f32_16x16x32_bf16 v[208:211], v[24:27], v[140:143], v[208:211]\n\t"
    "v_add_u32 v0, 0x14000, v0\n\t"
    "ds_read_b64_tr_b16 v[24:25], v0 offset:24576\n\t"
    "ds_read_b64_tr_b16 v[26:27], v0 offset:25088\n\t"
    "v_mul_f32 v12, 0xbfb8aa3b, v246\n\t"
    "v_mul_f32 v13, 0xbfb8aa3b, v247\n\t"
    "v_exp_f32 v12, v12\n\t"
    "s_add_u32 s84, s90, 0\n\t"
    "s_sub_u32 s83, s84, 3\n\t"
    "s_cmp_ge_u32 s84, 3\n\t"
    "s_cselect_b32 s84, s83, s84\n\t"
    "s_mul_i32 s83, s84, 73728\n\t"
    "s_add_u32 s37, s42, s83\n\t"
    "s_mul_i32 s83, s84, 18432\n\t"
    "s_add_u32 s9, s46, s83\n\t"
    "s_waitcnt lgkmcnt(6)\n\t"
    "v_mfma_f32_16x16x32_bf16 v[204:207], v[28:31], v[136:139], v[204:207]\n\t"
    "s_add_u32 m0, s36, 98304\n\t"
    "s_add_u32 s85, s8, 0x180\n\t"
    "buffer_load_dwordx4 v4, s[20:23], s85 offen lds\n\t"
    "v_mfma_f32_16x16x32_bf16 v[212:215], v[28:31], v[140:143], v[212:215]\n\t"
    "v_add_u32 v1, 0x14000, v1\n\t"
    "ds_read_b64_tr_b16 v[28:29], v1 offset:24576\n\t"
    "ds_read_b64_tr_b16 v[30:31], v1 offset:25088\n\t"
    "v_exp_f32 v13, v13\n\t"
    "v_add_f32 v12, 1.0, v12\n\t"
    "v_add_f32 v13, 1.0, v13\n\t"
    "s_setprio 0\n\t"
    "s_waitcnt lgkmcnt(6)\n\t"
    "v_mfma_f32_16x16x32_bf16 v[200:203], v[32:35], v[144:147], v[200:203]\n\t"
    "s_add_u32 m0, s36, 106496\n\t"
    "s_add_u32 s85, s0, 0x200\n\t"
    "buffer_load_dwordx4 v4, s[20:23], s85 offen lds\n\t"
    "v_mfma_f32_16x16x32_bf16 v[208:211], v[32:35], v[148:151], v[208:211]\n\t"
    "ds_read_b64_tr_b16 v[32:33], v0 offset:28672\n\t"
    "v_rcp_f32 v12, v12\n\t"
    "v_rcp_f32 v13, v13\n\t"
    "s_waitcnt lgkmcnt(5)\n\t"
    "v_mfma_f32_16x16x32_bf16 v[204:207], v[36:39], v[144:147], v[204:207]\n\t"
    "ds_read_b64_tr_b16 v[34:35], v0 offset:29184\n\t"
    "v_mfma_f32_16x16x32_bf16 v[212:215], v[36:39], v[148:151], v[212:215]\n\t"
    "ds_read_b64_tr_b16 v[36:37], v1 offset:28672\n\t"
    "ds_read_b64_tr_b16 v[38:39], v1 offset:29184\n\t"
    "v_mul_f32 v12, v246, v12\n\t"
    "v_mul_f32 v13, v247, v13\n\t"
    "v_cvt_pk_bf16_f32 v195, v12, v13\n\t"
    "s_waitcnt vmcnt(13)\n\t"
    "s_barrier\n\t"
    "s_setprio 3\n\t"
    "s_waitcnt lgkmcnt(6)\n\t"
    "v_mfma_f32_16x16x32_bf16 v[200:203], v[24:27], v[152:155], v[200:203]\n\t"
    "s_add_u32 s85, s47, 0x4800\n\t"
    "buffer_store_dwordx4 v[192:195], v6, s[28:31], s85 offen sc1\n\t"
    "v_add3_u32 v20, v192, v193, v194\n\t"
    "v_add_u32 v20, v20, v195\n\t"
    "v_mfma_f32_16x16x32_bf16 v[208:211], v[24:27], v[156:159], v[208:211]\n\t"
    "ds_read_b64_tr_b16 v[24:25], v0 offset:32768\n\t"
    "v_mul_f32 v12, 0xbfb8aa3b, v248\n\t"
    "v_mul_f32 v13, 0xbfb8aa3b, v249\n\t"
    "v_exp_f32 v12, v12\n\t"
    "s_add_u32 s84, s90, 1\n\t"
    "s_sub_u32 s83, s84, 3\n\t"
    "s_cmp_ge_u32 s84, 3\n\t"
    "s_cselect_b32 s84, s83, s84\n\t"
    "s_mul_i32 s83, s84, 73728\n\t"
    "s_add_u32 s38, s42, s83\n\t"
    "s_mul_i32 s83, s84, 18432\n\t"
    "s_add_u32 s10, s46, s83\n\t"
    "s_waitcnt lgkmcnt(5)\n\t"
    "v_mfma_f32_16x16x32_bf16 v[204:207], v[28:31], v[152:155], v[204:207]\n\t"
    "ds_read_b64_tr_b16 v[26:27], v0 offset:33280\n\t"
    "s_add_u32 m0, s36, 139264\n\t"
    "s_add_u32 s85, s1, 0x200\n\t"
    "buffer_load_dwordx4 v4, s[20:23], s85 offen lds\n\t"
    "v_mfma_f32_16x16x32_bf16 v[212:215], v[28:31], v[156:159], v[212:215]\n\t"
    "ds_read_b64_tr_b16 v[28:29], v1 offset:32768\n\t"
    "v_exp_f32 v13, v13\n\t"
    "v_add_f32 v12, 1.0, v12\n\t"
    "v_add_f32 v13, 1.0, v13\n\t"
    "s_waitcnt lgkmcnt(5)\n\t"
    "v_mfma_f32_16x16x32_bf16 v[200:203], v[32:35], v[160:163], v[200:203]\n\t"
    "ds_read_b64_tr_b16 v[30:31], v1 offset:33280\n\t"
    "v_mfma_f32_16x16x32_bf16 v[208:211], v[32:35], v[164:167], v[208:211]\n\t"
    "ds_read_b64_tr_b16 v[32:33], v0 offset:36864\n\t"
    "v_rcp_f32 v12, v12\n\t"
    "v_rcp_f32 v13, v13\n\t"
    "s_setprio 2\n\t"
    "s_waitcnt lgkmcnt(5)\n\t"
    "v_mfma_f32_16x16x32_bf16 v[204:207], v[36:39], v[160:163], v[204:207]\n\t"
    "ds_read_b64_tr_b16 v[34:35], v0 offset:37376\n\t"
    "v_mfma_f32_16x16x32_bf16 v[212:215], v[36:39], v[164:167], v[212:215]\n\t"
    "ds_read_b64_tr_b16 v[36:37], v1 offset:36864\n\t"
    "v_mul_f32 v12, v248, v12\n\t"
    "v_mul_f32 v13, v249, v13\n\t"
    "v_cvt_pk_bf16_f32 v196, v12, v13\n\t"
    "s_waitcnt lgkmcnt(5)\n\t"
    "v_mfma_f32_16x16x32_bf16 v[200:203], v[24:27], v[168:171], v[200:203]\n\t"
    "ds_read_b64_tr_b16 v[38:39], v1 offset:37376\n\t"
    "v_mfma_f32_16x16x32_bf16 v[208:211], v[24:27], v[172:175], v[208:211]\n\t"
    "v_add_u32 v0, 0xffff0000, v0\n\t"
    "ds_read_b64_tr_b16 v[24:25], v0 offset:0\n\t"
    "v_mul_f32 v12, 0xbfb8aa3b, v250\n\t"
    "v_mul_f32 v13, 0xbfb8aa3b, v251\n\t"
    "v_exp_f32 v12, v12\n\t"
    "s_add_u32 s84, s90, 2\n\t"
    "s_sub_u32 s83, s84, 3\n\t"
    "s_cmp_ge_u32 s84, 3\n\t"
    "s_cselect_b32 s84, s83, s84\n\t"
    "s_mul_i32 s83, s84, 73728\n\t"
    "s_add_u32 s96, s42, s83\n\t"
    "s_mul_i32 s83, s84, 18432\n\t"
    "s_add_u32 s97, s46, s83\n\t"
    "s_waitcnt lgkmcnt(5)\n\t"
    "v_mfma_f32_16x16x32_bf16 v[204:207], v[28:31], v[168:171], v[204:207]\n\t"
    "ds_read_b64_tr_b16 v[26:27], v0 offset:512\n\t"
    "s_add_u32 m0, s36, 147456\n\t"
    "s_add_u32 s85, s2, 0x200\n\t"
    "buffer_load_dwordx4 v4, s[20:23], s85 offen lds\n\t"
    "v_mfma_f32_16x16x32_bf16 v[212:215], v[28:31], v[172:175], v[212:215]\n\t"
    "v_add_u32 v1, 0xffff0000, v1\n\t"
    "ds_read_b64_tr_b16 v[28:29], v1 offset:0\n\t"
    "v_exp_f32 v13, v13\n\t"
    "v_add_f32 v12, 1.0, v12\n\t"
    "v_add_f32 v13, 1.0, v13\n\t"
    "s_setprio 1\n\t"
    "s_waitcnt lgkmcnt(5)\n\t"
    "v_mfma_f32_16x16x32_bf16 v[200:203], v[32:35], v[176:179], v[200:203]\n\t"
    "ds_read_b64_tr_b16 v[30:31], v1 offset:512\n\t"
    "v_mfma_f32_16x16x32_bf16 v[208:211], v[32:35], v[180:183], v[208:211]\n\t"
    "ds_read_b64_tr_b16 v[32:33], v0 offset:4096\n\t"
    "v_rcp_f32 v12, v12\n\t"
    "v_rcp_f32 v13, v13\n\t"
    "s_waitcnt lgkmcnt(5)\n\t"
    "v_mfma_f32_16x16x32_bf16 v[204:207], v[36:39], v[176:179], v[204:207]\n\t"
    "ds_read_b64_tr_b16 v[34:35], v0 offset:4608\n\t"
    "v_mfma_f32_16x16x32_bf16 v[212:215], v[36:39], v[180:183], v[212:215]\n\t"
    "ds_read_b64_tr_b16 v[36:37], v1 offset:4096\n\t"
    "v_mul_f32 v12, v250, v12\n\t"
    "v_mul_f32 v13, v251, v13\n\t"
    "v_cvt_pk_bf16_f32 v197, v12, v13\n\t"
    "s_waitcnt lgkmcnt(5)\n\t"
    "v_mfma_f32_16x16x32_bf16 v[224:227], v[24:27], v[40:43], 0\n\t"
    "ds_read_b64_tr_b16 v[38:39], v1 offset:4608\n\t"
    "v_mfma_f32_16x16x32_bf16 v[232:235], v[24:27], v[44:47], 0\n\t"
    "ds_read_b64_tr_b16 v[24:25], v0 offset:8192\n\t"
    "v_mul_f32 v12, 0xbfb8aa3b, v252\n\t"
    "v_mul_f32 v13, 0xbfb8aa3b, v253\n\t"
    "v_exp_f32 v12, v12\n\t"
    "s_setprio 0\n\t"
    "s_waitcnt lgkmcnt(5)\n\t"
    "v_mfma_f32_16x16x32_bf16 v[228:231], v[28:31], v[40:43], 0\n\t"
    "ds_read_b64_tr_b16 v[26:27], v0 offset:8704\n\t"
    "s_add_u32 m0, s36, 155648\n\t"
    "s_add_u32 s85, s3, 0x200\n\t"
    "buffer_load_dwordx4 v4, s[20:23], s85 offen lds\n\t"
    "v_mfma_f32_16x16x32_bf16 v[236:239], v[28:31], v[44:47], 0\n\t"
    "ds_read_b64_tr_b16 v[28:29], v1 offset:8192\n\t"
    "v_exp_f32 v13, v13\n\t"
    "v_add_f32 v12, 1.0, v12\n\t"
    "v_add_f32 v13, 1.0, v13\n\t"
    "s_waitcnt lgkmcnt(5)\n\t"
    "v_mfma_f32_16x16x32_bf16 v[224:227], v[32:35], v[48:51], v[224:227]\n\t"
    "ds_read_b64_tr_b16 v[30:31], v1 offset:8704\n\t"
    "s_add_u32 m0, s36, 0\n\t"
    "s_add_u32 s85, s4, 0x200\n\t"
    "buffer_load_dwordx4 v4, s[20:23], s85 offen lds\n\t"
    "v_mfma_f32_16x16x32_bf16 v[232:235], v[32:35], v[52:55], v[232:235]\n\t"
    "ds_read_b64_tr_b16 v[32:33], v0 offset:12288\n\t"
    "v_rcp_f32 v12, v12\n\t"
    "v_rcp_f32 v13, v13\n\t"
    "s_waitcnt lgkmcnt(5)\n\t"
    "v_mfma_f32_16x16x32_bf16 v[228:231], v[36:39], v[48:51], v[228:231]\n\t"
    "ds_read_b64_tr_b16 v[34:35], v0 offset:12800\n\t"
    "v_mfma_f32_16x16x32_bf16 v[236:239], v[36:39], v[52:55], v[236:239]\n\t"
    "ds_read_b64_tr_b16 v[36:37], v1 offset:12288\n\t"
    "v_mul_f32 v12, v252, v12\n\t"
    "v_mul_f32 v13, v253, v13\n\t"
    "v_cvt_pk_bf16_f32 v198, v12, v13\n\t"
    "s_waitcnt vmcnt(10)\n\t"
    "s_barrier\n\t"
    "s_setprio 3\n\t"
    "s_waitcnt lgkmcnt(5)\n\t"
    "v_mfma_f32_16x16x32_bf16 v[224:227], v[24:27], v[56:59], v[224:227]\n\t"
    "ds_read_b64_tr_b16 v[38:39], v1 offset:12800\n\t"
    "v_mfma_f32_16x16x32_bf16 v[232:235], v[24:27], v[60:63], v[232:235]\n\t"
    "ds_read_b64_tr_b16 v[24:25], v0 offset:16384\n\t"
    "ds_read_b64_tr_b16 v[26:27], v0 offset:16896\n\t"
    "v_mul_f32 v12, 0xbfb8aa3b, v254\n\t"
    "v_mul_f32 v13, 0xbfb8aa3b, v255\n\t"
    "v_exp_f32 v12, v12\n\t"
    "s_waitcnt lgkmcnt(6)\n\t"
    "v_mfma_f32_16x16x32_bf16 v[228:231], v[28:31], v[56:59], v[228:231]\n\t"
    "s_add_u32 m0, s36, 8192\n\t"
    "s_add_u32 s85, s5, 0x200\n\t"
    "buffer_load_dwordx4 v4, s[20:23], s85 offen lds\n\t"
    "v_mfma_f32_16x16x32_bf16 v[236:239], v[28:31], v[60:63], v[236:239]\n\t"
    "ds_read_b64_tr_b16 v[28:29], v1 offset:16384\n\t"
    "v_exp_f32 v13, v13\n\t"
    "v_add_f32 v12, 1.0, v12\n\t"
    "v_add_f32 v13, 1.0, v13\n\t"
    "s_waitcnt lgkmcnt(5)\n\t"
    "v_mfma_f32_16x16x32_bf16 v[224:227], v[32:35], v[64:67], v[224:227]\n\t"
    "ds_read_b64_tr_b16 v[30:31], v1 offset:16896\n\t"
    "v_mfma_f32_16x16x32_bf16 v[232:235], v[32:35], v[68:71], v[232:235]\n\t"
    "ds_read_b64_tr_b16 v[32:33], v0 offset:20480\n\t"
    "v_rcp_f32 v12, v12\n\t"
    "v_rcp_f32 v13, v13\n\t"
    "s_waitcnt lgkmcnt(5)\n\t"
    "v_mfma_f32_16x16x32_bf16 v[228:231], v[36:39], v[64:67], v[228:231]\n\t"
    "ds_read_b64_tr_b16 v[34:35], v0 offset:20992\n\t"
    "v_mfma_f32_16x16x32_bf16 v[236:239], v[36:39], v[68:71], v[236:239]\n\t"
    "v_mul_f32 v12, v254, v12\n\t"
    "v_mul_f32 v13, v255, v13\n\t"
    "v_cvt_pk_bf16_f32 v199, v12, v13\n\t"
    "ds_read_b64_tr_b16 v[36:37], v1 offset:20480\n\t"
    "ds_read_b64_tr_b16 v[38:39], v1 offset:20992\n\t"
    "s_waitcnt lgkmcnt(6)\n\t"
    "v_mfma_f32_16x16x32_bf16 v[224:227], v[24:27], v[72:75], v[224:227]\n\t"
    "s_add_u32 s85, s47, 0x4800\n\t"
    "buffer_store_dwordx4 v[196:199], v6, s[28:31], s85 offen offset:1024 sc1\n\t"
    "v_add3_u32 v20, v20, v196, v197\n\t"
    "v_add3_u32 v20, v20, v198, v199\n\t"
    "v_sub_u32 v20, s55, v20\n\t"
    "buffer_store_dword v20, v7, s[28:31], s85 offen offset:2048 sc1\n\t"
    "v_mfma_f32_16x16x32_bf16 v[232:235], v[24:27], v[76:79], v[232:235]\n\t"
    "ds_read_b64_tr_b16 v[24:25], v0 offset:24576\n\t"
    "ds_read_b64_tr_b16 v[26:27], v0 offset:25088\n\t"
    "v_mul_f32 v12, 0xbfb8aa3b, v200\n\t"
    "v_mul_f32 v13, 0xbfb8aa3b, v201\n\t"
    "v_exp_f32 v12, v12\n\t"
    "s_waitcnt lgkmcnt(6)\n\t"
    "v_mfma_f32_16x16x32_bf16 v[228:231], v[28:31], v[72:75], v[228:231]\n\t"
    "s_add_u32 m0, s36, 16384\n\t"
    "s_add_u32 s85, s6, 0x200\n\t"
    "buffer_load_dwordx4 v4, s[20:23], s85 offen lds\n\t"
    "v_mfma_f32_16x16x32_bf16 v[236:239], v[28:31], v[76:79], v[236:239]\n\t"
    "ds_read_b64_tr_b16 v[28:29], v1 offset:24576\n\t"
    "v_exp_f32 v13, v13\n\t"
    "v_add_f32 v12, 1.0, v12\n\t"
    "v_add_f32 v13, 1.0, v13\n\t"
    "s_setprio 2\n\t"
    "s_waitcnt lgkmcnt(5)\n\t"
    "v_mfma_f32_16x16x32_bf16 v[224:227], v[32:35], v[80:83], v[224:227]\n\t"
    "ds_read_b64_tr_b16 v[30:31], v1 offset:25088\n\t"
    "v_mfma_f32_16x16x32_bf16 v[232:235], v[32:35], v[84:87], v[232:235]\n\t"
    "ds_read_b64_tr_b16 v[32:33], v0 offset:28672\n\t"
    "v_rcp_f32 v12, v12\n\t"
    "v_rcp_f32 v13, v13\n\t"
    "s_waitcnt lgkmcnt(5)\n\t"
    "v_mfma_f32_16x16x32_bf16 v[228:231], v[36:39], v[80:83], v[228:231]\n\t"
    "ds_read_b64_tr_b16 v[34:35], v0 offset:29184\n\t"
    "v_mfma_f32_16x16x32_bf16 v[236:239], v[36:39], v[84:87], v[236:239]\n\t"
    "ds_read_b64_tr_b16 v[36:37], v1 offset:28672\n\t"
    "v_mul_f32 v12, v200, v12\n\t"
    "v_mul_f32 v13, v201, v13\n\t"
    "v_cvt_pk_bf16_f32 v200, v12, v13\n\t"
    "s_setprio 1\n\t"
    "s_waitcnt lgkmcnt(5)\n\t"
    "v_mfma_f32_16x16x32_bf16 v[224:227], v[24:27], v[88:91], v[224:227]\n\t"
    "ds_read_b64_tr_b16 v[38:39], v1 offset:29184\n\t"
    "v_mfma_f32_16x16x32_bf16 v[232:235], v[24:27], v[92:95], v[232:235]\n\t"
    "ds_read_b64_tr_b16 v[24:25], v0 offset:32768\n\t"
    "ds_read_b64_tr_b16 v[26:27], v0 offset:33280\n\t"
    "v_mul_f32 v12, 0xbfb8aa3b, v202\n\t"
    "v_mul_f32 v13, 0xbfb8aa3b, v203\n\t"
    "v_exp_f32 v12, v12\n\t"
    "s_waitcnt lgkmcnt(6)\n\t"
    "v_mfma_f32_16x16x32_bf16 v[228:231], v[28:31], v[88:91], v[228:231]\n\t"
    "s_add_u32 m0, s36, 24576\n\t"
    "s_add_u32 s85, s7, 0x200\n\t"
    "buffer_load_dwordx4 v4, s[20:23], s85 offen lds\n\t"
    "v_mfma_f32_16x16x32_bf16 v[236:239], v[28:31], v[92:95], v[236:239]\n\t"
    "ds_read_b64_tr_b16 v[28:29], v1 offset:32768\n\t"
    "v_exp_f32 v13, v13\n\t"
    "v_add_f32 v12, 1.0, v12\n\t"
    "v_add_f32 v13, 1.0, v13\n\t"
    "s_setprio 0\n\t"
    "s_waitcnt lgkmcnt(5)\n\t"
    "v_mfma_f32_16x16x32_bf16 v[224:227], v[32:35], v[96:99], v[224:227]\n\t"
    "ds_read_b64_tr_b16 v[30:31], v1 offset:33280\n\t"
    "v_mfma_f32_16x16x32_bf16 v[232:235], v[32:35], v[100:103], v[232:235]\n\t"
    "v_rcp_f32 v12, v12\n\t"
    "v_rcp_f32 v13, v13\n\t"
    "ds_read_b64_tr_b16 v[32:33], v0 offset:36864\n\t"
    "s_waitcnt lgkmcnt(5)\n\t"
    "v_mfma_f32_16x16x32_bf16 v[228:231], v[36:39], v[96:99], v[228:231]\n\t"
    "ds_read_b64_tr_b16 v[34:35], v0 offset:37376\n\t"
    "v_mfma_f32_16x16x32_bf16 v[236:239], v[36:39], v[100:103], v[236:239]\n\t"
    "ds_read_b64_tr_b16 v[36:37], v1 offset:36864\n\t"
    "v_mul_f32 v12, v202, v12\n\t"
    "v_mul_f32 v13, v203, v13\n\t"
    "v_cvt_pk_bf16_f32 v201, v12, v13\n\t"
    "s_waitcnt vmcnt(12)\n\t"
    "s_barrier\n\t"
    "s_setprio 3\n\t"
    "s_waitcnt lgkmcnt(5)\n\t"
    "v_mfma_f32_16x16x32_bf16 v[224:227], v[24:27], v[104:107], v[224:227]\n\t"
    "ds_read_b64_tr_b16 v[38:39], v1 offset:37376\n\t"
    "v_mfma_f32_16x16x32_bf16 v[232:235], v[24:27], v[108:111], v[232:235]\n\t"
    "v_mul_f32 v12, 0xbfb8aa3b, v204\n\t"
    "v_mul_f32 v13, 0xbfb8aa3b, v205\n\t"
    "v_exp_f32 v12, v12\n\t"
    "ds_read_b64_tr_b16 v[24:25], v0 offset:40960\n\t"
    "ds_read_b64_tr_b16 v[26:27], v0 offset:41472\n\t"
    "s_waitcnt lgkmcnt(6)\n\t"
    "v_mfma_f32_16x16x32_bf16 v[228:231], v[28:31], v[104:107], v[228:231]\n\t"
    "s_add_u32 m0, s36, 32768\n\t"
    "s_add_u32 s85, s8, 0x200\n\t"
    "buffer_load_dwordx4 v4, s[20:23], s85 offen lds\n\t"
    "v_mfma_f32_16x16x32_bf16 v[236:239], v[28:31], v[108:111], v[236:239]\n\t"
    "ds_read_b64_tr_b16 v[28:29], v1 offset:40960\n\t"
    "ds_read_b64_tr_b16 v[30:31], v1 offset:41472\n\t"
    "v_exp_f32 v13, v13\n\t"
    "v_add_f32 v12, 1.0, v12\n\t"
    "v_add_f32 v13, 1.0, v13\n\t"
    "s_waitcnt lgkmcnt(6)\n\t"
    "v_mfma_f32_16x16x32_bf16 v[224:227], v[32:35], v[112:115], v[224:227]\n\t"
    "s_add_u32 m0, s36, 114688\n\t"
    "s_add_u32 s85, s39, 0x0\n\t"
    "buffer_load_dwordx4 v5, s[24:27], s85 offen lds\n\t"
    "v_mfma_f32_16x16x32_bf16 v[232:235], v[32:35], v[116:119], v[232:235]\n\t"
    "ds_read_b64_tr_b16 v[32:33], v0 offset:45056\n\t"
    "ds_read_b64_tr_b16 v[34:35], v0 offset:45568\n\t"
    "v_rcp_f32 v12, v12\n\t"
    "v_rcp_f32 v13, v13\n\t"
    "s_waitcnt lgkmcnt(6)\n\t"
    "v_mfma_f32_16x16x32_bf16 v[228:231], v[36:39], v[112:115], v[228:231]\n\t"
    "v_mfma_f32_16x16x32_bf16 v[236:239], v[36:39], v[116:119], v[236:239]\n\t"
    "ds_read_b64_tr_b16 v[36:37], v1 offset:45056\n\t"
    "ds_read_b64_tr_b16 v[38:39], v1 offset:45568\n\t"
    "v_mul_f32 v12, v204, v12\n\t"
    "v_mul_f32 v13, v205, v13\n\t"
    "v_cvt_pk_bf16_f32 v202, v12, v13\n\t"
    "s_waitcnt lgkmcnt(6)\n\t"
    "v_mfma_f32_16x16x32_bf16 v[224:227], v[24:27], v[120:123], v[224:227]\n\t"
    "v_mfma_f32_16x16x32_bf16 v[232:235], v[24:27], v[124:127], v[232:235]\n\t"
    "ds_read_b64_tr_b16 v[24:25], v0 offset:49152\n\t"
    "ds_read_b64_tr_b16 v[26:27], v0 offset:49664\n\t"
    "v_mul_f32 v12, 0xbfb8aa3b, v206\n\t"
    "v_mul_f32 v13, 0xbfb8aa3b, v207\n\t"
    "v_exp_f32 v12, v12\n\t"
    "s_waitcnt lgkmcnt(6)\n\t"
    "v_mfma_f32_16x16x32_bf16 v[228:231], v[28:31], v[120:123], v[228:231]\n\t"
    "s_add_u32 m0, s36, 122880\n\t"
    "s_add_u32 s85, s39, 0x0\n\t"
    "s_add_u32 s85, s85, s100\n\t"
    "buffer_load_dwordx4 v5, s[24:27], s85 offen lds\n\t"
    "v_mfma_f32_16x16x32_bf16 v[236:239], v[28:31], v[124:127], v[236:239]\n\t"
    "ds_read_b64_tr_b16 v[28:29], v1 offset:49152\n\t"
    "ds_read_b64_tr_b16 v[30:31], v1 offset:49664\n\t"
    "v_exp_f32 v13, v13\n\t"
    "v_add_f32 v12, 1.0, v12\n\t"
    "v_add_f32 v13, 1.0, v13\n\t"
    "s_setprio 2\n\t"
    "s_waitcnt lgkmcnt(6)\n\t"
    "v_mfma_f32_16x16x32_bf16 v[224:227], v[32:35], v[128:131], v[224:227]\n\t"
    "s_add_u32 m0, s36, 131072\n\t"
    "s_add_u32 s85, s39, 0x12000\n\t"
    "buffer_load_dwordx4 v5, s[24:27], s85 offen lds\n\t"
    "v_mfma_f32_16x16x32_bf16 v[232:235], v[32:35], v[132:135], v[232:235]\n\t"
    "ds_read_b64_tr_b16 v[32:33], v0 offset:53248\n\t"
    "v_rcp_f32 v12, v12\n\t"
    "v_rcp_f32 v13, v13\n\t"
    "s_waitcnt lgkmcnt(5)\n\t"
    "v_mfma_f32_16x16x32_bf16 v[228:231], v[36:39], v[128:131], v[228:231]\n\t"
    "ds_read_b64_tr_b16 v[34:35], v0 offset:53760\n\t"
    "v_mfma_f32_16x16x32_bf16 v[236:239], v[36:39], v[132:135], v[236:239]\n\t"
    "ds_read_b64_tr_b16 v[36:37], v1 offset:53248\n\t"
    "ds_read_b64_tr_b16 v[38:39], v1 offset:53760\n\t"
    "v_mul_f32 v12, v206, v12\n\t"
    "v_mul_f32 v13, v207, v13\n\t"
    "v_cvt_pk_bf16_f32 v203, v12, v13\n\t"
    "s_setprio 1\n\t"
    "s_waitcnt lgkmcnt(6)\n\t"
    "v_mfma_f32_16x16x32_bf16 v[224:227], v[24:27], v[136:139], v[224:227]\n\t"
    "s_add_u32 s85, s47, 0x9000\n\t"
    "buffer_store_dwordx4 v[200:203], v6, s[28:31], s85 offen sc1\n\t"
    "v_add3_u32 v20, v200, v201, v202\n\t"
    "v_add_u32 v20, v20, v203\n\t"
    "v_mfma_f32_16x16x32_bf16 v[232:235], v[24:27], v[140:143], v[232:235]\n\t"
    "ds_read_b64_tr_b16 v[24:25], v0 offset:57344\n\t"
    "v_mul_f32 v12, 0xbfb8aa3b, v208\n\t"
    "v_mul_f32 v13, 0xbfb8aa3b, v209\n\t"
    "v_exp_f32 v12, v12\n\t"
    "s_waitcnt lgkmcnt(5)\n\t"
    "v_mfma_f32_16x16x32_bf16 v[228:231], v[28:31], v[136:139], v[228:231]\n\t"
    "ds_read_b64_tr_b16 v[26:27], v0 offset:57856\n\t"
    "v_mfma_f32_16x16x32_bf16 v[236:239], v[28:31], v[140:143], v[236:239]\n\t"
    "ds_read_b64_tr_b16 v[28:29], v1 offset:57344\n\t"
    "v_exp_f32 v13, v13\n\t"
    "v_add_f32 v12, 1.0, v12\n\t"
    "v_add_f32 v13, 1.0, v13\n\t"
    "s_setprio 0\n\t"
    "s_waitcnt lgkmcnt(5)\n\t"
    "v_mfma_f32_16x16x32_bf16 v[224:227], v[32:35], v[144:147], v[224:227]\n\t"
    "ds_read_b64_tr_b16 v[30:31], v1 offset:57856\n\t"
    "v_mfma_f32_16x16x32_bf16 v[232:235], v[32:35], v[148:151], v[232:235]\n\t"
    "ds_read_b64_tr_b16 v[32:33], v0 offset:61440\n\t"
    "v_rcp_f32 v12, v12\n\t"
    "v_rcp_f32 v13, v13\n\t"
    "s_waitcnt lgkmcnt(5)\n\t"
    "v_mfma_f32_16x16x32_bf16 v[228:231], v[36:39], v[144:147], v[228:231]\n\t"
    "ds_read_b64_tr_b16 v[34:35], v0 offset:61952\n\t"
    "v_mfma_f32_16x16x32_bf16 v[236:239], v[36:39], v[148:151], v[236:239]\n\t"
    "ds_read_b64_tr_b16 v[36:37], v1 offset:61440\n\t"
    "v_mul_f32 v12, v208, v12\n\t"
    "v_mul_f32 v13, v209, v13\n\t"
    "v_cvt_pk_bf16_f32 v204, v12, v13\n\t"
    "s_waitcnt vmcnt(13)\n\t"
    "s_barrier\n\t"
    "s_setprio 3\n\t"
    "s_waitcnt lgkmcnt(5)\n\t"
    "v_mfma_f32_16x16x32_bf16 v[224:227], v[24:27], v[152:155], v[224:227]\n\t"
    "ds_read_b64_tr_b16 v[38:39], v1 offset:61952\n\t"
    "v_mfma_f32_16x16x32_bf16 v[232:235], v[24:27], v[156:159], v[232:235]\n\t"
    "v_add_u32 v0, 0x10000, v0\n\t"
    "ds_read_b64_tr_b16 v[24:25], v0 offset:0\n\t"
    "v_mul_f32 v12, 0xbfb8aa3b, v210\n\t"
    "v_mul_f32 v13, 0xbfb8aa3b, v211\n\t"
    "v_exp_f32 v12, v12\n\t"
    "s_waitcnt lgkmcnt(5)\n\t"
    "v_mfma_f32_16x16x32_bf16 v[228:231], v[28:31], v[152:155], v[228:231]\n\t"
    "ds_read_b64_tr_b16 v[26:27], v0 offset:512\n\t"
    "s_add_u32 m0, s36, 40960\n\t"
    "s_add_u32 s85, s39, 0x12000\n\t"
    "s_add_u32 s85, s85, s100\n\t"
    "buffer_load_dwordx4 v5, s[24:27], s85 offen lds\n\t"
    "v_mfma_f32_16x16x32_bf16 v[236:239], v[28:31], v[156:159], v[236:239]\n\t"
    "v_add_u32 v1, 0x10000, v1\n\t"
    "ds_read_b64_tr_b16 v[28:29], v1 offset:0\n\t"
    "v_exp_f32 v13, v13\n\t"
    "v_add_f32 v12, 1.0, v12\n\t"
    "v_add_f32 v13, 1.0, v13\n\t"
    "s_waitcnt lgkmcnt(5)\n\t"
    "v_mfma_f32_16x16x32_bf16 v[224:227], v[32:35], v[160:163], v[224:227]\n\t"
    "ds_read_b64_tr_b16 v[30:31], v1 offset:512\n\t"
    "s_add_u32 m0, s36, 49152\n\t"
    "s_add_u32 s85, s39, 0x24000\n\t"
    "buffer_load_dwordx4 v5, s[24:27], s85 offen lds\n\t"
    "v_mfma_f32_16x16x32_bf16 v[232:235], v[32:35], v[164:167], v[232:235]\n\t"
    "ds_read_b64_tr_b16 v[32:33], v0 offset:4096\n\t"
    "v_rcp_f32 v12, v12\n\t"
    "v_rcp_f32 v13, v13\n\t"
    "s_waitcnt lgkmcnt(5)\n\t"
    "v_mfma_f32_16x16x32_bf16 v[228:231], v[36:39], v[160:163], v[228:231]\n\t"
    "ds_read_b64_tr_b16 v[34:35], v0 offset:4608\n\t"
    "v_mfma_f32_16x16x32_bf16 v[236:239], v[36:39], v[164:167], v[236:239]\n\t"
    "ds_read_b64_tr_b16 v[36:37], v1 offset:4096\n\t"
    "v_mul_f32 v12, v210, v12\n\t"
    "v_mul_f32 v13, v211, v13\n\t"
    "v_cvt_pk_bf16_f32 v205, v12, v13\n\t"
    "s_waitcnt lgkmcnt(5)\n\t"
    "v_mfma_f32_16x16x32_bf16 v[224:227], v[24:27], v[168:171], v[224:227]\n\t"
    "ds_read_b64_tr_b16 v[38:39], v1 offset:4608\n\t"
    "v_mfma_f32_16x16x32_bf16 v[232:235], v[24:27], v[172:175], v[232:235]\n\t"
    "ds_read_b64_tr_b16 v[24:25], v0 offset:8192\n\t"
    "v_mul_f32 v12, 0xbfb8aa3b, v212\n\t"
    "v_mul_f32 v13, 0xbfb8aa3b, v213\n\t"
    "v_exp_f32 v12, v12\n\t"
    "s_waitcnt lgkmcnt(5)\n\t"
    "v_mfma_f32_16x16x32_bf16 v[228:231], v[28:31], v[168:171], v[228:231]\n\t"
    "ds_read_b64_tr_b16 v[26:27], v0 offset:8704\n\t"
    "s_add_u32 m0, s36, 57344\n\t"
    "s_add_u32 s85, s39, 0x36000\n\t"
    "buffer_load_dwordx4 v5, s[24:27], s85 offen lds\n\t"
    "v_mfma_f32_16x16x32_bf16 v[236:239], v[28:31], v[172:175], v[236:239]\n\t"
    "ds_read_b64_tr_b16 v[28:29], v1 offset:8192\n\t"
    "v_exp_f32 v13, v13\n\t"
    "v_add_f32 v12, 1.0, v12\n\t"
    "v_add_f32 v13, 1.0, v13\n\t"
    "s_setprio 2\n\t"
    "s_waitcnt lgkmcnt(5)\n\t"
    "v_mfma_f32_16x16x32_bf16 v[224:227], v[32:35], v[176:179], v[224:227]\n\t"
    "ds_read_b64_tr_b16 v[30:31], v1 offset:8704\n\t"
    "s_add_u32 m0, s36, 65536\n\t"
    "s_mov_b32 s85, s37\n\t"
    "buffer_load_dwordx4 v5, s[24:27], s85 offen lds\n\t"
    "v_mfma_f32_16x16x32_bf16 v[232:235], v[32:35], v[180:183], v[232:235]\n\t"
    "ds_read_b64_tr_b16 v[32:33], v0 offset:12288\n\t"
    "v_rcp_f32 v12, v12\n\t"
    "v_rcp_f32 v13, v13\n\t"
    "s_waitcnt lgkmcnt(5)\n\t"
    "v_mfma_f32_16x16x32_bf16 v[228:231], v[36:39], v[176:179], v[228:231]\n\t"
    "ds_read_b64_tr_b16 v[34:35], v0 offset:12800\n\t"
    "v_mfma_f32_16x16x32_bf16 v[236:239], v[36:39], v[180:183], v[236:239]\n\t"
    "ds_read_b64_tr_b16 v[36:37], v1 offset:12288\n\t"
    "v_mul_f32 v12, v212, v12\n\t"
    "v_mul_f32 v13, v213, v13\n\t"
    "v_cvt_pk_bf16_f32 v206, v12, v13\n\t"
    "s_setprio 1\n\t"
    "s_waitcnt lgkmcnt(5)\n\t"
    "v_mfma_f32_16x16x32_bf16 v[240:243], v[24:27], v[40:43], 0\n\t"
    "ds_read_b64_tr_b16 v[38:39], v1 offset:12800\n\t"
    "v_mfma_f32_16x16x32_bf16 v[248:251], v[24:27], v[44:47], 0\n\t"
    "ds_read_b64_tr_b16 v[24:25], v0 offset:40960\n\t"
    "v_mul_f32 v12, 0xbfb8aa3b, v214\n\t"
    "v_mul_f32 v13, 0xbfb8aa3b, v215\n\t"
    "v_exp_f32 v12, v12\n\t"
    "s_waitcnt lgkmcnt(5)\n\t"
    "v_mfma_f32_16x16x32_bf16 v[244:247], v[28:31], v[40:43], 0\n\t"
    "ds_read_b64_tr_b16 v[26:27], v0 offset:41472\n\t"
    "s_add_u32 m0, s36, 73728\n\t"
    "s_mov_b32 s85, s38\n\t"
    "buffer_load_dwordx4 v5, s[24:27], s85 offen lds\n\t"
    "v_mfma_f32_16x16x32_bf16 v[252:255], v[28:31], v[44:47], 0\n\t"
    "ds_read_b64_tr_b16 v[28:29], v1 offset:40960\n\t"
    "v_exp_f32 v13, v13\n\t"
    "v_add_f32 v12, 1.0, v12\n\t"
    "v_add_f32 v13, 1.0, v13\n\t"
    "s_setprio 0\n\t"
    "s_waitcnt lgkmcnt(5)\n\t"
    "v_mfma_f32_16x16x32_bf16 v[240:243], v[32:35], v[48:51], v[240:243]\n\t"
    "ds_read_b64_tr_b16 v[30:31], v1 offset:41472\n\t"
    "s_add_u32 m0, s36, 81920\n\t"
    "s_mov_b32 s85, s96\n\t"
    "buffer_load_dwordx4 v5, s[24:27], s85 offen lds\n\t"
    "v_mfma_f32_16x16x32_bf16 v[248:251], v[32:35], v[52:55], v[248:251]\n\t"
    "ds_read_b64_tr_b16 v[32:33], v0 offset:45056\n\t"
    "v_rcp_f32 v12, v12\n\t"
    "v_rcp_f32 v13, v13\n\t"
    "s_waitcnt lgkmcnt(5)\n\t"
    "v_mfma_f32_16x16x32_bf16 v[244:247], v[36:39], v[48:51], v[244:247]\n\t"
    "ds_read_b64_tr_b16 v[34:35], v0 offset:45568\n\t"
    "v_mfma_f32_16x16x32_bf16 v[252:255], v[36:39], v[52:55], v[252:255]\n\t"
    "ds_read_b64_tr_b16 v[36:37], v1 offset:45056\n\t"
    "v_mul_f32 v12, v214, v12\n\t"
    "v_mul_f32 v13, v215, v13\n\t"
    "v_cvt_pk_bf16_f32 v207, v12, v13\n\t"
    "s_waitcnt vmcnt(16)\n\t"
    "s_barrier\n\t"
    "s_setprio 3\n\t"
    "s_waitcnt lgkmcnt(5)\n\t"
    "v_mfma_f32_16x16x32_bf16 v[240:243], v[24:27], v[56:59], v[240:243]\n\t"
    "ds_read_b64_tr_b16 v[38:39], v1 offset:45568\n\t"
    "s_add_u32 s85, s47, 0x9000\n\t"
    "buffer_store_dwordx4 v[204:207], v6, s[28:31], s85 offen offset:1024 sc1\n\t"
    "v_add3_u32 v20, v20, v204, v205\n\t"
    "v_add3_u32 v20, v20, v206, v207\n\t"
    "v_sub_u32 v20, s55, v20\n\t"
    "buffer_store_dword v20, v7, s[28:31], s85 offen offset:2048 sc1\n\t"
    "v_mfma_f32_16x16x32_bf16 v[248:251], v[24:27], v[60:63], v[248:251]\n\t"
    "ds_read_b64_tr_b16 v[24:25], v0 offset:49152\n\t"
    "ds_read_b64_tr_b16 v[26:27], v0 offset:49664\n\t"
    "v_mul_f32 v12, 0xbfb8aa3b, v224\n\t"
    "v_mul_f32 v13, 0xbfb8aa3b, v225\n\t"
    "v_exp_f32 v12, v12\n\t"
    "s_waitcnt lgkmcnt(6)\n\t"
    "v_mfma_f32_16x16x32_bf16 v[244:247], v[28:31], v[56:59], v[244:247]\n\t"
    "v_mfma_f32_16x16x32_bf16 v[252:255], v[28:31], v[60:63], v[252:255]\n\t"
    "ds_read_b64_tr_b16 v[28:29], v1 offset:49152\n\t"
    "v_exp_f32 v13, v13\n\t"
    "v_add_f32 v12, 1.0, v12\n\t"
    "v_add_f32 v13, 1.0, v13\n\t"
    "s_waitcnt lgkmcnt(5)\n\t"
    "v_mfma_f32_16x16x32_bf16 v[240:243], v[32:35], v[64:67], v[240:243]\n\t"
    "ds_read_b64_tr_b16 v[30:31], v1 offset:49664\n\t"
    "v_mfma_f32_16x16x32_bf16 v[248:251], v[32:35], v[68:71], v[248:251]\n\t"
    "ds_read_b64_tr_b16 v[32:33], v0 offset:53248\n\t"
    "ds_read_b64_tr_b16 v[34:35], v0 offset:53760\n\t"
    "v_rcp_f32 v12, v12\n\t"
    "v_rcp_f32 v13, v13\n\t"
    "s_waitcnt lgkmcnt(6)\n\t"
    "v_mfma_f32_16x16x32_bf16 v[244:247], v[36:39], v[64:67], v[244:247]\n\t"
    "v_mfma_f32_16x16x32_bf16 v[252:255], v[36:39], v[68:71], v[252:255]\n\t"
    "ds_read_b64_tr_b16 v[36:37], v1 offset:53248\n\t"
    "v_mul_f32 v12, v224, v12\n\t"
    "v_mul_f32 v13, v225, v13\n\t"
    "v_cvt_pk_bf16_f32 v208, v12, v13\n\t"
    "s_waitcnt lgkmcnt(5)\n\t"
    "v_mfma_f32_16x16x32_bf16 v[240:243], v[24:27], v[72:75], v[240:243]\n\t"
    "ds_read_b64_tr_b16 v[38:39], v1 offset:53760\n\t"
    "v_mfma_f32_16x16x32_bf16 v[248:251], v[24:27], v[76:79], v[248:251]\n\t"
    "ds_read_b64_tr_b16 v[24:25], v0 offset:57344\n\t"
    "ds_read_b64_tr_b16 v[26:27], v0 offset:57856\n\t"
    "v_mul_f32 v12, 0xbfb8aa3b, v226\n\t"
    "v_mul_f32 v13, 0xbfb8aa3b, v227\n\t"
    "v_exp_f32 v12, v12\n\t"
    "s_waitcnt lgkmcnt(6)\n\t"
    "v_mfma_f32_16x16x32_bf16 v[244:247], v[28:31], v[72:75], v[244:247]\n\t"
    "s_add_u32 m0, s36, 90112\n\t"
    "s_add_u32 s85, s39, 0x48000\n\t"
    "buffer_load_dwordx4 v5, s[24:27], s85 offen lds\n\t"
    "v_mfma_f32_16x16x32_bf16 v[252:255], v[28:31], v[76:79], v[252:255]\n\t"
    "ds_read_b64_tr_b16 v[28:29], v1 offset:57344\n\t"
    "v_exp_f32 v13, v13\n\t"
    "v_add_f32 v12, 1.0, v12\n\t"
    "v_add_f32 v13, 1.0, v13\n\t"
    "s_setprio 2\n\t"
    "s_waitcnt lgkmcnt(5)\n\t"
    "v_mfma_f32_16x16x32_bf16 v[240:243], v[32:35], v[80:83], v[240:243]\n\t"
    "ds_read_b64_tr_b16 v[30:31], v1 offset:57856\n\t"
    "v_mfma_f32_16x16x32_bf16 v[248:251], v[32:35], v[84:87], v[248:251]\n\t"
    "ds_read_b64_tr_b16 v[32:33], v0 offset:61440\n\t"
    "v_rcp_f32 v12, v12\n\t"
    "v_rcp_f32 v13, v13\n\t"
    "s_mov_b32 s85, s9\n\t"
    "buffer_load_dwordx4 v[40:43], v6, s[28:31], s85 offen sc1\n\t"
    "s_waitcnt lgkmcnt(5)\n\t"
    "v_mfma_f32_16x16x32_bf16 v[244:247], v[36:39], v[80:83], v[244:247]\n\t"
    "ds_read_b64_tr_b16 v[34:35], v0 offset:61952\n\t"
    "v_mfma_f32_16x16x32_bf16 v[252:255], v[36:39], v[84:87], v[252:255]\n\t"
    "ds_read_b64_tr_b16 v[36:37], v1 offset:61440\n\t"
    "ds_read_b64_tr_b16 v[38:39], v1 offset:61952\n\t"
    "v_mul_f32 v12, v226, v12\n\t"
    "v_mul_f32 v13, v227, v13\n\t"
    "v_cvt_pk_bf16_f32 v209, v12, v13\n\t"
    "s_mov_b32 s85, s9\n\t"
    "buffer_load_dwordx4 v[44:47], v6, s[28:31], s85 offen offset:1024 sc1\n\t"
    "LIPA2x0_%=:\n\t"
    "s_cbranch_execz LIPRA_%=\n\t"
    "s_setprio 1\n\t"
    "s_waitcnt lgkmcnt(6)\n\t"
    "v_mfma_f32_16x16x32_bf16 v[240:243], v[24:27], v[88:91], v[240:243]\n\t"
    "s_mov_b32 s85, s9\n\t"
    "buffer_load_dword v16, v7, s[28:31], s85 offen offset:2048 sc1\n\t"
    "v_mfma_f32_16x16x32_bf16 v[248:251], v[24:27], v[92:95], v[248:251]\n\t"
    "v_add_u32 v0, 0xfffe8000, v0\n\t"
    "ds_read_b64_tr_b16 v[24:25], v0 offset:0\n\t"
    "v_mul_f32 v12, 0xbfb8aa3b, v228\n\t"
    "v_mul_f32 v13, 0xbfb8aa3b, v229\n\t"
    "v_exp_f32 v12, v12\n\t"
    "s_waitcnt lgkmcnt(5)\n\t"
    "v_mfma_f32_16x16x32_bf16 v[244:247], v[28:31], v[88:91], v[244:247]\n\t"
    "ds_read_b64_tr_b16 v[26:27], v0 offset:512\n\t"
    "v_mfma_f32_16x16x32_bf16 v[252:255], v[28:31], v[92:95], v[252:255]\n\t"
    "v_add_u32 v1, 0xfffe8000, v1\n\t"
    "ds_read_b64_tr_b16 v[28:29], v1 offset:0\n\t"
    "ds_read_b64_tr_b16 v[30:31], v1 offset:512\n\t"
    "v_exp_f32 v13, v13\n\t"
    "v_add_f32 v12, 1.0, v12\n\t"
    "v_add_f32 v13, 1.0, v13\n\t"
    "s_setprio 0\n\t"
    "s_waitcnt lgkmcnt(6)\n\t"
    "v_mfma_f32_16x16x32_bf16 v[240:243], v[32:35], v[96:99], v[240:243]\n\t"
    "v_mfma_f32_16x16x32_bf16 v[248:251], v[32:35], v[100:103], v[248:251]\n\t"
    "ds_read_b64_tr_b16 v[32:33], v0 offset:4096\n\t"
    "ds_read_b64_tr_b16 v[34:35], v0 offset:4608\n\t"
    "v_rcp_f32 v12, v12\n\t"
    "v_rcp_f32 v13, v13\n\t"
    "s_waitcnt lgkmcnt(6)\n\t"
    "v_mfma_f32_16x16x32_bf16 v[244:247], v[36:39], v[96:99], v[244:247]\n\t"
    "s_mov_b32 s85, s10\n\t"
    "buffer_load_dwordx4 v[48:51], v6, s[28:31], s85 offen sc1\n\t"
    "v_mfma_f32_16x16x32_bf16 v[252:255], v[36:39], v[100:103], v[252:255]\n\t"
    "ds_read_b64_tr_b16 v[36:37], v1 offset:4096\n\t"
    "v_mul_f32 v12, v228, v12\n\t"
    "v_mul_f32 v13, v229, v13\n\t"
    "v_cvt_pk_bf16_f32 v210, v12, v13\n\t"
    "s_waitcnt vmcnt(18)\n\t"
    "s_barrier\n\t"
    "s_setprio 3\n\t"
    "s_waitcnt lgkmcnt(5)\n\t"
    "v_mfma_f32_16x16x32_bf16 v[240:243], v[24:27], v[104:107], v[240:243]\n\t"
    "ds_read_b64_tr_b16 v[38:39], v1 offset:4608\n\t"
    "s_mov_b32 s85, s10\n\t"
    "buffer_load_dword v17, v7, s[28:31], s85 offen offset:2048 sc1\n\t"
    "v_mfma_f32_16x16x32_bf16 v[248:251], v[24:27], v[108:111], v[248:251]\n\t"
    "ds_read_b64_tr_b16 v[24:25], v0 offset:8192\n\t"
    "v_mul_f32 v12, 0xbfb8aa3b, v230\n\t"
    "v_mul_f32 v13, 0xbfb8aa3b, v231\n\t"
    "v_exp_f32 v12, v12\n\t"
    "s_waitcnt lgkmcnt(5)\n\t"
    "v_mfma_f32_16x16x32_bf16 v[244:247], v[28:31], v[104:107], v[244:247]\n\t"
    "ds_read_b64_tr_b16 v[26:27], v0 offset:8704\n\t"
    "s_add_u32 m0, s36, 98304\n\t"
    "s_add_u32 s85, s40, 0x0\n\t"
    "buffer_load_dwordx4 v5, s[24:27], s85 offen lds\n\t"
    "v_mfma_f32_16x16x32_bf16 v[252:255], v[28:31], v[108:111], v[252:255]\n\t"
    "ds_read_b64_tr_b16 v[28:29], v1 offset:8192\n\t"
    "v_exp_f32 v13, v13\n\t"
    "v_add_f32 v12, 1.0, v12\n\t"
    "v_add_f32 v13, 1.0, v13\n\t"
    "s_waitcnt lgkmcnt(5)\n\t"
    "v_mfma_f32_16x16x32_bf16 v[240:243], v[32:35], v[112:115], v[240:243]\n\t"
    "ds_read_b64_tr_b16 v[30:31], v1 offset:8704\n\t"
    "v_mfma_f32_16x16x32_bf16 v[248:251], v[32:35], v[116:119], v[248:251]\n\t"
    "ds_read_b64_tr_b16 v[32:33], v0 offset:12288\n\t"
    "v_rcp_f32 v12, v12\n\t"
    "v_rcp_f32 v13, v13\n\t"
    "s_waitcnt lgkmcnt(5)\n\t"
    "v_mfma_f32_16x16x32_bf16 v[244:247], v[36:39], v[112:115], v[244:247]\n\t"
    "ds_read_b64_tr_b16 v[34:35], v0 offset:12800\n\t"
    "s_mov_b32 s85, s10\n\t"
    "buffer_load_dwordx4 v[52:55], v6, s[28:31], s85 offen offset:1024 sc1\n\t"
    "v_mfma_f32_16x16x32_bf16 v[252:255], v[36:39], v[116:119], v[252:255]\n\t"
    "ds_read_b64_tr_b16 v[36:37], v1 offset:12288\n\t"
    "v_mul_f32 v12, v230, v12\n\t"
    "v_mul_f32 v13, v231, v13\n\t"
    "v_cvt_pk_bf16_f32 v211, v12, v13\n\t"
    "s_waitcnt lgkmcnt(5)\n\t"
    "v_mfma_f32_16x16x32_bf16 v[240:243], v[24:27], v[120:123], v[240:243]\n\t"
    "ds_read_b64_tr_b16 v[38:39], v1 offset:12800\n\t"
    "s_add_u32 s85, s47, 0xd800\n\t"
    "buffer_store_dwordx4 v[208:211], v6, s[28:31], s85 offen sc1\n\t"
    "v_add3_u32 v20, v208, v209, v210\n\t"
    "v_add_u32 v20, v20, v211\n\t"
    "v_mfma_f32_16x16x32_bf16 v[248:251], v[24:27], v[124:127], v[248:251]\n\t"
    "ds_read_b64_tr_b16 v[24:25], v0 offset:16384\n\t"
    "v_mul_f32 v12, 0xbfb8aa3b, v232\n\t"
    "v_mul_f32 v13, 0xbfb8aa3b, v233\n\t"
    "v_exp_f32 v12, v12\n\t"
    "s_waitcnt lgkmcnt(5)\n\t"
    "v_mfma_f32_16x16x32_bf16 v[244:247], v[28:31], v[120:123], v[244:247]\n\t"
    "ds_read_b64_tr_b16 v[26:27], v0 offset:16896\n\t"
    "v_mfma_f32_16x16x32_bf16 v[252:255], v[28:31], v[124:127], v[252:255]\n\t"
    "ds_read_b64_tr_b16 v[28:29], v1 offset:16384\n\t"
    "v_exp_f32 v13, v13\n\t"
    "v_add_f32 v12, 1.0, v12\n\t"
    "v_add_f32 v13, 1.0, v13\n\t"
    "s_setprio 2\n\t"
    "s_waitcnt lgkmcnt(5)\n\t"
    "v_mfma_f32_16x16x32_bf16 v[240:243], v[32:35], v[128:131], v[240:243]\n\t"
    "ds_read_b64_tr_b16 v[30:31], v1 offset:16896\n\t"
    "v_mfma_f32_16x16x32_bf16 v[248:251], v[32:35], v[132:135], v[248:251]\n\t"
    "ds_read_b64_tr_b16 v[32:33], v0 offset:20480\n\t"
    "ds_read_b64_tr_b16 v[34:35], v0 offset:20992\n\t"
    "v_rcp_f32 v12, v12\n\t"
    "v_rcp_f32 v13, v13\n\t"
    "s_mov_b32 s85, s97\n\t"
    "buffer_load_dwordx4 v[56:59], v6, s[28:31], s85 offen sc1\n\t"
    "s_waitcnt lgkmcnt(6)\n\t"
    "v_mfma_f32_16x16x32_bf16 v[244:247], v[36:39], v[128:131], v[244:247]\n\t"
    "v_mfma_f32_16x16x32_bf16 v[252:255], v[36:39], v[132:135], v[252:255]\n\t"
    "ds_read_b64_tr_b16 v[36:37], v1 offset:20480\n\t"
    "v_mul_f32 v12, v232, v12\n\t"
    "v_mul_f32 v13, v233, v13\n\t"
    "v_cvt_pk_bf16_f32 v212, v12, v13\n\t"
    "s_mov_b32 s85, s97\n\t"
    "buffer_load_dwordx4 v[60:63], v6, s[28:31], s85 offen offset:1024 sc1\n\t"
    "s_setprio 1\n\t"
    "s_waitcnt lgkmcnt(5)\n\t"
    "v_mfma_f32_16x16x32_bf16 v[240:243], v[24:27], v[136:139], v[240:243]\n\t"
    "ds_read_b64_tr_b16 v[38:39], v1 offset:20992\n\t"
    "s_mov_b32 s85, s97\n\t"
    "buffer_load_dword v18, v7, s[28:31], s85 offen offset:2048 sc1\n\t"
    "v_mfma_f32_16x16x32_bf16 v[248:251], v[24:27], v[140:143], v[248:251]\n\t"
    "ds_read_b64_tr_b16 v[24:25], v0 offset:24576\n\t"
    "v_mul_f32 v12, 0xbfb8aa3b, v234\n\t"
    "v_mul_f32 v13, 0xbfb8aa3b, v235\n\t"
    "v_exp_f32 v12, v12\n\t"
    "s_waitcnt lgkmcnt(5)\n\t"
    "v_mfma_f32_16x16x32_bf16 v[244:247], v[28:31], v[136:139], v[244:247]\n\t"
    "ds_read_b64_tr_b16 v[26:27], v0 offset:25088\n\t"
    "s_add_u32 m0, s36, 139264\n\t"
    "s_add_u32 s85, s41, 0x0\n\t"
    "buffer_load_dwordx4 v5, s[24:27], s85 offen lds\n\t"
    "v_mfma_f32_16x16x32_bf16 v[252:255], v[28:31], v[140:143], v[252:255]\n\t"
    "ds_read_b64_tr_b16 v[28:29], v1 offset:24576\n\t"
    "v_exp_f32 v13, v13\n\t"
    "v_add_f32 v12, 1.0, v12\n\t"
    "v_add_f32 v13, 1.0, v13\n\t"
    "s_setprio 0\n\t"
    "s_waitcnt lgkmcnt(5)\n\t"
    "v_mfma_f32_16x16x32_bf16 v[240:243], v[32:35], v[144:147], v[240:243]\n\t"
    "ds_read_b64_tr_b16 v[30:31], v1 offset:25088\n\t"
    "v_mfma_f32_16x16x32_bf16 v[248:251], v[32:35], v[148:151], v[248:251]\n\t"
    "ds_read_b64_tr_b16 v[32:33], v0 offset:28672\n\t"
    "v_rcp_f32 v12, v12\n\t"
    "v_rcp_f32 v13, v13\n\t"
    "s_waitcnt lgkmcnt(5)\n\t"
    "v_mfma_f32_16x16x32_bf16 v[244:247], v[36:39], v[144:147], v[244:247]\n\t"
    "ds_read_b64_tr_b16 v[34:35], v0 offset:29184\n\t"
    "s_add_u32 s85, s44, 0x0\n\t"
    "buffer_load_dwordx4 v[64:67], v6, s[28:31], s85 offen sc1\n\t"
    "v_mfma_f32_16x16x32_bf16 v[252:255], v[36:39], v[148:151], v[252:255]\n\t"
    "ds_read_b64_tr_b16 v[36:37], v1 offset:28672\n\t"
    "v_mul_f32 v12, v234, v12\n\t"
    "v_mul_f32 v13, v235, v13\n\t"
    "v_cvt_pk_bf16_f32 v213, v12, v13\n\t"
    "s_waitcnt vmcnt(24)\n\t"
    "s_barrier\n\t"
    "s_setprio 3\n\t"
    "s_waitcnt lgkmcnt(5)\n\t"
    "v_mfma_f32_16x16x32_bf16 v[240:243], v[24:27], v[152:155], v[240:243]\n\t"
    "ds_read_b64_tr_b16 v[38:39], v1 offset:29184\n\t"
    "s_add_u32 s85, s44, 0x0\n\t"
    "buffer_load_dword v19, v7, s[28:31], s85 offen offset:2048 sc1\n\t"
    "v_mfma_f32_16x16x32_bf16 v[248:251], v[24:27], v[156:159], v[248:251]\n\t"
    "ds_read_b64_tr_b16 v[24:25], v0 offset:32768\n\t"
    "ds_read_b64_tr_b16 v[26:27], v0 offset:33280\n\t"
    "v_mul_f32 v12, 0xbfb8aa3b, v236\n\t"
    "v_mul_f32 v13, 0xbfb8aa3b, v237\n\t"
    "v_exp_f32 v12, v12\n\t"
    "s_waitcnt lgkmcnt(6)\n\t"
    "v_mfma_f32_16x16x32_bf16 v[244:247], v[28:31], v[152:155], v[244:247]\n\t"
    "s_add_u32 m0, s36, 147456\n\t"
    "s_add_u32 s85, s40, 0x12000\n\t"
    "buffer_load_dwordx4 v5, s[24:27], s85 offen lds\n\t"
    "v_mfma_f32_16x16x32_bf16 v[252:255], v[28:31], v[156:159], v[252:255]\n\t"
    "ds_read_b64_tr_b16 v[28:29], v1 offset:32768\n\t"
    "v_exp_f32 v13, v13\n\t"
    "v_add_f32 v12, 1.0, v12\n\t"
    "v_add_f32 v13, 1.0, v13\n\t"
    "s_waitcnt lgkmcnt(5)\n\t"
    "v_mfma_f32_16x16x32_bf16 v[240:243], v[32:35], v[160:163], v[240:243]\n\t"
    "ds_read_b64_tr_b16 v[30:31], v1 offset:33280\n\t"
    "v_mfma_f32_16x16x32_bf16 v[248:251], v[32:35], v[164:167], v[248:251]\n\t"
    "ds_read_b64_tr_b16 v[32:33], v0 offset:36864\n\t"
    "ds_read_b64_tr_b16 v[34:35], v0 offset:37376\n\t"
    "v_rcp_f32 v12, v12\n\t"
    "v_rcp_f32 v13, v13\n\t"
    "s_waitcnt vmcnt(12)\n\t"
    "v_add3_u32 v16, v40, v41, v16\n\t"
    "v_add3_u32 v16, v42, v43, v16\n\t"
    "v_add3_u32 v16, v44, v45, v16\n\t"
    "v_add3_u32 v16, v46, v47, v16\n\t"
    "v_cmp_ne_u32 vcc, s51, v16\n\t"
    "s_cbranch_vccnz LA1_%=\n\t"
    "LA2_%=:\n\t"
    "s_setprio 2\n\t"
    "s_waitcnt lgkmcnt(6)\n\t"
    "v_mfma_f32_16x16x32_bf16 v[244:247], v[36:39], v[160:163], v[244:247]\n\t"
    "s_add_u32 s85, s44, 0x0\n\t"
    "buffer_load_dwordx4 v[68:71], v6, s[28:31], s85 offen offset:1024 sc1\n\t"
    "v_mfma_f32_16x16x32_bf16 v[252:255], v[36:39], v[164:167], v[252:255]\n\t"
    "ds_read_b64_tr_b16 v[36:37], v1 offset:36864\n\t"
    "v_mul_f32 v12, v236, v12\n\t"
    "v_mul_f32 v13, v237, v13\n\t"
    "v_cvt_pk_bf16_f32 v214, v12, v13\n\t"
    "s_waitcnt lgkmcnt(5)\n\t"
    "v_mfma_f32_16x16x32_bf16 v[240:243], v[24:27], v[168:171], v[240:243]\n\t"
    "ds_read_b64_tr_b16 v[38:39], v1 offset:37376\n\t"
    "v_mfma_f32_16x16x32_bf16 v[248:251], v[24:27], v[172:175], v[248:251]\n\t"
    "v_add_u32 v0, v0, v3\n\t"
    "v_xor_b32 v1, 32, v0\n\t"
    "v_xor_b32 v2, 64, v0\n\t"
    "v_xor_b32 v3, 96, v0\n\t"
    "v_add_u32 v1, 0x18000, v1\n\t"
    "ds_read_b64_tr_b16 v[24:25], v1 offset:16384\n\t"
    "v_mul_f32 v12, 0xbfb8aa3b, v238\n\t"
    "v_mul_f32 v13, 0xbfb8aa3b, v239\n\t"
    "v_exp_f32 v12, v12\n\t"
    "s_waitcnt lgkmcnt(5)\n\t"
    "v_mfma_f32_16x16x32_bf16 v[244:247], v[28:31], v[168:171], v[244:247]\n\t"
    "ds_read_b64_tr_b16 v[26:27], v1 offset:18432\n\t"
    "v_mfma_f32_16x16x32_bf16 v[252:255], v[28:31], v[172:175], v[252:255]\n\t"
    "v_add_u32 v3, 0x18000, v3\n\t"
    "ds_read_b64_tr_b16 v[28:29], v3 offset:16384\n\t"
    "v_exp_f32 v13, v13\n\t"
    "v_add_f32 v12, 1.0, v12\n\t"
    "v_add_f32 v13, 1.0, v13\n\t"
    "s_setprio 1\n\t"
    "s_waitcnt lgkmcnt(5)\n\t"
    "v_mfma_f32_16x16x32_bf16 v[240:243], v[32:35], v[176:179], v[240:243]\n\t"
    "ds_read_b64_tr_b16 v[30:31], v3 offset:18432\n\t"
    "v_mfma_f32_16x16x32_bf16 v[248:251], v[32:35], v[180:183], v[248:251]\n\t"
    "v_add_u32 v0, 0x18000, v0\n\t"
    "ds_read_b64_tr_b16 v[32:33], v0 offset:16384\n\t"
    "v_rcp_f32 v12, v12\n\t"
    "v_rcp_f32 v13, v13\n\t"
    "s_add_u32 s85, s45, 0x0\n\t"
    "buffer_load_dwordx4 v[72:75], v6, s[28:31], s85 offen sc1\n\t"
    "s_waitcnt vmcnt(12)\n\t"
    "v_add3_u32 v17, v48, v49, v17\n\t"
    "v_add3_u32 v17, v50, v51, v17\n\t"
    "s_waitcnt vmcnt(10)\n\t"
    "v_add3_u32 v17, v52, v53, v17\n\t"
    "v_add3_u32 v17, v54, v55, v17\n\t"
    "v_cmp_ne_u32 vcc, s51, v17\n\t"
    "s_cbranch_vccnz LA3_%=\n\t"
    "LA4_%=:\n\t"
    "s_waitcnt lgkmcnt(5)\n\t"
    "v_mfma_f32_16x16x32_bf16 v[244:247], v[36:39], v[176:179], v[244:247]\n\t"
    "ds_read_b64_tr_b16 v[34:35], v0 offset:18432\n\t"
    "v_mfma_f32_16x16x32_bf16 v[252:255], v[36:39], v[180:183], v[252:255]\n\t"
    "v_add_u32 v2, 0x18000, v2\n\t"
    "ds_read_b64_tr_b16 v[36:37], v2 offset:16384\n\t"
    "ds_read_b64_tr_b16 v[38:39], v2 offset:18432\n\t"
    "v_mul_f32 v12, v238, v12\n\t"
    "v_mul_f32 v13, v239, v13\n\t"
    "v_cvt_pk_bf16_f32 v215, v12, v13\n\t"
    "s_add_u32 s85, s45, 0x0\n\t"
    "buffer_load_dwordx4 v[76:79], v6, s[28:31], s85 offen offset:1024 sc1\n\t"
    "s_waitcnt lgkmcnt(6)\n\t"
    "v_mfma_f32_16x16x32_bf16 v[80:83], v[24:27], v[184:187], 0\n\t"
    "s_add_u32 s85, s47, 0xd800\n\t"
    "buffer_store_dwordx4 v[212:215], v6, s[28:31], s85 offen offset:1024 sc1\n\t"
    "v_add3_u32 v20, v20, v212, v213\n\t"
    "v_add3_u32 v20, v20, v214, v215\n\t"
    "v_sub_u32 v20, s55, v20\n\t"
    "buffer_store_dword v20, v7, s[28:31], s85 offen offset:2048 sc1\n\t"
    "v_mul_f32 v12, 0xbfb8aa3b, v240\n\t"
    "v_mul_f32 v13, 0xbfb8aa3b, v241\n\t"
    "v_exp_f32 v12, v12\n\t"
    "s_add_u32 s85, s45, 0x0\n\t"
    "buffer_load_dword v16, v7, s[28:31], s85 offen offset:2048 sc1\n\t"
    "v_mfma_f32_16x16x32_bf16 v[84:87], v[24:27], v[188:191], 0\n\t"
    "ds_read_b64_tr_b16 v[24:25], v1 offset:24576\n\t"
    "ds_read_b64_tr_b16 v[26:27], v1 offset:26624\n\t"
    "v_exp_f32 v13, v13\n\t"
    "v_add_f32 v12, 1.0, v12\n\t"
    "v_add_f32 v13, 1.0, v13\n\t"
    "s_setprio 0\n\t"
    "s_waitcnt lgkmcnt(6)\n\t"
    "v_mfma_f32_16x16x32_bf16 v[88:91], v[28:31], v[184:187], 0\n\t"
    "v_rcp_f32 v12, v12\n\t"
    "v_rcp_f32 v13, v13\n\t"
    "v_mfma_f32_16x16x32_bf16 v[92:95], v[28:31], v[188:191], 0\n\t"
    "ds_read_b64_tr_b16 v[28:29], v3 offset:24576\n\t"
    "ds_read_b64_tr_b16 v[30:31], v3 offset:26624\n\t"
    "v_mul_f32 v12, v240, v12\n\t"
    "v_mul_f32 v13, v241, v13\n\t"
    "v_cvt_pk_bf16_f32 v216, v12, v13\n\t"
    "s_waitcnt lgkmcnt(6)\n\t"
    "v_mfma_f32_16x16x32_bf16 v[96:99], v[32:35], v[184:187], 0\n\t"
    "v_mul_f32 v14, 0xbfb8aa3b, v242\n\t"
    "v_mul_f32 v15, 0xbfb8aa3b, v243\n\t"
    "v_exp_f32 v14, v14\n\t"
    "v_mfma_f32_16x16x32_bf16 v[100:103], v[32:35], v[188:191], 0\n\t"
    "ds_read_b64_tr_b16 v[32:33], v0 offset:24576\n\t"
    "ds_read_b64_tr_b16 v[34:35], v0 offset:26624\n\t"
    "v_exp_f32 v15, v15\n\t"
    "v_add_f32 v14, 1.0, v14\n\t"
    "v_add_f32 v15, 1.0, v15\n\t"
    "s_waitcnt lgkmcnt(6)\n\t"
    "v_mfma_f32_16x16x32_bf16 v[104:107], v[36:39], v[184:187], 0\n\t"
    "v_rcp_f32 v14, v14\n\t"
    "v_rcp_f32 v15, v15\n\t"
    "s_add_u32 s85, s44, 0x4800\n\t"
    "buffer_load_dwordx4 v[112:115], v6, s[28:31], s85 offen sc1\n\t"
    "v_mfma_f32_16x16x32_bf16 v[108:111], v[36:39], v[188:191], 0\n\t"
    "ds_read_b64_tr_b16 v[36:37], v2 offset:24576\n\t"
    "ds_read_b64_tr_b16 v[38:39], v2 offset:26624\n\t"
    "v_mul_f32 v14, v242, v14\n\t"
    "v_mul_f32 v15, v243, v15\n\t"
    "v_cvt_pk_bf16_f32 v217, v14, v15\n\t"
    "s_barrier\n\t"
    "s_setprio 3\n\t"
    "s_waitcnt lgkmcnt(6)\n\t"
    "v_mfma_f32_16x16x32_bf16 v[120:123], v[24:27], v[184:187], 0\n\t"
    "v_mul_f32 v12, 0xbfb8aa3b, v244\n\t"
    "v_mul_f32 v13, 0xbfb8aa3b, v245\n\t"
    "v_exp_f32 v12, v12\n\t"
    "s_add_u32 s85, s44, 0x4800\n\t"
    "buffer_load_dword v17, v7, s[28:31], s85 offen offset:2048 sc1\n\t"
    "v_mfma_f32_16x16x32_bf16 v[124:127], v[24:27], v[188:191], 0\n\t"
    "ds_read_b64_tr_b16 v[24:25], v1 offset:32768\n\t"
    "ds_read_b64_tr_b16 v[26:27], v1 offset:34816\n\t"
    "v_exp_f32 v13, v13\n\t"
    "v_add_f32 v12, 1.0, v12\n\t"
    "v_add_f32 v13, 1.0, v13\n\t"
    "s_waitcnt lgkmcnt(6)\n\t"
    "v_mfma_f32_16x16x32_bf16 v[128:131], v[28:31], v[184:187], 0\n\t"
    "s_add_u32 m0, s36, 155648\n\t"
    "s_add_u32 s85, s41, 0x12000\n\t"
    "buffer_load_dwordx4 v5, s[24:27], s85 offen lds\n\t"
    "v_rcp_f32 v12, v12\n\t"
    "v_rcp_f32 v13, v13\n\t"
    "v_mfma_f32_16x16x32_bf16 v[132:135], v[28:31], v[188:191], 0\n\t"
    "ds_read_b64_tr_b16 v[28:29], v3 offset:32768\n\t"
    "ds_read_b64_tr_b16 v[30:31], v3 offset:34816\n\t"
    "v_mul_f32 v12, v244, v12\n\t"
    "v_mul_f32 v13, v245, v13\n\t"
    "v_cvt_pk_bf16_f32 v218, v12, v13\n\t"
    "s_waitcnt lgkmcnt(6)\n\t"
    "v_mfma_f32_16x16x32_bf16 v[136:139], v[32:35], v[184:187], 0\n\t"
    "v_mul_f32 v14, 0xbfb8aa3b, v246\n\t"
    "v_mul_f32 v15, 0xbfb8aa3b, v247\n\t"
    "v_exp_f32 v14, v14\n\t"
    "v_mfma_f32_16x16x32_bf16 v[140:143], v[32:35], v[188:191], 0\n\t"
    "ds_read_b64_tr_b16 v[32:33], v0 offset:32768\n\t"
    "ds_read_b64_tr_b16 v[34:35], v0 offset:34816\n\t"
    "v_exp_f32 v15, v15\n\t"
    "v_add_f32 v14, 1.0, v14\n\t"
    "v_add_f32 v15, 1.0, v15\n\t"
    "s_waitcnt vmcnt(13)\n\t"
    "v_add3_u32 v18, v56, v57, v18\n\t"
    "v_add3_u32 v18, v58, v59, v18\n\t"
    "v_add3_u32 v18, v60, v61, v18\n\t"
    "v_add3_u32 v18, v62, v63, v18\n\t"
    "v_cmp_ne_u32 vcc, s51, v18\n\t"
    "s_cbranch_vccnz LA5_%=\n\t"
    "LA6_%=:\n\t"
    "s_waitcnt lgkmcnt(6)\n\t"
    "v_mfma_f32_16x16x32_bf16 v[144:147], v[36:39], v[184:187], 0\n\t"
    "v_rcp_f32 v14, v14\n\t"
    "v_rcp_f32 v15, v15\n\t"
    "s_add_u32 s85, s44, 0x4800\n\t"
    "buffer_load_dwordx4 v[116:119], v6, s[28:31], s85 offen offset:1024 sc1\n\t"
    "v_mfma_f32_16x16x32_bf16 v[148:151], v[36:39], v[188:191], 0\n\t"
    "ds_read_b64_tr_b16 v[36:37], v2 offset:32768\n\t"
    "ds_read_b64_tr_b16 v[38:39], v2 offset:34816\n\t"
    "v_mul_f32 v14, v246, v14\n\t"
    "v_mul_f32 v15, v247, v15\n\t"
    "v_cvt_pk_bf16_f32 v219, v14, v15\n\t"
    "s_waitcnt lgkmcnt(6)\n\t"
    "v_mfma_f32_16x16x32_bf16 v[80:83], v[24:27], v[192:195], v[80:83]\n\t"
    "s_add_u32 s85, s47, 0x12000\n\t"
    "buffer_store_dwordx4 v[216:219], v6, s[28:31], s85 offen sc1\n\t"
    "v_add3_u32 v20, v216, v217, v218\n\t"
    "v_add_u32 v20, v20, v219\n\t"
    "v_mul_f32 v12, 0xbfb8aa3b, v248\n\t"
    "v_mul_f32 v13, 0xbfb8aa3b, v249\n\t"
    "v_exp_f32 v12, v12\n\t"
    "v_mfma_f32_16x16x32_bf16 v[84:87], v[24:27], v[196:199], v[84:87]\n\t"
    "v_add_u32 v1, 0xffff2000, v1\n\t"
    "ds_read_b64_tr_b16 v[24:25], v1 offset:0\n\t"
    "ds_read_b64_tr_b16 v[26:27], v1 offset:2048\n\t"
    "v_exp_f32 v13, v13\n\t"
    "v_add_f32 v12, 1.0, v12\n\t"
    "v_add_f32 v13, 1.0, v13\n\t"
    "s_waitcnt lgkmcnt(6)\n\t"
    "v_mfma_f32_16x16x32_bf16 v[88:91], v[28:31], v[192:195], v[88:91]\n\t"
    "v_rcp_f32 v12, v12\n\t"
    "v_rcp_f32 v13, v13\n\t"
    "v_mfma_f32_16x16x32_bf16 v[92:95], v[28:31], v[196:199], v[92:95]\n\t"
    "v_add_u32 v3, 0xffff2000, v3\n\t"
    "ds_read_b64_tr_b16 v[28:29], v3 offset:0\n\t"
    "ds_read_b64_tr_b16 v[30:31], v3 offset:2048\n\t"
    "v_mul_f32 v12, v248, v12\n\t"
    "v_mul_f32 v13, v249, v13\n\t"
    "v_cvt_pk_bf16_f32 v220, v12, v13\n\t"
    "s_setprio 2\n\t"
    "s_waitcnt lgkmcnt(6)\n\t"
    "v_mfma_f32_16x16x32_bf16 v[96:99], v[32:35], v[192:195], v[96:99]\n\t"
    "v_mul_f32 v14, 0xbfb8aa3b, v250\n\t"
    "v_mul_f32 v15, 0xbfb8aa3b, v251\n\t"
    "v_exp_f32 v14, v14\n\t"
    "v_mfma_f32_16x16x32_bf16 v[100:103], v[32:35], v[196:199], v[100:103]\n\t"
    "v_add_u32 v0, 0xffff2000, v0\n\t"
    "ds_read_b64_tr_b16 v[32:33], v0 offset:0\n\t"
    "ds_read_b64_tr_b16 v[34:35], v0 offset:2048\n\t"
    "v_exp_f32 v15, v15\n\t"
    "v_add_f32 v14, 1.0, v14\n\t"
    "v_add_f32 v15, 1.0, v15\n\t"
    "s_add_u32 s85, s45, 0x4800\n\t"
    "buffer_load_dwordx4 v[152:155], v6, s[28:31], s85 offen sc1\n\t"
    "s_waitcnt vmcnt(13)\n\t"
    "v_add3_u32 v19, v64, v65, v19\n\t"
    "v_add3_u32 v19, v66, v67, v19\n\t"
    "s_waitcnt vmcnt(11)\n\t"
    "v_add3_u32 v19, v68, v69, v19\n\t"
    "v_add3_u32 v19, v70, v71, v19\n\t"
    "v_cmp_ne_u32 vcc, s49, v19\n\t"
    "s_cbranch_vccnz LA7_%=\n\t"
    "LA8_%=:\n\t"
    "s_waitcnt lgkmcnt(6)\n\t"
    "v_mfma_f32_16x16x32_bf16 v[104:107], v[36:39], v[192:195], v[104:107]\n\t"
    "v_rcp_f32 v14, v14\n\t"
    "v_rcp_f32 v15, v15\n\t"
    "v_mfma_f32_16x16x32_bf16 v[108:111], v[36:39], v[196:199], v[108:111]\n\t"
    "v_add_u32 v2, 0xffff2000, v2\n\t"
    "ds_read_b64_tr_b16 v[36:37], v2 offset:0\n\t"
    "ds_read_b64_tr_b16 v[38:39], v2 offset:2048\n\t"
    "v_mul_f32 v14, v250, v14\n\t"
    "v_mul_f32 v15, v251, v15\n\t"
    "v_cvt_pk_bf16_f32 v221, v14, v15\n\t"
    "s_add_u32 s85, s45, 0x4800\n\t"
    "buffer_load_dwordx4 v[156:159], v6, s[28:31], s85 offen offset:1024 sc1\n\t"
    "s_setprio 1\n\t"
    "s_waitcnt lgkmcnt(6)\n\t"
    "v_mfma_f32_16x16x32_bf16 v[120:123], v[24:27], v[192:195], v[120:123]\n\t"
    "v_mul_f32 v12, 0xbfb8aa3b, v252\n\t"
    "v_mul_f32 v13, 0xbfb8aa3b, v253\n\t"
    "v_exp_f32 v12, v12\n\t"
    "s_add_u32 s85, s45, 0x4800\n\t"
    "buffer_load_dword v18, v7, s[28:31], s85 offen offset:2048 sc1\n\t"
    "v_mfma_f32_16x16x32_bf16 v[124:127], v[24:27], v[196:199], v[124:127]\n\t"
    "ds_read_b64_tr_b16 v[24:25], v1 offset:8192\n\t"
    "v_exp_f32 v13, v13\n\t"
    "v_add_f32 v12, 1.0, v12\n\t"
    "v_add_f32 v13, 1.0, v13\n\t"
    "s_waitcnt lgkmcnt(5)\n\t"
    "v_mfma_f32_16x16x32_bf16 v[128:131], v[28:31], v[192:195], v[128:131]\n\t"
    "ds_read_b64_tr_b16 v[26:27], v1 offset:10240\n\t"
    "s_add_u32 m0, s36, 114688\n\t"
    "s_add_u32 s85, s40, 0x24000\n\t"
    "buffer_load_dwordx4 v5, s[24:27], s85 offen lds\n\t"
    "v_rcp_f32 v12, v12\n\t"
    "v_rcp_f32 v13, v13\n\t"
    "v_mfma_f32_16x16x32_bf16 v[132:135], v[28:31], v[196:199], v[132:135]\n\t"
    "ds_read_b64_tr_b16 v[28:29], v3 offset:8192\n\t"
    "ds_read_b64_tr_b16 v[30:31], v3 offset:10240\n\t"
    "v_mul_f32 v12, v252, v12\n\t"
    "v_mul_f32 v13, v253, v13\n\t"
    "v_cvt_pk_bf16_f32 v222, v12, v13\n\t"
    "s_setprio 0\n\t"
    "s_waitcnt lgkmcnt(6)\n\t"
    "v_mfma_f32_16x16x32_bf16 v[136:139], v[32:35], v[192:195], v[136:139]\n\t"
    "v_mul_f32 v14, 0xbfb8aa3b, v254\n\t"
    "v_mul_f32 v15, 0xbfb8aa3b, v255\n\t"
    "v_exp_f32 v14, v14\n\t"
    "v_mfma_f32_16x16x32_bf16 v[140:143], v[32:35], v[196:199], v[140:143]\n\t"
    "ds_read_b64_tr_b16 v[32:33], v0 offset:8192\n\t"
    "ds_read_b64_tr_b16 v[34:35], v0 offset:10240\n\t"
    "v_exp_f32 v15, v15\n\t"
    "v_add_f32 v14, 1.0, v14\n\t"
    "v_add_f32 v15, 1.0, v15\n\t"
    "s_waitcnt lgkmcnt(6)\n\t"
    "v_mfma_f32_16x16x32_bf16 v[144:147], v[36:39], v[192:195], v[144:147]\n\t"
    "v_rcp_f32 v14, v14\n\t"
    "v_rcp_f32 v15, v15\n\t"
    "s_add_u32 s85, s44, 0x9000\n\t"
    "buffer_load_dwordx4 v[160:163], v6, s[28:31], s85 offen sc1\n\t"
    "v_mfma_f32_16x16x32_bf16 v[148:151], v[36:39], v[196:199], v[148:151]\n\t"
    "ds_read_b64_tr_b16 v[36:37], v2 offset:8192\n\t"
    "ds_read_b64_tr_b16 v[38:39], v2 offset:10240\n\t"
    "v_mul_f32 v14, v254, v14\n\t"
    "v_mul_f32 v15, v255, v15\n\t"
    "v_cvt_pk_bf16_f32 v223, v14, v15\n\t"
    "s_barrier\n\t"
    "s_setprio 3\n\t"
    "s_waitcnt lgkmcnt(6)\n\t"
    "v_mfma_f32_16x16x32_bf16 v[80:83], v[24:27], v[200:203], v[80:83]\n\t"
    "s_add_u32 s85, s47, 0x12000\n\t"
    "buffer_store_dwordx4 v[220:223], v6, s[28:31], s85 offen offset:1024 sc1\n\t"
    "v_add3_u32 v20, v20, v220, v221\n\t"
    "v_add3_u32 v20, v20, v222, v223\n\t"
    "v_sub_u32 v20, s55, v20\n\t"
    "buffer_store_dword v20, v7, s[28:31], s85 offen offset:2048 sc1\n\t"
    "s_add_u32 s85, s44, 0x9000\n\t"
    "buffer_load_dword v19, v7, s[28:31], s85 offen offset:2048 sc1\n\t"
    "v_mfma_f32_16x16x32_bf16 v[84:87], v[24:27], v[204:207], v[84:87]\n\t"
    "ds_read_b64_tr_b16 v[24:25], v1 offset:16384\n\t"
    "ds_read_b64_tr_b16 v[26:27], v1 offset:18432\n\t"
    "s_waitcnt lgkmcnt(6)\n\t"
    "v_mfma_f32_16x16x32_bf16 v[88:91], v[28:31], v[200:203], v[88:91]\n\t"
    "v_mfma_f32_16x16x32_bf16 v[92:95], v[28:31], v[204:207], v[92:95]\n\t"
    "ds_read_b64_tr_b16 v[28:29], v3 offset:16384\n\t"
    "s_waitcnt lgkmcnt(5)\n\t"
    "v_mfma_f32_16x16x32_bf16 v[96:99], v[32:35], v[200:203], v[96:99]\n\t"
    "ds_read_b64_tr_b16 v[30:31], v3 offset:18432\n\t"
    "v_mfma_f32_16x16x32_bf16 v[100:103], v[32:35], v[204:207], v[100:103]\n\t"
    "ds_read_b64_tr_b16 v[32:33], v0 offset:16384\n\t"
    "ds_read_b64_tr_b16 v[34:35], v0 offset:18432\n\t"
    "s_waitcnt vmcnt(13)\n\t"
    "v_add3_u32 v16, v72, v73, v16\n\t"
    "v_add3_u32 v16, v74, v75, v16\n\t"
    "v_add3_u32 v16, v76, v77, v16\n\t"
    "v_add3_u32 v16, v78, v79, v16\n\t"
    "v_cmp_ne_u32 vcc, s50, v16\n\t"
    "s_cbranch_vccnz LA9_%=\n\t"
    "LA10_%=:\n\t"
    "s_waitcnt lgkmcnt(6)\n\t"
    "v_mfma_f32_16x16x32_bf16 v[104:107], v[36:39], v[200:203], v[104:107]\n\t"
    "s_add_u32 s85, s44, 0x9000\n\t"
    "buffer_load_dwordx4 v[164:167], v6, s[28:31], s85 offen offset:1024 sc1\n\t"
    "v_mfma_f32_16x16x32_bf16 v[108:111], v[36:39], v[204:207], v[108:111]\n\t"
    "ds_read_b64_tr_b16 v[36:37], v2 offset:16384\n\t"
    "s_waitcnt lgkmcnt(5)\n\t"
    "v_mfma_f32_16x16x32_bf16 v[80:83], v[24:27], v[208:211], v[80:83]\n\t"
    "ds_read_b64_tr_b16 v[38:39], v2 offset:18432\n\t"
    "v_mfma_f32_16x16x32_bf16 v[84:87], v[24:27], v[212:215], v[84:87]\n\t"
    "ds_read_b64_tr_b16 v[24:25], v1 offset:24576\n\t"
    "ds_read_b64_tr_b16 v[26:27], v1 offset:26624\n\t"
    "s_waitcnt lgkmcnt(6)\n\t"
    "v_mfma_f32_16x16x32_bf16 v[88:91], v[28:31], v[208:211], v[88:91]\n\t"
    "v_mfma_f32_16x16x32_bf16 v[92:95], v[28:31], v[212:215], v[92:95]\n\t"
    "ds_read_b64_tr_b16 v[28:29], v3 offset:24576\n\t"
    "s_setprio 2\n\t"
    "s_waitcnt lgkmcnt(5)\n\t"
    "v_mfma_f32_16x16x32_bf16 v[96:99], v[32:35], v[208:211], v[96:99]\n\t"
    "ds_read_b64_tr_b16 v[30:31], v3 offset:26624\n\t"
    "v_mfma_f32_16x16x32_bf16 v[100:103], v[32:35], v[212:215], v[100:103]\n\t"
    "ds_read_b64_tr_b16 v[32:33], v0 offset:24576\n\t"
    "s_add_u32 s85, s45, 0x9000\n\t"
    "buffer_load_dwordx4 v[168:171], v6, s[28:31], s85 offen sc1\n\t"
    "s_waitcnt vmcnt(13)\n\t"
    "v_add3_u32 v17, v112, v113, v17\n\t"
    "v_add3_u32 v17, v114, v115, v17\n\t"
    "s_waitcnt vmcnt(11)\n\t"
    "v_add3_u32 v17, v116, v117, v17\n\t"
    "v_add3_u32 v17, v118, v119, v17\n\t"
    "v_cmp_ne_u32 vcc, s49, v17\n\t"
    "s_cbranch_vccnz LA11_%=\n\t"
    "LA12_%=:\n\t"
    "s_waitcnt lgkmcnt(5)\n\t"
    "v_mfma_f32_16x16x32_bf16 v[104:107], v[36:39], v[208:211], v[104:107]\n\t"
    "ds_read_b64_tr_b16 v[34:35], v0 offset:26624\n\t"
    "v_mfma_f32_16x16x32_bf16 v[108:111], v[36:39], v[212:215], v[108:111]\n\t"
    "ds_read_b64_tr_b16 v[36:37], v2 offset:24576\n\t"
    "ds_read_b64_tr_b16 v[38:39], v2 offset:26624\n\t"
    "s_add_u32 s85, s45, 0x9000\n\t"
    "buffer_load_dwordx4 v[172:175], v6, s[28:31], s85 offen offset:1024 sc1\n\t"
    "s_setprio 1\n\t"
    "s_waitcnt lgkmcnt(6)\n\t"
    "v_mfma_f32_16x16x32_bf16 v[80:83], v[24:27], v[40:43], v[80:83]\n\t"
    "s_add_u32 s85, s45, 0x9000\n\t"
    "buffer_load_dword v16, v7, s[28:31], s85 offen offset:2048 sc1\n\t"
    "v_mfma_f32_16x16x32_bf16 v[84:87], v[24:27], v[44:47], v[84:87]\n\t"
    "ds_read_b64_tr_b16 v[24:25], v1 offset:32768\n\t"
    "ds_read_b64_tr_b16 v[26:27], v1 offset:34816\n\t"
    "s_waitcnt lgkmcnt(6)\n\t"
    "v_mfma_f32_16x16x32_bf16 v[88:91], v[28:31], v[40:43], v[88:91]\n\t"
    "s_add_u32 m0, s36, 0\n\t"
    "s_add_u32 s85, s41, 0x24000\n\t"
    "buffer_load_dwordx4 v5, s[24:27], s85 offen lds\n\t"
    "v_mfma_f32_16x16x32_bf16 v[92:95], v[28:31], v[44:47], v[92:95]\n\t"
    "ds_read_b64_tr_b16 v[28:29], v3 offset:32768\n\t"
    "ds_read_b64_tr_b16 v[30:31], v3 offset:34816\n\t"
    "s_setprio 0\n\t"
    "s_waitcnt lgkmcnt(6)\n\t"
    "v_mfma_f32_16x16x32_bf16 v[96:99], v[32:35], v[40:43], v[96:99]\n\t"
    "v_mfma_f32_16x16x32_bf16 v[100:103], v[32:35], v[44:47], v[100:103]\n\t"
    "ds_read_b64_tr_b16 v[32:33], v0 offset:32768\n\t"
    "ds_read_b64_tr_b16 v[34:35], v0 offset:34816\n\t"
    "s_waitcnt lgkmcnt(6)\n\t"
    "v_mfma_f32_16x16x32_bf16 v[104:107], v[36:39], v[40:43], v[104:107]\n\t"
    "s_add_u32 s85, s44, 0xd800\n\t"
    "buffer_load_dwordx4 v[176:179], v6, s[28:31], s85 offen sc1\n\t"
    "v_mfma_f32_16x16x32_bf16 v[108:111], v[36:39], v[44:47], v[108:111]\n\t"
    "ds_read_b64_tr_b16 v[36:37], v2 offset:32768\n\t"
    "ds_read_b64_tr_b16 v[38:39], v2 offset:34816\n\t"
    "s_barrier\n\t"
    "s_setprio 3\n\t"
    "s_waitcnt lgkmcnt(6)\n\t"
    "v_mfma_f32_16x16x32_bf16 v[80:83], v[24:27], v[48:51], v[80:83]\n\t"
    "s_add_u32 s85, s44, 0xd800\n\t"
    "buffer_load_dword v17, v7, s[28:31], s85 offen offset:2048 sc1\n\t"
    "v_mfma_f32_16x16x32_bf16 v[84:87], v[24:27], v[52:55], v[84:87]\n\t"
    "ds_read_b64_tr_b16 v[24:25], v1 offset:40960\n\t"
    "ds_read_b64_tr_b16 v[26:27], v1 offset:43008\n\t"
    "s_waitcnt lgkmcnt(6)\n\t"
    "v_mfma_f32_16x16x32_bf16 v[88:91], v[28:31], v[48:51], v[88:91]\n\t"
    "s_add_u32 m0, s36, 122880\n\t"
    "s_add_u32 s85, s40, 0x36000\n\t"
    "buffer_load_dwordx4 v5, s[24:27], s85 offen lds\n\t"
    "v_mfma_f32_16x16x32_bf16 v[92:95], v[28:31], v[52:55], v[92:95]\n\t"
    "ds_read_b64_tr_b16 v[28:29], v3 offset:40960\n\t"
    "ds_read_b64_tr_b16 v[30:31], v3 offset:43008\n\t"
    "s_waitcnt lgkmcnt(6)\n\t"
    "v_mfma_f32_16x16x32_bf16 v[96:99], v[32:35], v[48:51], v[96:99]\n\t"
    "s_add_u32 m0, s36, 131072\n\t"
    "s_add_u32 s85, s41, 0x36000\n\t"
    "buffer_load_dwordx4 v5, s[24:27], s85 offen lds\n\t"
    "v_mfma_f32_16x16x32_bf16 v[100:103], v[32:35], v[52:55], v[100:103]\n\t"
    "ds_read_b64_tr_b16 v[32:33], v0 offset:40960\n\t"
    "ds_read_b64_tr_b16 v[34:35], v0 offset:43008\n\t"
    "s_waitcnt vmcnt(14)\n\t"
    "v_add3_u32 v18, v152, v153, v18\n\t"
    "v_add3_u32 v18, v154, v155, v18\n\t"
    "v_add3_u32 v18, v156, v157, v18\n\t"
    "v_add3_u32 v18, v158, v159, v18\n\t"
    "v_cmp_ne_u32 vcc, s50, v18\n\t"
    "s_cbranch_vccnz LA13_%=\n\t"
    "LA14_%=:\n\t"
    "s_waitcnt lgkmcnt(6)\n\t"
    "v_mfma_f32_16x16x32_bf16 v[104:107], v[36:39], v[48:51], v[104:107]\n\t"
    "s_add_u32 s85, s44, 0xd800\n\t"
    "buffer_load_dwordx4 v[180:183], v6, s[28:31], s85 offen offset:1024 sc1\n\t"
    "v_mfma_f32_16x16x32_bf16 v[108:111], v[36:39], v[52:55], v[108:111]\n\t"
    "ds_read_b64_tr_b16 v[36:37], v2 offset:40960\n\t"
    "ds_read_b64_tr_b16 v[38:39], v2 offset:43008\n\t"
    "s_waitcnt lgkmcnt(6)\n\t"
    "v_mfma_f32_16x16x32_bf16 v[80:83], v[24:27], v[56:59], v[80:83]\n\t"
    "v_mfma_f32_16x16x32_bf16 v[84:87], v[24:27], v[60:63], v[84:87]\n\t"
    "ds_read_b64_tr_b16 v[24:25], v1 offset:49152\n\t"
    "ds_read_b64_tr_b16 v[26:27], v1 offset:51200\n\t"
    "s_waitcnt lgkmcnt(6)\n\t"
    "v_mfma_f32_16x16x32_bf16 v[88:91], v[28:31], v[56:59], v[88:91]\n\t"
    "LIPA3x0_%=:\n\t"
    "s_cbranch_execz LIPRA_%=\n\t"
    "s_add_u32 m0, s36, 8192\n\t"
    "s_add_u32 s85, s40, 0x48000\n\t"
    "buffer_load_dwordx4 v5, s[24:27], s85 offen lds\n\t"
    "v_mfma_f32_16x16x32_bf16 v[92:95], v[28:31], v[60:63], v[92:95]\n\t"
    "ds_read_b64_tr_b16 v[28:29], v3 offset:49152\n\t"
    "ds_read_b64_tr_b16 v[30:31], v3 offset:51200\n\t"
    "s_setprio 2\n\t"
    "s_waitcnt lgkmcnt(6)\n\t"
    "v_mfma_f32_16x16x32_bf16 v[96:99], v[32:35], v[56:59], v[96:99]\n\t"
    "v_mfma_f32_16x16x32_bf16 v[100:103], v[32:35], v[60:63], v[100:103]\n\t"
    "ds_read_b64_tr_b16 v[32:33], v0 offset:49152\n\t"
    "ds_read_b64_tr_b16 v[34:35], v0 offset:51200\n\t"
    "s_waitcnt vmcnt(11)\n\t"
    "v_add3_u32 v19, v160, v161, v19\n\t"
    "v_add3_u32 v19, v162, v163, v19\n\t"
    "s_waitcnt vmcnt(10)\n\t"
    "v_add3_u32 v19, v164, v165, v19\n\t"
    "v_add3_u32 v19, v166, v167, v19\n\t"
    "v_cmp_ne_u32 vcc, s49, v19\n\t"
    "s_cbranch_vccnz LA15_%=\n\t"
    "LA16_%=:\n\t"
    "s_waitcnt lgkmcnt(6)\n\t"
    "v_mfma_f32_16x16x32_bf16 v[104:107], v[36:39], v[56:59], v[104:107]\n\t"
    "s_add_u32 s85, s45, 0xd800\n\t"
    "buffer_load_dwordx4 v[184:187], v6, s[28:31], s85 offen sc1\n\t"
    "v_mfma_f32_16x16x32_bf16 v[108:111], v[36:39], v[60:63], v[108:111]\n\t"
    "ds_read_b64_tr_b16 v[36:37], v2 offset:49152\n\t"
    "ds_read_b64_tr_b16 v[38:39], v2 offset:51200\n\t"
    "s_setprio 1\n\t"
    "s_waitcnt lgkmcnt(6)\n\t"
    "v_mfma_f32_16x16x32_bf16 v[80:83], v[24:27], v[216:219], v[80:83]\n\t"
    "s_add_u32 s85, s45, 0xd800\n\t"
    "buffer_load_dword v20, v7, s[28:31], s85 offen offset:2048 sc1\n\t"
    "v_mfma_f32_16x16x32_bf16 v[84:87], v[24:27], v[220:223], v[84:87]\n\t"
    "ds_read_b64_tr_b16 v[24:25], v1 offset:57344\n\t"
    "ds_read_b64_tr_b16 v[26:27], v1 offset:59392\n\t"
    "s_waitcnt lgkmcnt(6)\n\t"
    "v_mfma_f32_16x16x32_bf16 v[88:91], v[28:31], v[216:219], v[88:91]\n\t"
    "s_add_u32 m0, s36, 16384\n\t"
    "s_add_u32 s85, s41, 0x48000\n\t"
    "buffer_load_dwordx4 v5, s[24:27], s85 offen lds\n\t"
    "v_mfma_f32_16x16x32_bf16 v[92:95], v[28:31], v[220:223], v[92:95]\n\t"
    "ds_read_b64_tr_b16 v[28:29], v3 offset:57344\n\t"
    "ds_read_b64_tr_b16 v[30:31], v3 offset:59392\n\t"
    "s_setprio 0\n\t"
    "s_waitcnt lgkmcnt(6)\n\t"
    "v_mfma_f32_16x16x32_bf16 v[96:99], v[32:35], v[216:219], v[96:99]\n\t"
    "s_add_u32 m0, s36, 24576\n\t"
    "s_add_u32 s85, s39, 0x24000\n\t"
    "s_add_u32 s85, s85, s100\n\t"
    "buffer_load_dwordx4 v5, s[24:27], s85 offen lds\n\t"
    "v_mfma_f32_16x16x32_bf16 v[100:103], v[32:35], v[220:223], v[100:103]\n\t"
    "ds_read_b64_tr_b16 v[32:33], v0 offset:57344\n\t"
    "ds_read_b64_tr_b16 v[34:35], v0 offset:59392\n\t"
    "s_waitcnt lgkmcnt(6)\n\t"
    "v_mfma_f32_16x16x32_bf16 v[104:107], v[36:39], v[216:219], v[104:107]\n\t"
    "s_add_u32 s85, s45, 0xd800\n\t"
    "buffer_load_dwordx4 v[188:191], v6, s[28:31], s85 offen offset:1024 sc1\n\t"
    "v_mfma_f32_16x16x32_bf16 v[108:111], v[36:39], v[220:223], v[108:111]\n\t"
    "ds_read_b64_tr_b16 v[36:37], v2 offset:57344\n\t"
    "ds_read_b64_tr_b16 v[38:39], v2 offset:59392\n\t"
    "s_barrier\n\t"
    "s_setprio 3\n\t"
    "s_waitcnt lgkmcnt(6)\n\t"
    "v_mfma_f32_16x16x32_bf16 v[80:83], v[24:27], v[64:67], v[80:83]\n\t"
    "v_mfma_f32_16x16x32_bf16 v[84:87], v[24:27], v[68:71], v[84:87]\n\t"
    "v_add_u32 v1, 0xe000, v1\n\t"
    "ds_read_b64_tr_b16 v[24:25], v1 offset:40960\n\t"
    "s_waitcnt lgkmcnt(5)\n\t"
    "v_mfma_f32_16x16x32_bf16 v[88:91], v[28:31], v[64:67], v[88:91]\n\t"
    "ds_read_b64_tr_b16 v[26:27], v1 offset:43008\n\t"
    "s_add_u32 m0, s36, 32768\n\t"
    "s_add_u32 s85, s39, 0x36000\n\t"
    "s_add_u32 s85, s85, s100\n\t"
    "buffer_load_dwordx4 v5, s[24:27], s85 offen lds\n\t"
    "v_mfma_f32_16x16x32_bf16 v[92:95], v[28:31], v[68:71], v[92:95]\n\t"
    "v_add_u32 v3, 0xe000, v3\n\t"
    "ds_read_b64_tr_b16 v[28:29], v3 offset:40960\n\t"
    "s_waitcnt lgkmcnt(5)\n\t"
    "v_mfma_f32_16x16x32_bf16 v[96:99], v[32:35], v[64:67], v[96:99]\n\t"
    "ds_read_b64_tr_b16 v[30:31], v3 offset:43008\n\t"
    "v_mfma_f32_16x16x32_bf16 v[100:103], v[32:35], v[68:71], v[100:103]\n\t"
    "v_add_u32 v0, 0xe000, v0\n\t"
    "ds_read_b64_tr_b16 v[32:33], v0 offset:40960\n\t"
    "s_waitcnt vmcnt(13)\n\t"
    "v_add3_u32 v16, v168, v169, v16\n\t"
    "v_add3_u32 v16, v170, v171, v16\n\t"
    "v_add3_u32 v16, v172, v173, v16\n\t"
    "v_add3_u32 v16, v174, v175, v16\n\t"
    "v_cmp_ne_u32 vcc, s50, v16\n\t"
    "s_cbranch_vccnz LA17_%=\n\t"
    "LA18_%=:\n\t"
    "s_setprio 2\n\t"
    "s_waitcnt lgkmcnt(5)\n\t"
    "v_mfma_f32_16x16x32_bf16 v[104:107], v[36:39], v[64:67], v[104:107]\n\t"
    "ds_read_b64_tr_b16 v[34:35], v0 offset:43008\n\t"
    "s_add_u32 s85, s44, 0x12000\n\t"
    "buffer_load_dwordx4 v[192:195], v6, s[28:31], s85 offen sc1\n\t"
    "v_mfma_f32_16x16x32_bf16 v[108:111], v[36:39], v[68:71], v[108:111]\n\t"
    "v_add_u32 v2, 0xe000, v2\n\t"
    "ds_read_b64_tr_b16 v[36:37], v2 offset:40960\n\t"
    "s_waitcnt lgkmcnt(5)\n\t"
    "v_mfma_f32_16x16x32_bf16 v[80:83], v[24:27], v[72:75], v[80:83]\n\t"
    "ds_read_b64_tr_b16 v[38:39], v2 offset:43008\n\t"
    "s_add_u32 s85, s44, 0x12000\n\t"
    "buffer_load_dword v21, v7, s[28:31], s85 offen offset:2048 sc1\n\t"
    "v_mfma_f32_16x16x32_bf16 v[84:87], v[24:27], v[76:79], v[84:87]\n\t"
    "ds_read_b64_tr_b16 v[24:25], v1 offset:49152\n\t"
    "s_xor_b32 s74, s91, 5\n\t"
    "v_readlane_b32 s0, v8, s74\n\t"
    "s_nop 0\n\t"
    "s_mul_i32 s0, s0, 0x9e3779b1\n\t"
    "s_add_u32 s0, s0, 0x7f4a7c15\n\t"
    "s_movk_i32 s3, 0x1ff\n\t"
    "s_mov_b32 s4, 0x38800100\n\t"
    "s_waitcnt lgkmcnt(5)\n\t"
    "v_mfma_f32_16x16x32_bf16 v[88:91], v[28:31], v[72:75], v[88:91]\n\t"
    "ds_read_b64_tr_b16 v[26:27], v1 offset:51200\n\t"
    "s_add_u32 m0, s36, 40960\n\t"
    "s_add_u32 s85, s37, s100\n\t"
    "buffer_load_dwordx4 v5, s[24:27], s85 offen lds\n\t"
    "v_mfma_f32_16x16x32_bf16 v[92:95], v[28:31], v[76:79], v[92:95]\n\t"
    "ds_read_b64_tr_b16 v[28:29], v3 offset:49152\n\t"
    "s_setprio 1\n\t"
    "s_waitcnt lgkmcnt(5)\n\t"
    "v_mfma_f32_16x16x32_bf16 v[96:99], v[32:35], v[72:75], v[96:99]\n\t"
    "ds_read_b64_tr_b16 v[30:31], v3 offset:51200\n\t"
    "v_mfma_f32_16x16x32_bf16 v[100:103], v[32:35], v[76:79], v[100:103]\n\t"
    "ds_read_b64_tr_b16 v[32:33], v0 offset:49152\n\t"
    "s_waitcnt vmcnt(13)\n\t"
    "v_add3_u32 v17, v176, v177, v17\n\t"
    "v_add3_u32 v17, v178, v179, v17\n\t"
    "s_waitcnt vmcnt(10)\n\t"
    "v_add3_u32 v17, v180, v181, v17\n\t"
    "v_add3_u32 v17, v182, v183, v17\n\t"
    "v_cmp_ne_u32 vcc, s49, v17\n\t"
    "s_cbranch_vccnz LA19_%=\n\t"
    "LA20_%=:\n\t"
    "s_waitcnt lgkmcnt(5)\n\t"
    "v_mfma_f32_16x16x32_bf16 v[104:107], v[36:39], v[72:75], v[104:107]\n\t"
    "ds_read_b64_tr_b16 v[34:35], v0 offset:51200\n\t"
    "s_add_u32 s85, s44, 0x12000\n\t"
    "buffer_load_dwordx4 v[196:199], v6, s[28:31], s85 offen offset:1024 sc1\n\t"
    "v_mfma_f32_16x16x32_bf16 v[108:111], v[36:39], v[76:79], v[108:111]\n\t"
    "ds_read_b64_tr_b16 v[36:37], v2 offset:49152\n\t"
    "s_waitcnt lgkmcnt(5)\n\t"
    "v_mfma_f32_16x16x32_bf16 v[80:83], v[24:27], v[112:115], v[80:83]\n\t"
    "ds_read_b64_tr_b16 v[38:39], v2 offset:51200\n\t"
    "v_mfma_f32_16x16x32_bf16 v[84:87], v[24:27], v[116:119], v[84:87]\n\t"
    "ds_read_b64_tr_b16 v[24:25], v1 offset:57344\n\t"
    "s_xor_b32 s74, s91, 6\n\t"
    "v_readlane_b32 s1, v8, s74\n\t"
    "s_nop 0\n\t"
    "s_mul_i32 s1, s1, 0x9e3779b1\n\t"
    "s_add_u32 s1, s1, 0x7f4a7c15\n\t"
    "s_setprio 0\n\t"
    "s_waitcnt lgkmcnt(5)\n\t"
    "v_mfma_f32_16x16x32_bf16 v[88:91], v[28:31], v[112:115], v[88:91]\n\t"
    "ds_read_b64_tr_b16 v[26:27], v1 offset:59392\n\t"
    "s_add_u32 m0, s36, 49152\n\t"
    "s_add_u32 s85, s38, s100\n\t"
    "buffer_load_dwordx4 v5, s[24:27], s85 offen lds\n\t"
    "v_mfma_f32_16x16x32_bf16 v[92:95], v[28:31], v[116:119], v[92:95]\n\t"
    "ds_read_b64_tr_b16 v[28:29], v3 offset:57344\n\t"
    "s_waitcnt lgkmcnt(5)\n\t"
    "v_mfma_f32_16x16x32_bf16 v[96:99], v[32:35], v[112:115], v[96:99]\n\t"
    "ds_read_b64_tr_b16 v[30:31], v3 offset:59392\n\t"
    "v_mfma_f32_16x16x32_bf16 v[100:103], v[32:35], v[116:119], v[100:103]\n\t"
    "ds_read_b64_tr_b16 v[32:33], v0 offset:57344\n\t"
    "s_waitcnt lgkmcnt(5)\n\t"
    "v_mfma_f32_16x16x32_bf16 v[104:107], v[36:39], v[112:115], v[104:107]\n\t"
    "ds_read_b64_tr_b16 v[34:35], v0 offset:59392\n\t"
    "s_add_u32 s85, s45, 0x12000\n\t"
    "buffer_load_dwordx4 v[224:227], v6, s[28:31], s85 offen sc1\n\t"
    "v_mfma_f32_16x16x32_bf16 v[108:111], v[36:39], v[116:119], v[108:111]\n\t"
    "ds_read_b64_tr_b16 v[36:37], v2 offset:57344\n\t"
    "s_barrier\n\t"
    "s_setprio 3\n\t"
    "s_waitcnt lgkmcnt(5)\n\t"
    "v_mfma_f32_16x16x32_bf16 v[80:83], v[24:27], v[152:155], v[80:83]\n\t"
    "ds_read_b64_tr_b16 v[38:39], v2 offset:59392\n\t"
    "s_add_u32 s85, s45, 0x12000\n\t"
    "buffer_load_dword v22, v7, s[28:31], s85 offen offset:2048 sc1\n\t"
    "v_mfma_f32_16x16x32_bf16 v[84:87], v[24:27], v[156:159], v[84:87]\n\t"
    "ds_read_b64_tr_b16 v[24:25], v1 offset:16384\n\t"
    "s_xor_b32 s74, s91, 7\n\t"
    "v_readlane_b32 s2, v8, s74\n\t"
    "s_nop 0\n\t"
    "s_mul_i32 s2, s2, 0x9e3779b1\n\t"
    "s_add_u32 s2, s2, 0x7f4a7c15\n\t"
    "s_waitcnt lgkmcnt(5)\n\t"
    "v_mfma_f32_16x16x32_bf16 v[88:91], v[28:31], v[152:155], v[88:91]\n\t"
    "ds_read_b64_tr_b16 v[26:27], v1 offset:18432\n\t"
    "s_add_u32 m0, s36, 57344\n\t"
    "s_add_u32 s85, s96, s100\n\t"
    "buffer_load_dwordx4 v5, s[24:27], s85 offen lds\n\t"
    "v_mfma_f32_16x16x32_bf16 v[92:95], v[28:31], v[156:159], v[92:95]\n\t"
    "ds_read_b64_tr_b16 v[28:29], v3 offset:16384\n\t"
    "ds_read_b64_tr_b16 v[30:31], v3 offset:18432\n\t"
    "s_waitcnt lgkmcnt(6)\n\t"
    "v_mfma_f32_16x16x32_bf16 v[96:99], v[32:35], v[152:155], v[96:99]\n\t"
    "s_add_u32 m0, s36, 65536\n\t"
    "s_add_u32 s85, s39, 0x48000\n\t"
    "s_add_u32 s85, s85, s100\n\t"
    "buffer_load_dwordx4 v5, s[24:27], s85 offen lds\n\t"
    "v_mfma_f32_16x16x32_bf16 v[100:103], v[32:35], v[156:159], v[100:103]\n\t"
    "ds_read_b64_tr_b16 v[32:33], v0 offset:16384\n\t"
    "s_waitcnt vmcnt(13)\n\t"
    "v_add3_u32 v20, v184, v185, v20\n\t"
    "v_add3_u32 v20, v186, v187, v20\n\t"
    "s_waitcnt vmcnt(10)\n\t"
    "v_add3_u32 v20, v188, v189, v20\n\t"
    "v_add3_u32 v20, v190, v191, v20\n\t"
    "v_cmp_ne_u32 vcc, s50, v20\n\t"
    "s_cbranch_vccnz LA21_%=\n\t"
    "LA22_%=:\n\t"
    "s_waitcnt lgkmcnt(5)\n\t"
    "v_mfma_f32_16x16x32_bf16 v[104:107], v[36:39], v[152:155], v[104:107]\n\t"
    "ds_read_b64_tr_b16 v[34:35], v0 offset:18432\n\t"
    "s_add_u32 s85, s45, 0x12000\n\t"
    "buffer_load_dwordx4 v[228:231], v6, s[28:31], s85 offen offset:1024 sc1\n\t"
    "v_mfma_f32_16x16x32_bf16 v[108:111], v[36:39], v[156:159], v[108:111]\n\t"
    "ds_read_b64_tr_b16 v[36:37], v2 offset:16384\n\t"
    "s_waitcnt lgkmcnt(5)\n\t"
    "v_mfma_f32_16x16x32_bf16 v[80:83], v[24:27], v[160:163], v[80:83]\n\t"
    "ds_read_b64_tr_b16 v[38:39], v2 offset:18432\n\t"
    "v_mfma_f32_16x16x32_bf16 v[84:87], v[24:27], v[164:167], v[84:87]\n\t"
    "v_add_u32 v1, 0xfffe8000, v1\n\t"
    "ds_read_b64_tr_b16 v[24:25], v1 offset:0\n\t"
    "ds_read_b64_tr_b16 v[26:27], v1 offset:2048\n\t"
    "s_waitcnt lgkmcnt(6)\n\t"
    "v_mfma_f32_16x16x32_bf16 v[88:91], v[28:31], v[160:163], v[88:91]\n\t"
    "s_add_u32 m0, s36, 73728\n\t"
    "s_add_u32 s85, s40, 0x0\n\t"
    "s_add_u32 s85, s85, s100\n\t"
    "buffer_load_dwordx4 v5, s[24:27], s85 offen lds\n\t"
    "v_mfma_f32_16x16x32_bf16 v[92:95], v[28:31], v[164:167], v[92:95]\n\t"
    "v_add_u32 v3, 0xfffe8000, v3\n\t"
    "ds_read_b64_tr_b16 v[28:29], v3 offset:0\n\t"
    "ds_read_b64_tr_b16 v[30:31], v3 offset:2048\n\t"
    "s_setprio 2\n\t"
    "s_waitcnt lgkmcnt(6)\n\t"
    "v_mfma_f32_16x16x32_bf16 v[96:99], v[32:35], v[160:163], v[96:99]\n\t"
    "s_add_u32 m0, s36, 81920\n\t"
    "s_add_u32 s85, s41, 0x0\n\t"
    "s_add_u32 s85, s85, s100\n\t"
    "buffer_load_dwordx4 v5, s[24:27], s85 offen lds\n\t"
    "v_mfma_f32_16x16x32_bf16 v[100:103], v[32:35], v[164:167], v[100:103]\n\t"
    "v_add_u32 v0, 0xfffe8000, v0\n\t"
    "ds_read_b64_tr_b16 v[32:33], v0 offset:0\n\t"
    "s_waitcnt lgkmcnt(5)\n\t"
    "v_mfma_f32_16x16x32_bf16 v[104:107], v[36:39], v[160:163], v[104:107]\n\t"
    "ds_read_b64_tr_b16 v[34:35], v0 offset:2048\n\t"
    "v_mfma_f32_16x16x32_bf16 v[108:111], v[36:39], v[164:167], v[108:111]\n\t"
    "v_add_u32 v2, 0xfffe8000, v2\n\t"
    "ds_read_b64_tr_b16 v[36:37], v2 offset:0\n\t"
    "s_setprio 1\n\t"
    "s_waitcnt lgkmcnt(5)\n\t"
    "v_mfma_f32_16x16x32_bf16 v[80:83], v[24:27], v[168:171], v[80:83]\n\t"
    "ds_read_b64_tr_b16 v[38:39], v2 offset:2048\n\t"
    "v_mfma_f32_16x16x32_bf16 v[84:87], v[24:27], v[172:175], v[84:87]\n\t"
    "v_add_u32 v1, 0x18000, v1\n\t"
    "ds_read_b64_tr_b16 v[24:25], v1 offset:24576\n\t"
    "s_waitcnt lgkmcnt(5)\n\t"
    "v_mfma_f32_16x16x32_bf16 v[88:91], v[28:31], v[168:171], v[88:91]\n\t"
    "ds_read_b64_tr_b16 v[26:27], v1 offset:26624\n\t"
    "s_add_u32 m0, s36, 90112\n\t"
    "s_add_u32 s85, s40, 0x12000\n\t"
    "s_add_u32 s85, s85, s100\n\t"
    "buffer_load_dwordx4 v5, s[24:27], s85 offen lds\n\t"
    "v_mfma_f32_16x16x32_bf16 v[92:95], v[28:31], v[172:175], v[92:95]\n\t"
    "v_add_u32 v3, 0x18000, v3\n\t"
    "ds_read_b64_tr_b16 v[28:29], v3 offset:24576\n\t"
    "ds_read_b64_tr_b16 v[30:31], v3 offset:26624\n\t"
    "s_setprio 0\n\t"
    "s_waitcnt lgkmcnt(6)\n\t"
    "v_mfma_f32_16x16x32_bf16 v[96:99], v[32:35], v[168:171], v[96:99]\n\t"
    "s_add_u32 m0, s36, 98304\n\t"
    "s_add_u32 s85, s41, 0x12000\n\t"
    "s_add_u32 s85, s85, s100\n\t"
    "buffer_load_dwordx4 v5, s[24:27], s85 offen lds\n\t"
    "v_mfma_f32_16x16x32_bf16 v[100:103], v[32:35], v[172:175], v[100:103]\n\t"
    "s_waitcnt vmcnt(12)\n\t"
    "v_add3_u32 v21, v192, v193, v21\n\t"
    "v_add3_u32 v21, v194, v195, v21\n\t"
    "s_waitcnt vmcnt(10)\n\t"
    "v_add3_u32 v21, v196, v197, v21\n\t"
    "v_add3_u32 v21, v198, v199, v21\n\t"
    "v_cmp_ne_u32 vcc, s49, v21\n\t"
    "s_cbranch_vccnz LA23_%=\n\t"
    "LA24_%=:\n\t"
    "v_add_u32 v0, 0x18000, v0\n\t"
    "ds_read_b64_tr_b16 v[32:33], v0 offset:24576\n\t"
    "s_waitcnt lgkmcnt(5)\n\t"
    "v_mfma_f32_16x16x32_bf16 v[104:107], v[36:39], v[168:171], v[104:107]\n\t"
    "ds_read_b64_tr_b16 v[34:35], v0 offset:26624\n\t"
    "v_mfma_f32_16x16x32_bf16 v[108:111], v[36:39], v[172:175], v[108:111]\n\t"
    "v_add_u32 v2, 0x18000, v2\n\t"
    "ds_read_b64_tr_b16 v[36:37], v2 offset:24576\n\t"
    "s_barrier\n\t"
    "s_setprio 3\n\t"
    "s_waitcnt lgkmcnt(5)\n\t"
    "v_mfma_f32_16x16x32_bf16 v[80:83], v[24:27], v[176:179], v[80:83]\n\t"
    "ds_read_b64_tr_b16 v[38:39], v2 offset:26624\n\t"
    "v_mfma_f32_16x16x32_bf16 v[84:87], v[24:27], v[180:183], v[84:87]\n\t"
    "ds_read_b64_tr_b16 v[24:25], v1 offset:32768\n\t"
    "ds_read_b64_tr_b16 v[26:27], v1 offset:34816\n\t"
    "s_waitcnt lgkmcnt(6)\n\t"
    "v_mfma_f32_16x16x32_bf16 v[88:91], v[28:31], v[176:179], v[88:91]\n\t"
    "s_add_u32 m0, s36, 106496\n\t"
    "s_add_u32 s85, s40, 0x24000\n\t"
    "s_add_u32 s85, s85, s100\n\t"
    "buffer_load_dwordx4 v5, s[24:27], s85 offen lds\n\t"
    "v_mfma_f32_16x16x32_bf16 v[92:95], v[28:31], v[180:183], v[92:95]\n\t"
    "ds_read_b64_tr_b16 v[28:29], v3 offset:32768\n\t"
    "ds_read_b64_tr_b16 v[30:31], v3 offset:34816\n\t"
    "s_waitcnt lgkmcnt(6)\n\t"
    "v_mfma_f32_16x16x32_bf16 v[96:99], v[32:35], v[176:179], v[96:99]\n\t"
    "s_add_u32 m0, s36, 114688\n\t"
    "s_add_u32 s85, s41, 0x24000\n\t"
    "s_add_u32 s85, s85, s100\n\t"
    "buffer_load_dwordx4 v5, s[24:27], s85 offen lds\n\t"
    "v_mfma_f32_16x16x32_bf16 v[100:103], v[32:35], v[180:183], v[100:103]\n\t"
    "ds_read_b64_tr_b16 v[32:33], v0 offset:32768\n\t"
    "s_waitcnt lgkmcnt(5)\n\t"
    "v_mfma_f32_16x16x32_bf16 v[104:107], v[36:39], v[176:179], v[104:107]\n\t"
    "ds_read_b64_tr_b16 v[34:35], v0 offset:34816\n\t"
    "v_mfma_f32_16x16x32_bf16 v[108:111], v[36:39], v[180:183], v[108:111]\n\t"
    "ds_read_b64_tr_b16 v[36:37], v2 offset:32768\n\t"
    "s_waitcnt lgkmcnt(5)\n\t"
    "v_mfma_f32_16x16x32_bf16 v[80:83], v[24:27], v[184:187], v[80:83]\n\t"
    "ds_read_b64_tr_b16 v[38:39], v2 offset:34816\n\t"
    "v_mfma_f32_16x16x32_bf16 v[84:87], v[24:27], v[188:191], v[84:87]\n\t"
    "v_add_u32 v1, 0xfffea000, v1\n\t"
    "ds_read_b64_tr_b16 v[24:25], v1 offset:0\n\t"
    "s_lshl_b32 s84, s89, 11\n\t"
    "s_add_u32 s84, s84, 98304\n\t"
    "v_add_u32 v4, s84, v6\n\t"
    "v_xor_b32 v8, 0x2000, v4\n\t"
    "v_mov_b32 v11, 0xffffff00\n\t"
    "s_waitcnt lgkmcnt(5)\n\t"
    "v_mfma_f32_16x16x32_bf16 v[88:91], v[28:31], v[184:187], v[88:91]\n\t"
    "ds_read_b64_tr_b16 v[26:27], v1 offset:2048\n\t"
    "s_add_u32 m0, s36, 0\n\t"
    "s_add_u32 s85, s40, 0x36000\n\t"
    "s_add_u32 s85, s85, s100\n\t"
    "buffer_load_dwordx4 v5, s[24:27], s85 offen lds\n\t"
    "v_mfma_f32_16x16x32_bf16 v[92:95], v[28:31], v[188:191], v[92:95]\n\t"
    "ds_read_b64_tr_b16 v[28:29], v1 offset:8192\n\t"
    "s_setprio 2\n\t"
    "s_waitcnt lgkmcnt(5)\n\t"
    "v_mfma_f32_16x16x32_bf16 v[96:99], v[32:35], v[184:187], v[96:99]\n\t"
    "ds_read_b64_tr_b16 v[30:31], v1 offset:10240\n\t"
    "v_mfma_f32_16x16x32_bf16 v[100:103], v[32:35], v[188:191], v[100:103]\n\t"
    "v_add_u32 v3, 0xfffea000, v3\n\t"
    "ds_read_b64_tr_b16 v[32:33], v3 offset:0\n\t"
    "ds_read_b64_tr_b16 v[34:35], v3 offset:2048\n\t"
    "s_waitcnt vmcnt(10)\n\t"
    "v_add3_u32 v22, v224, v225, v22\n\t"
    "v_add3_u32 v22, v226, v227, v22\n\t"
    "s_waitcnt vmcnt(7)\n\t"
    "v_add3_u32 v22, v228, v229, v22\n\t"
    "v_add3_u32 v22, v230, v231, v22\n\t"
    "v_cmp_ne_u32 vcc, s50, v22\n\t"
    "s_cbranch_vccnz LA25_%=\n\t"
    "LA26_%=:\n\t"
    "s_waitcnt lgkmcnt(6)\n\t"
    "v_mfma_f32_16x16x32_bf16 v[104:107], v[36:39], v[184:187], v[104:107]\n\t"
    "v_mfma_f32_16x16x32_bf16 v[108:111], v[36:39], v[188:191], v[108:111]\n\t"
    "ds_read_b64_tr_b16 v[36:37], v3 offset:8192\n\t"
    "s_setprio 1\n\t"
    "s_waitcnt lgkmcnt(5)\n\t"
    "v_mfma_f32_16x16x32_bf16 v[80:83], v[24:27], v[192:195], v[80:83]\n\t"
    "ds_read_b64_tr_b16 v[38:39], v3 offset:10240\n\t"
    "v_mfma_f32_16x16x32_bf16 v[84:87], v[24:27], v[196:199], v[84:87]\n\t"
    "v_add_u32 v0, 0xfffea000, v0\n\t"
    "ds_read_b64_tr_b16 v[24:25], v0 offset:0\n\t"
    "s_waitcnt lgkmcnt(5)\n\t"
    "v_mfma_f32_16x16x32_bf16 v[80:83], v[28:31], v[224:227], v[80:83]\n\t"
    "ds_read_b64_tr_b16 v[26:27], v0 offset:2048\n\t"
    "v_mfma_f32_16x16x32_bf16 v[84:87], v[28:31], v[228:231], v[84:87]\n\t"
    "ds_read_b64_tr_b16 v[28:29], v0 offset:8192\n\t"
    "s_setprio 0\n\t"
    "s_waitcnt lgkmcnt(5)\n\t"
    "v_mfma_f32_16x16x32_bf16 v[88:91], v[32:35], v[192:195], v[88:91]\n\t"
    "ds_read_b64_tr_b16 v[30:31], v0 offset:10240\n\t"
    "v_mfma_f32_16x16x32_bf16 v[92:95], v[32:35], v[196:199], v[92:95]\n\t"
    "v_add_u32 v2, 0xfffea000, v2\n\t"
    "ds_read_b64_tr_b16 v[32:33], v2 offset:0\n\t"
    "ds_write_b128 v4, v[80:83] offset:49152\n\t"
    "s_waitcnt lgkmcnt(6)\n\t"
    "v_mfma_f32_16x16x32_bf16 v[88:91], v[36:39], v[224:227], v[88:91]\n\t"
    "ds_read_b64_tr_b16 v[34:35], v2 offset:2048\n\t"
    "v_mfma_f32_16x16x32_bf16 v[92:95], v[36:39], v[228:231], v[92:95]\n\t"
    "ds_read_b64_tr_b16 v[36:37], v2 offset:8192\n\t"
    "s_barrier\n\t"
    "s_setprio 3\n\t"
    "s_waitcnt lgkmcnt(6)\n\t"
    "v_mfma_f32_16x16x32_bf16 v[96:99], v[24:27], v[192:195], v[96:99]\n\t"
    "ds_read_b64_tr_b16 v[38:39], v2 offset:10240\n\t"
    "v_mfma_f32_16x16x32_bf16 v[100:103], v[24:27], v[196:199], v[100:103]\n\t"
    "ds_read_b64_tr_b16 v[24:25], v3 offset:16384\n\t"
    "ds_write_b128 v4, v[84:87] offset:50176\n\t"
    "s_waitcnt lgkmcnt(7)\n\t"
    "v_mfma_f32_16x16x32_bf16 v[96:99], v[28:31], v[224:227], v[96:99]\n\t"
    "ds_read_b64_tr_b16 v[26:27], v3 offset:18432\n\t"
    "s_add_u32 m0, s36, 122880\n\t"
    "s_add_u32 s85, s41, 0x36000\n\t"
    "s_add_u32 s85, s85, s100\n\t"
    "buffer_load_dwordx4 v5, s[24:27], s85 offen lds\n\t"
    "v_mfma_f32_16x16x32_bf16 v[100:103], v[28:31], v[228:231], v[100:103]\n\t"
    "ds_read_b64_tr_b16 v[28:29], v2 offset:16384\n\t"
    "s_setprio 2\n\t"
    "s_waitcnt lgkmcnt(6)\n\t"
    "v_mfma_f32_16x16x32_bf16 v[104:107], v[32:35], v[192:195], v[104:107]\n\t"
    "ds_read_b64_tr_b16 v[30:31], v2 offset:18432\n\t"
    "v_mfma_f32_16x16x32_bf16 v[108:111], v[32:35], v[196:199], v[108:111]\n\t"
    "ds_read_b64_tr_b16 v[32:33], v3 offset:24576\n\t"
    "ds_read_b64_tr_b16 v[34:35], v3 offset:26624\n\t"
    "ds_write_b128 v4, v[88:91] offset:32768\n\t"
    "s_waitcnt lgkmcnt(8)\n\t"
    "v_mfma_f32_16x16x32_bf16 v[104:107], v[36:39], v[224:227], v[104:107]\n\t"
    "v_mfma_f32_16x16x32_bf16 v[108:111], v[36:39], v[228:231], v[108:111]\n\t"
    "ds_read_b64_tr_b16 v[36:37], v2 offset:24576\n\t"
    "s_setprio 1\n\t"
    "s_waitcnt lgkmcnt(6)\n\t"
    "v_mfma_f32_16x16x32_bf16 v[128:131], v[24:27], v[200:203], v[128:131]\n\t"
    "ds_read_b64_tr_b16 v[38:39], v2 offset:26624\n\t"
    "v_mfma_f32_16x16x32_bf16 v[132:135], v[24:27], v[204:207], v[132:135]\n\t"
    "ds_read_b64_tr_b16 v[24:25], v3 offset:32768\n\t"
    "ds_read_b64_tr_b16 v[26:27], v3 offset:34816\n\t"
    "ds_write_b128 v4, v[92:95] offset:33792\n\t"
    "s_waitcnt lgkmcnt(8)\n\t"
    "v_mfma_f32_16x16x32_bf16 v[144:147], v[28:31], v[200:203], v[144:147]\n\t"
    "v_mfma_f32_16x16x32_bf16 v[148:151], v[28:31], v[204:207], v[148:151]\n\t"
    "ds_read_b64_tr_b16 v[28:29], v2 offset:32768\n\t"
    "s_setprio 0\n\t"
    "s_waitcnt lgkmcnt(7)\n\t"
    "v_mfma_f32_16x16x32_bf16 v[128:131], v[32:35], v[208:211], v[128:131]\n\t"
    "ds_read_b64_tr_b16 v[30:31], v2 offset:34816\n\t"
    "v_mfma_f32_16x16x32_bf16 v[132:135], v[32:35], v[212:215], v[132:135]\n\t"
    "ds_read_b64_tr_b16 v[32:33], v3 offset:40960\n\t"
    "s_waitcnt lgkmcnt(6)\n\t"
    "v_mfma_f32_16x16x32_bf16 v[144:147], v[36:39], v[208:211], v[144:147]\n\t"
    "ds_read_b64_tr_b16 v[34:35], v3 offset:43008\n\t"
    "v_mfma_f32_16x16x32_bf16 v[148:151], v[36:39], v[212:215], v[148:151]\n\t"
    "ds_read_b64_tr_b16 v[36:37], v2 offset:40960\n\t"
    "s_waitcnt vmcnt(4)\n\t"
    "s_waitcnt lgkmcnt(5)\n\t"
    "s_barrier\n\t"
    "s_setprio 3\n\t"
    "v_mfma_f32_16x16x32_bf16 v[128:131], v[24:27], v[40:43], v[128:131]\n\t"
    "ds_read_b64_tr_b16 v[38:39], v2 offset:43008\n\t"
    "ds_read_b128 v[80:83], v8 offset:49152\n\t"
    "ds_read_b128 v[88:91], v8 offset:32768\n\t"
    "v_mfma_f32_16x16x32_bf16 v[132:135], v[24:27], v[44:47], v[132:135]\n\t"
    "ds_read_b128 v[84:87], v8 offset:50176\n\t"
    "ds_read_b128 v[92:95], v8 offset:33792\n\t"
    "ds_read_b64_tr_b16 v[24:25], v3 offset:49152\n\t"
    "s_waitcnt lgkmcnt(9)\n\t"
    "v_mfma_f32_16x16x32_bf16 v[144:147], v[28:31], v[40:43], v[144:147]\n\t"
    "s_add_u32 m0, s36, 8192\n\t"
    "s_add_u32 s85, s40, 0x48000\n\t"
    "s_add_u32 s85, s85, s100\n\t"
    "buffer_load_dwordx4 v5, s[24:27], s85 offen lds\n\t"
    "s_waitcnt lgkmcnt(4)\n\t"
    "v_add_f32 v96, v96, v80\n\t"
    "v_add_f32 v97, v97, v81\n\t"
    "v_add_f32 v98, v98, v82\n\t"
    "v_add_f32 v99, v99, v83\n\t"
    "ds_read_b64_tr_b16 v[26:27], v3 offset:51200\n\t"
    "v_mfma_f32_16x16x32_bf16 v[148:151], v[28:31], v[44:47], v[148:151]\n\t"
    "s_waitcnt lgkmcnt(3)\n\t"
    "v_add_f32 v100, v100, v84\n\t"
    "v_add_f32 v101, v101, v85\n\t"
    "v_add_f32 v102, v102, v86\n\t"
    "v_add_f32 v103, v103, v87\n\t"
    "ds_read_b64_tr_b16 v[28:29], v2 offset:49152\n\t"
    "v_mfma_f32_16x16x32_bf16 v[128:131], v[32:35], v[48:51], v[128:131]\n\t"
    "s_add_u32 m0, s36, 16384\n\t"
    "s_add_u32 s85, s41, 0x48000\n\t"
    "s_add_u32 s85, s85, s100\n\t"
    "buffer_load_dwordx4 v5, s[24:27], s85 offen lds\n\t"
    "v_maximum3_f32 v80, |v96|, |v97|, |v98|\n\t"
    "v_maximum3_f32 v81, |v99|, |v100|, |v101|\n\t"
    "v_maximum3_f32 v80, v80, |v102|, |v103|\n\t"
    "v_maximum3_f32 v80, v80, v81, v81\n\t"
    "v_bfe_u32 v81, v80, 23, 8\n\t"
    "ds_read_b64_tr_b16 v[30:31], v2 offset:51200\n\t"
    "v_mfma_f32_16x16x32_bf16 v[132:135], v[32:35], v[52:55], v[132:135]\n\t"
    "v_sub_u32 v80, 0x7e, v81\n\t"
    "v_ldexp_f32 v96, v96, v80\n\t"
    "v_ldexp_f32 v97, v97, v80\n\t"
    "v_cvt_pknorm_i16_f32 v96, v96, v97\n\t"
    "v_ldexp_f32 v98, v98, v80\n\t"
    "ds_read_b64_tr_b16 v[32:33], v3 offset:57344\n\t"
    "v_mfma_f32_16x16x32_bf16 v[144:147], v[36:39], v[48:51], v[144:147]\n\t"
    "v_ldexp_f32 v99, v99, v80\n\t"
    "v_cvt_pknorm_i16_f32 v97, v98, v99\n\t"
    "v_ldexp_f32 v100, v100, v80\n\t"
    "v_ldexp_f32 v101, v101, v80\n\t"
    "v_cvt_pknorm_i16_f32 v98, v100, v101\n\t"
    "ds_read_b64_tr_b16 v[34:35], v3 offset:59392\n\t"
    "v_mfma_f32_16x16x32_bf16 v[148:151], v[36:39], v[52:55], v[148:151]\n\t"
    "v_ldexp_f32 v102, v102, v80\n\t"
    "v_ldexp_f32 v103, v103, v80\n\t"
    "v_cvt_pknorm_i16_f32 v99, v102, v103\n\t"
    "buffer_store_dwordx4 v[96:99], v6, s[28:31], s70 offen sc1\n\t"
    "v_add3_u32 v84, v96, v97, v98\n\t"
    "ds_read_b64_tr_b16 v[36:37], v2 offset:57344\n\t"
    "s_waitcnt lgkmcnt(5)\n\t"
    "v_mfma_f32_16x16x32_bf16 v[128:131], v[24:27], v[56:59], v[128:131]\n\t"
    "v_add3_u32 v84, v84, v99, s55\n\t"
    "v_bfi_b32 v84, s3, v81, v84\n\t"
    "buffer_store_dword v84, v7, s[28:31], s70 offen offset:1024 sc1\n\t"
    "ds_read_b64_tr_b16 v[38:39], v2 offset:59392\n\t"
    "v_mfma_f32_16x16x32_bf16 v[132:135], v[24:27], v[60:63], v[132:135]\n\t"
    "v_add_u32 v3, 0x10000, v3\n\t"
    "ds_read_b64_tr_b16 v[24:25], v3 offset:0\n\t"
    "v_add_f32 v104, v104, v88\n\t"
    "v_add_f32 v105, v105, v89\n\t"
    "v_add_f32 v106, v106, v90\n\t"
    "v_add_f32 v107, v107, v91\n\t"
    "s_waitcnt lgkmcnt(5)\n\t"
    "v_mfma_f32_16x16x32_bf16 v[144:147], v[28:31], v[56:59], v[144:147]\n\t"
    "v_add_f32 v108, v108, v92\n\t"
    "v_add_f32 v109, v109, v93\n\t"
    "v_add_f32 v110, v110, v94\n\t"
    "v_add_f32 v111, v111, v95\n\t"
    "ds_read_b64_tr_b16 v[26:27], v3 offset:2048\n\t"
    "v_mfma_f32_16x16x32_bf16 v[148:151], v[28:31], v[60:63], v[148:151]\n\t"
    "v_add_u32 v2, 0x10000, v2\n\t"
    "ds_read_b64_tr_b16 v[28:29], v2 offset:0\n\t"
    "v_maximum3_f32 v88, |v104|, |v105|, |v106|\n\t"
    "v_maximum3_f32 v89, |v107|, |v108|, |v109|\n\t"
    "v_maximum3_f32 v88, v88, |v110|, |v111|\n\t"
    "v_maximum3_f32 v88, v88, v89, v89\n\t"
    "v_bfe_u32 v89, v88, 23, 8\n\t"
    "s_setprio 2\n\t"
    "s_waitcnt lgkmcnt(5)\n\t"
    "v_mfma_f32_16x16x32_bf16 v[128:131], v[32:35], v[216:219], v[128:131]\n\t"
    "v_sub_u32 v88, 0x7e, v89\n\t"
    "v_ldexp_f32 v104, v104, v88\n\t"
    "v_ldexp_f32 v105, v105, v88\n\t"
    "v_cvt_pknorm_i16_f32 v104, v104, v105\n\t"
    "v_ldexp_f32 v106, v106, v88\n\t"
    "ds_read_b64_tr_b16 v[30:31], v2 offset:2048\n\t"
    "v_mfma_f32_16x16x32_bf16 v[132:135], v[32:35], v[220:223], v[132:135]\n\t"
    "v_ldexp_f32 v107, v107, v88\n\t"
    "v_cvt_pknorm_i16_f32 v105, v106, v107\n\t"
    "v_ldexp_f32 v108, v108, v88\n\t"
    "v_ldexp_f32 v109, v109, v88\n\t"
    "v_cvt_pknorm_i16_f32 v106, v108, v109\n\t"
    "ds_read_b64_tr_b16 v[32:33], v3 offset:8192\n\t"
    "s_waitcnt lgkmcnt(5)\n\t"
    "v_mfma_f32_16x16x32_bf16 v[144:147], v[36:39], v[216:219], v[144:147]\n\t"
    "v_ldexp_f32 v110, v110, v88\n\t"
    "v_ldexp_f32 v111, v111, v88\n\t"
    "v_cvt_pknorm_i16_f32 v107, v110, v111\n\t"
    "buffer_store_dwordx4 v[104:107], v6, s[28:31], s71 offen sc1\n\t"
    "v_add3_u32 v92, v104, v105, v106\n\t"
    "ds_read_b64_tr_b16 v[34:35], v3 offset:10240\n\t"
    "v_mfma_f32_16x16x32_bf16 v[148:151], v[36:39], v[220:223], v[148:151]\n\t"
    "v_add3_u32 v92, v92, v107, s55\n\t"
    "v_bfi_b32 v92, s3, v89, v92\n\t"
    "buffer_store_dword v92, v7, s[28:31], s71 offen offset:1024 sc1\n\t"
    "ds_read_b64_tr_b16 v[36:37], v2 offset:8192\n\t"
    "s_setprio 1\n\t"
    "s_waitcnt lgkmcnt(5)\n\t"
    "v_mfma_f32_16x16x32_bf16 v[128:131], v[24:27], v[64:67], v[128:131]\n\t"
    "ds_read_b64_tr_b16 v[38:39], v2 offset:10240\n\t"
    "v_mfma_f32_16x16x32_bf16 v[132:135], v[24:27], v[68:71], v[132:135]\n\t"
    "ds_read_b64_tr_b16 v[24:25], v3 offset:16384\n\t"
    "s_waitcnt lgkmcnt(5)\n\t"
    "v_mfma_f32_16x16x32_bf16 v[144:147], v[28:31], v[64:67], v[144:147]\n\t"
    "ds_read_b64_tr_b16 v[26:27], v3 offset:18432\n\t"
    "v_mfma_f32_16x16x32_bf16 v[148:151], v[28:31], v[68:71], v[148:151]\n\t"
    "ds_read_b64_tr_b16 v[28:29], v2 offset:16384\n\t"
    "s_setprio 0\n\t"
    "s_waitcnt lgkmcnt(5)\n\t"
    "v_mfma_f32_16x16x32_bf16 v[128:131], v[32:35], v[72:75], v[128:131]\n\t"
    "ds_read_b64_tr_b16 v[30:31], v2 offset:18432\n\t"
    "v_mfma_f32_16x16x32_bf16 v[132:135], v[32:35], v[76:79], v[132:135]\n\t"
    "ds_read_b64_tr_b16 v[32:33], v3 offset:24576\n\t"
    "s_waitcnt lgkmcnt(5)\n\t"
    "v_mfma_f32_16x16x32_bf16 v[144:147], v[36:39], v[72:75], v[144:147]\n\t"
    "ds_read_b64_tr_b16 v[34:35], v3 offset:26624\n\t"
    "v_mfma_f32_16x16x32_bf16 v[148:151], v[36:39], v[76:79], v[148:151]\n\t"
    "ds_read_b64_tr_b16 v[36:37], v2 offset:24576\n\t"
    "s_waitcnt vmcnt(4)\n\t"
    "s_barrier\n\t"
    "s_setprio 3\n\t"
    "s_waitcnt lgkmcnt(5)\n\t"
    "v_mfma_f32_16x16x32_bf16 v[128:131], v[24:27], v[112:115], v[128:131]\n\t"
    "ds_read_b64_tr_b16 v[38:39], v2 offset:26624\n\t"
    "v_mfma_f32_16x16x32_bf16 v[132:135], v[24:27], v[116:119], v[132:135]\n\t"
    "ds_read_b64_tr_b16 v[24:25], v3 offset:32768\n\t"
    "s_waitcnt lgkmcnt(5)\n\t"
    "v_mfma_f32_16x16x32_bf16 v[144:147], v[28:31], v[112:115], v[144:147]\n\t"
    "LIPA4x0_%=:\n\t"
    "s_cbranch_execz LIPRA_%=\n\t"
    "ds_read_b64_tr_b16 v[26:27], v3 offset:34816\n\t"
    "v_mfma_f32_16x16x32_bf16 v[148:151], v[28:31], v[116:119], v[148:151]\n\t"
    "ds_read_b64_tr_b16 v[28:29], v2 offset:32768\n\t"
    "s_waitcnt lgkmcnt(5)\n\t"
    "v_mfma_f32_16x16x32_bf16 v[128:131], v[32:35], v[152:155], v[128:131]\n\t"
    "ds_read_b64_tr_b16 v[30:31], v2 offset:34816\n\t"
    "v_mfma_f32_16x16x32_bf16 v[132:135], v[32:35], v[156:159], v[132:135]\n\t"
    "ds_read_b64_tr_b16 v[32:33], v3 offset:40960\n\t"
    "s_waitcnt lgkmcnt(5)\n\t"
    "v_mfma_f32_16x16x32_bf16 v[144:147], v[36:39], v[152:155], v[144:147]\n\t"
    "ds_read_b64_tr_b16 v[34:35], v3 offset:43008\n\t"
    "v_mfma_f32_16x16x32_bf16 v[148:151], v[36:39], v[156:159], v[148:151]\n\t"
    "ds_read_b64_tr_b16 v[36:37], v2 offset:40960\n\t"
    "s_waitcnt lgkmcnt(5)\n\t"
    "v_mfma_f32_16x16x32_bf16 v[128:131], v[24:27], v[160:163], v[128:131]\n\t"
    "ds_read_b64_tr_b16 v[38:39], v2 offset:43008\n\t"
    "v_mfma_f32_16x16x32_bf16 v[132:135], v[24:27], v[164:167], v[132:135]\n\t"
    "v_add_u32 v3, 0xfffee000, v3\n\t"
    "ds_read_b64_tr_b16 v[24:25], v3 offset:0\n\t"
    "s_waitcnt lgkmcnt(5)\n\t"
    "v_mfma_f32_16x16x32_bf16 v[144:147], v[28:31], v[160:163], v[144:147]\n\t"
    "ds_read_b64_tr_b16 v[26:27], v3 offset:2048\n\t"
    "v_mfma_f32_16x16x32_bf16 v[148:151], v[28:31], v[164:167], v[148:151]\n\t"
    "v_add_u32 v3, 0x18000, v3\n\t"
    "ds_read_b64_tr_b16 v[28:29], v3 offset:24576\n\t"
    "s_waitcnt lgkmcnt(5)\n\t"
    "v_mfma_f32_16x16x32_bf16 v[128:131], v[32:35], v[168:171], v[128:131]\n\t"
    "ds_read_b64_tr_b16 v[30:31], v3 offset:26624\n\t"
    "v_mfma_f32_16x16x32_bf16 v[132:135], v[32:35], v[172:175], v[132:135]\n\t"
    "v_add_u32 v3, 0xfffea000, v3\n\t"
    "ds_read_b64_tr_b16 v[32:33], v3 offset:0\n\t"
    "s_waitcnt lgkmcnt(5)\n\t"
    "v_mfma_f32_16x16x32_bf16 v[144:147], v[36:39], v[168:171], v[144:147]\n\t"
    "ds_read_b64_tr_b16 v[34:35], v3 offset:2048\n\t"
    "v_mfma_f32_16x16x32_bf16 v[148:151], v[36:39], v[172:175], v[148:151]\n\t"
    "ds_read_b64_tr_b16 v[36:37], v3 offset:8192\n\t"
    "s_waitcnt lgkmcnt(5)\n\t"
    "v_mfma_f32_16x16x32_bf16 v[128:131], v[24:27], v[176:179], v[128:131]\n\t"
    "ds_read_b64_tr_b16 v[38:39], v3 offset:10240\n\t"
    "v_mfma_f32_16x16x32_bf16 v[132:135], v[24:27], v[180:183], v[132:135]\n\t"
    "v_add_u32 v2, 0xfffee000, v2\n\t"
    "ds_read_b64_tr_b16 v[24:25], v2 offset:0\n\t"
    "ds_read_b64_tr_b16 v[26:27], v2 offset:2048\n\t"
    "s_waitcnt lgkmcnt(6)\n\t"
    "v_mfma_f32_16x16x32_bf16 v[128:131], v[28:31], v[184:187], v[128:131]\n\t"
    "v_mfma_f32_16x16x32_bf16 v[132:135], v[28:31], v[188:191], v[132:135]\n\t"
    "v_add_u32 v2, 0x18000, v2\n\t"
    "ds_read_b64_tr_b16 v[28:29], v2 offset:24576\n\t"
    "s_setprio 2\n\t"
    "s_waitcnt lgkmcnt(5)\n\t"
    "v_mfma_f32_16x16x32_bf16 v[128:131], v[32:35], v[192:195], v[128:131]\n\t"
    "ds_read_b64_tr_b16 v[30:31], v2 offset:26624\n\t"
    "v_mfma_f32_16x16x32_bf16 v[132:135], v[32:35], v[196:199], v[132:135]\n\t"
    "v_add_u32 v2, 0xfffea000, v2\n\t"
    "ds_read_b64_tr_b16 v[32:33], v2 offset:0\n\t"
    "s_waitcnt lgkmcnt(5)\n\t"
    "v_mfma_f32_16x16x32_bf16 v[128:131], v[36:39], v[224:227], v[128:131]\n\t"
    "ds_read_b64_tr_b16 v[34:35], v2 offset:2048\n\t"
    "v_mfma_f32_16x16x32_bf16 v[132:135], v[36:39], v[228:231], v[132:135]\n\t"
    "ds_read_b64_tr_b16 v[36:37], v2 offset:8192\n\t"
    "s_setprio 1\n\t"
    "s_waitcnt lgkmcnt(5)\n\t"
    "v_mfma_f32_16x16x32_bf16 v[144:147], v[24:27], v[176:179], v[144:147]\n\t"
    "ds_read_b64_tr_b16 v[38:39], v2 offset:10240\n\t"
    "v_mfma_f32_16x16x32_bf16 v[148:151], v[24:27], v[180:183], v[148:151]\n\t"
    "ds_read_b64_tr_b16 v[24:25], v1 offset:16384\n\t"
    "ds_write_b128 v4, v[128:131] offset:49152\n\t"
    "s_waitcnt lgkmcnt(6)\n\t"
    "v_mfma_f32_16x16x32_bf16 v[144:147], v[28:31], v[184:187], v[144:147]\n\t"
    "ds_read_b64_tr_b16 v[26:27], v1 offset:18432\n\t"
    "v_mfma_f32_16x16x32_bf16 v[148:151], v[28:31], v[188:191], v[148:151]\n\t"
    "ds_read_b64_tr_b16 v[28:29], v1 offset:24576\n\t"
    "ds_write_b128 v4, v[132:135] offset:50176\n\t"
    "s_setprio 0\n\t"
    "s_waitcnt lgkmcnt(7)\n\t"
    "v_mfma_f32_16x16x32_bf16 v[144:147], v[32:35], v[192:195], v[144:147]\n\t"
    "ds_read_b64_tr_b16 v[30:31], v1 offset:26624\n\t"
    "v_mfma_f32_16x16x32_bf16 v[148:151], v[32:35], v[196:199], v[148:151]\n\t"
    "ds_read_b64_tr_b16 v[32:33], v1 offset:32768\n\t"
    "s_waitcnt lgkmcnt(7)\n\t"
    "v_mfma_f32_16x16x32_bf16 v[144:147], v[36:39], v[224:227], v[144:147]\n\t"
    "ds_read_b64_tr_b16 v[34:35], v1 offset:34816\n\t"
    "v_mfma_f32_16x16x32_bf16 v[148:151], v[36:39], v[228:231], v[148:151]\n\t"
    "ds_read_b64_tr_b16 v[36:37], v1 offset:40960\n\t"
    "ds_read_b64_tr_b16 v[38:39], v1 offset:43008\n\t"
    "s_waitcnt lgkmcnt(5)\n\t"
    "s_barrier\n\t"
    "s_setprio 3\n\t"
    "v_mfma_f32_16x16x32_bf16 v[120:123], v[24:27], v[200:203], v[120:123]\n\t"
    "ds_read_b128 v[128:131], v8 offset:49152\n\t"
    "v_mfma_f32_16x16x32_bf16 v[124:127], v[24:27], v[204:207], v[124:127]\n\t"
    "ds_read_b128 v[132:135], v8 offset:50176\n\t"
    "s_add_u32 s85, s64, 24576\n\t"
    "buffer_load_dwordx4 v[80:83], v6, s[28:31], s85 offen sc1\n\t"
    "ds_read_b64_tr_b16 v[24:25], v1 offset:49152\n\t"
    "ds_read_b64_tr_b16 v[26:27], v1 offset:51200\n\t"
    "s_waitcnt lgkmcnt(8)\n\t"
    "v_mfma_f32_16x16x32_bf16 v[120:123], v[28:31], v[208:211], v[120:123]\n\t"
    "s_waitcnt lgkmcnt(3)\n\t"
    "v_add_f32 v144, v144, v128\n\t"
    "v_add_f32 v145, v145, v129\n\t"
    "v_add_f32 v146, v146, v130\n\t"
    "v_add_f32 v147, v147, v131\n\t"
    "v_mfma_f32_16x16x32_bf16 v[124:127], v[28:31], v[212:215], v[124:127]\n\t"
    "s_waitcnt lgkmcnt(2)\n\t"
    "v_add_f32 v148, v148, v132\n\t"
    "v_add_f32 v149, v149, v133\n\t"
    "v_add_f32 v150, v150, v134\n\t"
    "v_add_f32 v151, v151, v135\n\t"
    "s_add_u32 s85, s64, 24576\n\t"
    "buffer_load_dword v84, v7, s[28:31], s85 offen offset:1024 sc1\n\t"
    "ds_read_b64_tr_b16 v[28:29], v1 offset:57344\n\t"
    "ds_read_b64_tr_b16 v[30:31], v1 offset:59392\n\t"
    "v_mfma_f32_16x16x32_bf16 v[120:123], v[32:35], v[40:43], v[120:123]\n\t"
    "v_maximum3_f32 v128, |v144|, |v145|, |v146|\n\t"
    "v_maximum3_f32 v129, |v147|, |v148|, |v149|\n\t"
    "v_maximum3_f32 v128, v128, |v150|, |v151|\n\t"
    "v_maximum3_f32 v128, v128, v129, v129\n\t"
    "v_bfe_u32 v129, v128, 23, 8\n\t"
    "v_mfma_f32_16x16x32_bf16 v[124:127], v[32:35], v[44:47], v[124:127]\n\t"
    "v_sub_u32 v128, 0x7e, v129\n\t"
    "v_ldexp_f32 v144, v144, v128\n\t"
    "v_ldexp_f32 v145, v145, v128\n\t"
    "v_cvt_pknorm_i16_f32 v144, v144, v145\n\t"
    "v_ldexp_f32 v146, v146, v128\n\t"
    "s_add_u32 s85, s64, 49152\n\t"
    "buffer_load_dwordx4 v[88:91], v6, s[28:31], s85 offen sc1\n\t"
    "v_add_u32 v1, 0x10000, v1\n\t"
    "ds_read_b64_tr_b16 v[32:33], v1 offset:0\n\t"
    "ds_read_b64_tr_b16 v[34:35], v1 offset:2048\n\t"
    "v_mfma_f32_16x16x32_bf16 v[120:123], v[36:39], v[48:51], v[120:123]\n\t"
    "v_ldexp_f32 v147, v147, v128\n\t"
    "v_cvt_pknorm_i16_f32 v145, v146, v147\n\t"
    "v_ldexp_f32 v148, v148, v128\n\t"
    "v_ldexp_f32 v149, v149, v128\n\t"
    "v_cvt_pknorm_i16_f32 v146, v148, v149\n\t"
    "v_mfma_f32_16x16x32_bf16 v[124:127], v[36:39], v[52:55], v[124:127]\n\t"
    "v_ldexp_f32 v150, v150, v128\n\t"
    "v_ldexp_f32 v151, v151, v128\n\t"
    "v_cvt_pknorm_i16_f32 v147, v150, v151\n\t"
    "buffer_store_dwordx4 v[144:147], v6, s[28:31], s69 offen sc1\n\t"
    "v_add3_u32 v132, v144, v145, v146\n\t"
    "s_add_u32 s85, s64, 49152\n\t"
    "buffer_load_dword v92, v7, s[28:31], s85 offen offset:1024 sc1\n\t"
    "ds_read_b64_tr_b16 v[36:37], v1 offset:8192\n\t"
    "ds_read_b64_tr_b16 v[38:39], v1 offset:10240\n\t"
    "s_waitcnt lgkmcnt(6)\n\t"
    "v_mfma_f32_16x16x32_bf16 v[120:123], v[24:27], v[56:59], v[120:123]\n\t"
    "v_add3_u32 v132, v132, v147, s55\n\t"
    "v_bfi_b32 v132, s3, v129, v132\n\t"
    "buffer_store_dword v132, v7, s[28:31], s69 offen offset:1024 sc1\n\t"
    "v_mfma_f32_16x16x32_bf16 v[124:127], v[24:27], v[60:63], v[124:127]\n\t"
    "ds_read_b64_tr_b16 v[24:25], v1 offset:16384\n\t"
    "s_waitcnt lgkmcnt(5)\n\t"
    "v_mfma_f32_16x16x32_bf16 v[120:123], v[28:31], v[216:219], v[120:123]\n\t"
    "ds_read_b64_tr_b16 v[26:27], v1 offset:18432\n\t"
    "v_mfma_f32_16x16x32_bf16 v[124:127], v[28:31], v[220:223], v[124:127]\n\t"
    "ds_read_b64_tr_b16 v[28:29], v1 offset:24576\n\t"
    "s_waitcnt lgkmcnt(5)\n\t"
    "v_mfma_f32_16x16x32_bf16 v[120:123], v[32:35], v[64:67], v[120:123]\n\t"
    "ds_read_b64_tr_b16 v[30:31], v1 offset:26624\n\t"
    "v_mfma_f32_16x16x32_bf16 v[124:127], v[32:35], v[68:71], v[124:127]\n\t"
    "ds_read_b64_tr_b16 v[32:33], v1 offset:32768\n\t"
    "s_waitcnt lgkmcnt(5)\n\t"
    "v_mfma_f32_16x16x32_bf16 v[120:123], v[36:39], v[72:75], v[120:123]\n\t"
    "ds_read_b64_tr_b16 v[34:35], v1 offset:34816\n\t"
    "v_mfma_f32_16x16x32_bf16 v[124:127], v[36:39], v[76:79], v[124:127]\n\t"
    "ds_read_b64_tr_b16 v[36:37], v1 offset:40960\n\t"
    "s_waitcnt lgkmcnt(5)\n\t"
    "v_mfma_f32_16x16x32_bf16 v[120:123], v[24:27], v[112:115], v[120:123]\n\t"
    "ds_read_b64_tr_b16 v[38:39], v1 offset:43008\n\t"
    "v_mfma_f32_16x16x32_bf16 v[124:127], v[24:27], v[116:119], v[124:127]\n\t"
    "v_add_u32 v1, 0xfffee000, v1\n\t"
    "ds_read_b64_tr_b16 v[24:25], v1 offset:0\n\t"
    "s_waitcnt lgkmcnt(5)\n\t"
    "v_mfma_f32_16x16x32_bf16 v[120:123], v[28:31], v[152:155], v[120:123]\n\t"
    "ds_read_b64_tr_b16 v[26:27], v1 offset:2048\n\t"
    "v_mfma_f32_16x16x32_bf16 v[124:127], v[28:31], v[156:159], v[124:127]\n\t"
    "v_add_u32 v1, 0x18000, v1\n\t"
    "ds_read_b64_tr_b16 v[28:29], v1 offset:24576\n\t"
    "s_waitcnt lgkmcnt(5)\n\t"
    "v_mfma_f32_16x16x32_bf16 v[120:123], v[32:35], v[160:163], v[120:123]\n\t"
    "ds_read_b64_tr_b16 v[30:31], v1 offset:26624\n\t"
    "v_mfma_f32_16x16x32_bf16 v[124:127], v[32:35], v[164:167], v[124:127]\n\t"
    "v_add_u32 v1, 0xfffea000, v1\n\t"
    "ds_read_b64_tr_b16 v[32:33], v1 offset:0\n\t"
    "s_waitcnt lgkmcnt(5)\n\t"
    "v_mfma_f32_16x16x32_bf16 v[120:123], v[36:39], v[168:171], v[120:123]\n\t"
    "ds_read_b64_tr_b16 v[34:35], v1 offset:2048\n\t"
    "v_mfma_f32_16x16x32_bf16 v[124:127], v[36:39], v[172:175], v[124:127]\n\t"
    "ds_read_b64_tr_b16 v[36:37], v1 offset:8192\n\t"
    "s_waitcnt lgkmcnt(5)\n\t"
    "v_mfma_f32_16x16x32_bf16 v[120:123], v[24:27], v[176:179], v[120:123]\n\t"
    "ds_read_b64_tr_b16 v[38:39], v1 offset:10240\n\t"
    "v_mfma_f32_16x16x32_bf16 v[124:127], v[24:27], v[180:183], v[124:127]\n\t"
    "ds_read_b64_tr_b16 v[24:25], v0 offset:16384\n\t"
    "s_waitcnt lgkmcnt(5)\n\t"
    "v_mfma_f32_16x16x32_bf16 v[120:123], v[28:31], v[184:187], v[120:123]\n\t"
    "ds_read_b64_tr_b16 v[26:27], v0 offset:18432\n\t"
    "v_mfma_f32_16x16x32_bf16 v[124:127], v[28:31], v[188:191], v[124:127]\n\t"
    "ds_read_b64_tr_b16 v[28:29], v0 offset:24576\n\t"
    "s_waitcnt lgkmcnt(5)\n\t"
    "v_mfma_f32_16x16x32_bf16 v[120:123], v[32:35], v[192:195], v[120:123]\n\t"
    "ds_read_b64_tr_b16 v[30:31], v0 offset:26624\n\t"
    "v_mfma_f32_16x16x32_bf16 v[124:127], v[32:35], v[196:199], v[124:127]\n\t"
    "ds_read_b64_tr_b16 v[32:33], v0 offset:32768\n\t"
    "s_waitcnt lgkmcnt(5)\n\t"
    "v_mfma_f32_16x16x32_bf16 v[120:123], v[36:39], v[224:227], v[120:123]\n\t"
    "ds_read_b64_tr_b16 v[34:35], v0 offset:34816\n\t"
    "v_mfma_f32_16x16x32_bf16 v[124:127], v[36:39], v[228:231], v[124:127]\n\t"
    "ds_read_b64_tr_b16 v[36:37], v0 offset:40960\n\t"
    "s_waitcnt lgkmcnt(5)\n\t"
    "v_mfma_f32_16x16x32_bf16 v[136:139], v[24:27], v[200:203], v[136:139]\n\t"
    "ds_read_b64_tr_b16 v[38:39], v0 offset:43008\n\t"
    "v_mfma_f32_16x16x32_bf16 v[140:143], v[24:27], v[204:207], v[140:143]\n\t"
    "s_nop 0\n\t"
    "ds_write_b128 v4, v[120:123] offset:32768\n\t"
    "ds_read_b64_tr_b16 v[24:25], v0 offset:49152\n\t"
    "s_setprio 2\n\t"
    "s_waitcnt lgkmcnt(6)\n\t"
    "v_mfma_f32_16x16x32_bf16 v[136:139], v[28:31], v[208:211], v[136:139]\n\t"
    "ds_read_b64_tr_b16 v[26:27], v0 offset:51200\n\t"
    "v_mfma_f32_16x16x32_bf16 v[140:143], v[28:31], v[212:215], v[140:143]\n\t"
    "ds_write_b128 v4, v[124:127] offset:33792\n\t"
    "ds_read_b64_tr_b16 v[28:29], v0 offset:57344\n\t"
    "s_setprio 1\n\t"
    "s_waitcnt lgkmcnt(7)\n\t"
    "v_mfma_f32_16x16x32_bf16 v[136:139], v[32:35], v[40:43], v[136:139]\n\t"
    "ds_read_b64_tr_b16 v[30:31], v0 offset:59392\n\t"
    "v_mfma_f32_16x16x32_bf16 v[140:143], v[32:35], v[44:47], v[140:143]\n\t"
    "v_add_u32 v0, 0x10000, v0\n\t"
    "ds_read_b64_tr_b16 v[32:33], v0 offset:0\n\t"
    "s_setprio 0\n\t"
    "s_waitcnt lgkmcnt(7)\n\t"
    "v_mfma_f32_16x16x32_bf16 v[136:139], v[36:39], v[48:51], v[136:139]\n\t"
    "ds_read_b64_tr_b16 v[34:35], v0 offset:2048\n\t"
    "v_mfma_f32_16x16x32_bf16 v[140:143], v[36:39], v[52:55], v[140:143]\n\t"
    "ds_read_b64_tr_b16 v[36:37], v0 offset:8192\n\t"
    "ds_read_b64_tr_b16 v[38:39], v0 offset:10240\n\t"
    "s_waitcnt lgkmcnt(6)\n\t"
    "s_barrier\n\t"
    "s_setprio 3\n\t"
    "v_mfma_f32_16x16x32_bf16 v[136:139], v[24:27], v[56:59], v[136:139]\n\t"
    "ds_read_b128 v[120:123], v8 offset:32768\n\t"
    "s_waitcnt vmcnt(5)\n\t"
    "v_add3_u32 v85, v80, v81, v82\n\t"
    "v_add3_u32 v85, v85, v83, s1\n\t"
    "s_waitcnt vmcnt(4)\n\t"
    "v_xor_b32 v12, v85, v84\n\t"
    "s_waitcnt vmcnt(3)\n\t"
    "v_add3_u32 v93, v88, v89, v90\n\t"
    "v_add3_u32 v93, v93, v91, s2\n\t"
    "s_waitcnt vmcnt(1)\n\t"
    "v_xor_b32 v93, v93, v92\n\t"
    "v_or_b32 v12, v12, v93\n\t"
    "v_cmp_lt_u32 vcc, s3, v12\n\t"
    "s_cbranch_vccnz LA27_%=\n\t"
    "LA28_%=:\n\t"
    "v_mfma_f32_16x16x32_bf16 v[140:143], v[24:27], v[60:63], v[140:143]\n\t"
    "ds_read_b64_tr_b16 v[24:25], v0 offset:16384\n\t"
    "ds_read_b128 v[124:127], v8 offset:33792\n\t"
    "s_add_u32 s85, s64, 0\n\t"
    "buffer_load_dwordx4 v[40:43], v6, s[28:31], s85 offen sc1\n\t"
    "s_waitcnt lgkmcnt(7)\n\t"
    "v_mfma_f32_16x16x32_bf16 v[136:139], v[28:31], v[216:219], v[136:139]\n\t"
    "ds_read_b64_tr_b16 v[26:27], v0 offset:18432\n\t"
    "v_mfma_f32_16x16x32_bf16 v[140:143], v[28:31], v[220:223], v[140:143]\n\t"
    "ds_read_b64_tr_b16 v[28:29], v0 offset:24576\n\t"
    "v_lshlrev_b32 v84, 23, v84\n\t"
    "v_mul_f32 v84, s4, v84\n\t"
    "v_cvt_f32_i32_sdwa v86, sext(v80) dst_sel:DWORD dst_unused:UNUSED_PAD src0_sel:WORD_0\n\t"
    "v_cvt_f32_i32_sdwa v87, sext(v80) dst_sel:DWORD dst_unused:UNUSED_PAD src0_sel:WORD_1\n\t"
    "s_add_u32 s85, s64, 0\n\t"
    "buffer_load_dword v44, v7, s[28:31], s85 offen offset:1024 sc1\n\t"
    "s_waitcnt lgkmcnt(7)\n\t"
    "v_mfma_f32_16x16x32_bf16 v[136:139], v[32:35], v[64:67], v[136:139]\n\t"
    "ds_read_b64_tr_b16 v[30:31], v0 offset:26624\n\t"
    "s_waitcnt lgkmcnt(3)\n\t"
    "v_fmac_f32 v124, v86, v84\n\t"
    "v_fmac_f32 v125, v87, v84\n\t"
    "v_cvt_f32_i32_sdwa v86, sext(v81) dst_sel:DWORD dst_unused:UNUSED_PAD src0_sel:WORD_0\n\t"
    "v_cvt_f32_i32_sdwa v87, sext(v81) dst_sel:DWORD dst_unused:UNUSED_PAD src0_sel:WORD_1\n\t"
    "v_mfma_f32_16x16x32_bf16 v[140:143], v[32:35], v[68:71], v[140:143]\n\t"
    "ds_read_b64_tr_b16 v[32:33], v0 offset:32768\n\t"
    "v_fmac_f32 v126, v86, v84\n\t"
    "v_fmac_f32 v127, v87, v84\n\t"
    "v_cvt_f32_i32_sdwa v86, sext(v82) dst_sel:DWORD dst_unused:UNUSED_PAD src0_sel:WORD_0\n\t"
    "v_cvt_f32_i32_sdwa v87, sext(v82) dst_sel:DWORD dst_unused:UNUSED_PAD src0_sel:WORD_1\n\t"
    "v_mfma_f32_16x16x32_bf16 v[136:139], v[36:39], v[72:75], v[136:139]\n\t"
    "ds_read_b64_tr_b16 v[34:35], v0 offset:34816\n\t"
    "v_fmac_f32 v120, v86, v84\n\t"
    "v_fmac_f32 v121, v87, v84\n\t"
    "v_cvt_f32_i32_sdwa v86, sext(v83) dst_sel:DWORD dst_unused:UNUSED_PAD src0_sel:WORD_0\n\t"
    "v_cvt_f32_i32_sdwa v87, sext(v83) dst_sel:DWORD dst_unused:UNUSED_PAD src0_sel:WORD_1\n\t"
    "v_mfma_f32_16x16x32_bf16 v[140:143], v[36:39], v[76:79], v[140:143]\n\t"
    "ds_read_b64_tr_b16 v[36:37], v0 offset:40960\n\t"
    "v_fmac_f32 v122, v86, v84\n\t"
    "v_fmac_f32 v123, v87, v84\n\t"
    "v_lshlrev_b32 v92, 23, v92\n\t"
    "v_mul_f32 v92, s4, v92\n\t"
    "s_waitcnt lgkmcnt(5)\n\t"
    "v_mfma_f32_16x16x32_bf16 v[136:139], v[24:27], v[112:115], v[136:139]\n\t"
    "ds_read_b64_tr_b16 v[38:39], v0 offset:43008\n\t"
    "v_cvt_f32_i32_sdwa v94, sext(v88) dst_sel:DWORD dst_unused:UNUSED_PAD src0_sel:WORD_0\n\t"
    "v_cvt_f32_i32_sdwa v95, sext(v88) dst_sel:DWORD dst_unused:UNUSED_PAD src0_sel:WORD_1\n\t"
    "v_fmac_f32 v124, v94, v92\n\t"
    "v_fmac_f32 v125, v95, v92\n\t"
    "v_mfma_f32_16x16x32_bf16 v[140:143], v[24:27], v[116:119], v[140:143]\n\t"
    "v_cvt_f32_i32_sdwa v94, sext(v89) dst_sel:DWORD dst_unused:UNUSED_PAD src0_sel:WORD_0\n\t"
    "v_cvt_f32_i32_sdwa v95, sext(v89) dst_sel:DWORD dst_unused:UNUSED_PAD src0_sel:WORD_1\n\t"
    "v_fmac_f32 v126, v94, v92\n\t"
    "v_fmac_f32 v127, v95, v92\n\t"
    "v_add_u32 v0, 0xfffee000, v0\n\t"
    "ds_read_b64_tr_b16 v[24:25], v0 offset:0\n\t"
    "s_waitcnt lgkmcnt(5)\n\t"
    "v_mfma_f32_16x16x32_bf16 v[136:139], v[28:31], v[152:155], v[136:139]\n\t"
    "ds_read_b64_tr_b16 v[26:27], v0 offset:2048\n\t"
    "v_cvt_f32_i32_sdwa v94, sext(v90) dst_sel:DWORD dst_unused:UNUSED_PAD src0_sel:WORD_0\n\t"
    "v_cvt_f32_i32_sdwa v95, sext(v90) dst_sel:DWORD dst_unused:UNUSED_PAD src0_sel:WORD_1\n\t"
    "v_fmac_f32 v120, v94, v92\n\t"
    "v_fmac_f32 v121, v95, v92\n\t"
    "v_mfma_f32_16x16x32_bf16 v[140:143], v[28:31], v[156:159], v[140:143]\n\t"
    "v_cvt_f32_i32_sdwa v94, sext(v91) dst_sel:DWORD dst_unused:UNUSED_PAD src0_sel:WORD_0\n\t"
    "v_cvt_f32_i32_sdwa v95, sext(v91) dst_sel:DWORD dst_unused:UNUSED_PAD src0_sel:WORD_1\n\t"
    "v_fmac_f32 v122, v94, v92\n\t"
    "v_fmac_f32 v123, v95, v92\n\t"
    "v_add_u32 v0, 0x18000, v0\n\t"
    "ds_read_b64_tr_b16 v[28:29], v0 offset:24576\n\t"
    "s_setprio 2\n\t"
    "s_waitcnt lgkmcnt(5)\n\t"
    "v_mfma_f32_16x16x32_bf16 v[136:139], v[32:35], v[160:163], v[136:139]\n\t"
    "ds_read_b64_tr_b16 v[30:31], v0 offset:26624\n\t"
    "v_mfma_f32_16x16x32_bf16 v[140:143], v[32:35], v[164:167], v[140:143]\n\t"
    "v_add_u32 v0, 0xfffea000, v0\n\t"
    "ds_read_b64_tr_b16 v[32:33], v0 offset:0\n\t"
    "s_waitcnt lgkmcnt(5)\n\t"
    "v_mfma_f32_16x16x32_bf16 v[136:139], v[36:39], v[168:171], v[136:139]\n\t"
    "ds_read_b64_tr_b16 v[34:35], v0 offset:2048\n\t"
    "v_mfma_f32_16x16x32_bf16 v[140:143], v[36:39], v[172:175], v[140:143]\n\t"
    "ds_read_b64_tr_b16 v[36:37], v0 offset:8192\n\t"
    "s_setprio 1\n\t"
    "s_waitcnt lgkmcnt(5)\n\t"
    "v_mfma_f32_16x16x32_bf16 v[136:139], v[24:27], v[176:179], v[136:139]\n\t"
    "ds_read_b64_tr_b16 v[38:39], v0 offset:10240\n\t"
    "v_mfma_f32_16x16x32_bf16 v[140:143], v[24:27], v[180:183], v[140:143]\n\t"
    "s_waitcnt lgkmcnt(4)\n\t"
    "v_mfma_f32_16x16x32_bf16 v[136:139], v[28:31], v[184:187], v[136:139]\n\t"
    "v_mfma_f32_16x16x32_bf16 v[140:143], v[28:31], v[188:191], v[140:143]\n\t"
    "s_setprio 0\n\t"
    "s_waitcnt lgkmcnt(2)\n\t"
    "v_mfma_f32_16x16x32_bf16 v[136:139], v[32:35], v[192:195], v[136:139]\n\t"
    "v_mfma_f32_16x16x32_bf16 v[140:143], v[32:35], v[196:199], v[140:143]\n\t"
    "s_waitcnt lgkmcnt(0)\n\t"
    "v_mfma_f32_16x16x32_bf16 v[136:139], v[36:39], v[224:227], v[136:139]\n\t"
    "v_mfma_f32_16x16x32_bf16 v[140:143], v[36:39], v[228:231], v[140:143]\n\t"
    "s_nop 6\n\t"
    "v_add_f32 v120, v120, v136\n\t"
    "v_add_f32 v121, v121, v137\n\t"
    "v_add_f32 v122, v122, v138\n\t"
    "v_add_f32 v123, v123, v139\n\t"
    "v_add_f32 v124, v124, v140\n\t"
    "v_add_f32 v125, v125, v141\n\t"
    "v_add_f32 v126, v126, v142\n\t"
    "v_add_f32 v127, v127, v143\n\t"
    "s_waitcnt vmcnt(1)\n\t"
    "v_add3_u32 v45, v40, v41, v42\n\t"
    "v_add3_u32 v45, v45, v43, s0\n\t"
    "s_waitcnt vmcnt(0)\n\t"
    "v_xor_b32 v12, v45, v44\n\t"
    "v_cmp_lt_u32 vcc, s3, v12\n\t"
    "s_cbranch_vccnz LA29_%=\n\t"
    "LA30_%=:\n\t"
    "v_lshlrev_b32 v44, 23, v44\n\t"
    "v_mul_f32 v44, s4, v44\n\t"
    "v_cvt_f32_i32_sdwa v46, sext(v40) dst_sel:DWORD dst_unused:UNUSED_PAD src0_sel:WORD_0\n\t"
    "v_cvt_f32_i32_sdwa v47, sext(v40) dst_sel:DWORD dst_unused:UNUSED_PAD src0_sel:WORD_1\n\t"
    "v_fmac_f32 v120, v46, v44\n\t"
    "v_fmac_f32 v121, v47, v44\n\t"
    "v_cvt_f32_i32_sdwa v46, sext(v41) dst_sel:DWORD dst_unused:UNUSED_PAD src0_sel:WORD_0\n\t"
    "v_cvt_f32_i32_sdwa v47, sext(v41) dst_sel:DWORD dst_unused:UNUSED_PAD src0_sel:WORD_1\n\t"
    "v_fmac_f32 v122, v46, v44\n\t"
    "v_fmac_f32 v123, v47, v44\n\t"
    "v_cvt_f32_i32_sdwa v46, sext(v42) dst_sel:DWORD dst_unused:UNUSED_PAD src0_sel:WORD_0\n\t"
    "v_cvt_f32_i32_sdwa v47, sext(v42) dst_sel:DWORD dst_unused:UNUSED_PAD src0_sel:WORD_1\n\t"
    "v_fmac_f32 v124, v46, v44\n\t"
    "v_fmac_f32 v125, v47, v44\n\t"
    "v_cvt_f32_i32_sdwa v46, sext(v43) dst_sel:DWORD dst_unused:UNUSED_PAD src0_sel:WORD_0\n\t"
    "v_cvt_f32_i32_sdwa v47, sext(v43) dst_sel:DWORD dst_unused:UNUSED_PAD src0_sel:WORD_1\n\t"
    "v_fmac_f32 v126, v46, v44\n\t"
    "v_fmac_f32 v127, v47, v44\n\t"
    "v_cvt_pk_bf16_f32 v120, v120, v121\n\t"
    "v_cvt_pk_bf16_f32 v121, v122, v123\n\t"
    "buffer_store_dwordx2 v[120:121], v9, s[32:35], s62 offen\n\t"
    "v_cvt_pk_bf16_f32 v124, v124, v125\n\t"
    "v_cvt_pk_bf16_f32 v125, v126, v127\n\t"
    "buffer_store_dwordx2 v[124:125], v9, s[32:35], s67 offen\n\t"
    "s_cmp_lg_u32 s89, 0\n\t"
    "s_cbranch_scc1 LA31_%=\n\t"
    "s_add_u32 s84, s56, 1\n\t"
    "v_mov_b32 v12, s84\n\t"
    "s_mov_b64 exec, 1\n\t"
    "buffer_store_dword v12, off, s[28:31], s95 sc1\n\t"
    "s_mov_b64 exec, -1\n\t"
    "LA31_%=:\n\t"
    "s_branch LEND_%=\n\t"
    "LA1_%=:\n\t"
    "s_cmp_eq_u32 s63, 0\n\t"
    "s_cbranch_scc1 LA2_%=\n\t"
    "s_sub_u32 s63, s63, 1\n\t"
    "s_sleep 1\n\t"
    "s_mov_b32 s85, s9\n\t"
    "buffer_load_dwordx4 v[40:43], v6, s[28:31], s85 offen sc1\n\t"
    "buffer_load_dwordx4 v[44:47], v6, s[28:31], s85 offen offset:1024 sc1\n\t"
    "buffer_load_dword v16, v7, s[28:31], s85 offen offset:2048 sc1\n\t"
    "s_waitcnt vmcnt(0)\n\t"
    "v_add3_u32 v16, v40, v41, v16\n\t"
    "v_add3_u32 v16, v42, v43, v16\n\t"
    "v_add3_u32 v16, v44, v45, v16\n\t"
    "v_add3_u32 v16, v46, v47, v16\n\t"
    "v_cmp_ne_u32 vcc, s51, v16\n\t"
    "s_cbranch_vccnz LA1_%=\n\t"
    "s_nop 4\n\t"
    "s_branch LA2_%=\n\t"
    "LA3_%=:\n\t"
    "s_cmp_eq_u32 s63, 0\n\t"
    "s_cbranch_scc1 LA4_%=\n\t"
    "s_sub_u32 s63, s63, 1\n\t"
    "s_sleep 1\n\t"
    "s_mov_b32 s85, s10\n\t"
    "buffer_load_dwordx4 v[48:51], v6, s[28:31], s85 offen sc1\n\t"
    "buffer_load_dwordx4 v[52:55], v6, s[28:31], s85 offen offset:1024 sc1\n\t"
    "buffer_load_dword v17, v7, s[28:31], s85 offen offset:2048 sc1\n\t"
    "s_waitcnt vmcnt(0)\n\t"
    "v_add3_u32 v17, v48, v49, v17\n\t"
    "v_add3_u32 v17, v50, v51, v17\n\t"
    "v_add3_u32 v17, v52, v53, v17\n\t"
    "v_add3_u32 v17, v54, v55, v17\n\t"
    "v_cmp_ne_u32 vcc, s51, v17\n\t"
    "s_cbranch_vccnz LA3_%=\n\t"
    "s_nop 4\n\t"
    "s_branch LA4_%=\n\t"
    "LA5_%=:\n\t"
    "s_cmp_eq_u32 s63, 0\n\t"
    "s_cbranch_scc1 LA6_%=\n\t"
    "s_sub_u32 s63, s63, 1\n\t"
    "s_sleep 1\n\t"
    "s_mov_b32 s85, s97\n\t"
    "buffer_load_dwordx4 v[56:59], v6, s[28:31], s85 offen sc1\n\t"
    "buffer_load_dwordx4 v[60:63], v6, s[28:31], s85 offen offset:1024 sc1\n\t"
    "buffer_load_dword v18, v7, s[28:31], s85 offen offset:2048 sc1\n\t"
    "s_waitcnt vmcnt(0)\n\t"
    "v_add3_u32 v18, v56, v57, v18\n\t"
    "v_add3_u32 v18, v58, v59, v18\n\t"
    "v_add3_u32 v18, v60, v61, v18\n\t"
    "v_add3_u32 v18, v62, v63, v18\n\t"
    "v_cmp_ne_u32 vcc, s51, v18\n\t"
    "s_cbranch_vccnz LA5_%=\n\t"
    "s_nop 4\n\t"
    "s_branch LA6_%=\n\t"
    "LA7_%=:\n\t"
    "s_cmp_eq_u32 s63, 0\n\t"
    "s_cbranch_scc1 LA8_%=\n\t"
    "s_sub_u32 s63, s63, 1\n\t"
    "s_sleep 1\n\t"
    "s_add_u32 s85, s44, 0x0\n\t"
    "buffer_load_dwordx4 v[64:67], v6, s[28:31], s85 offen sc1\n\t"
    "buffer_load_dwordx4 v[68:71], v6, s[28:31], s85 offen offset:1024 sc1\n\t"
    "buffer_load_dword v19, v7, s[28:31], s85 offen offset:2048 sc1\n\t"
    "s_waitcnt vmcnt(0)\n\t"
    "v_add3_u32 v19, v64, v65, v19\n\t"
    "v_add3_u32 v19, v66, v67, v19\n\t"
    "v_add3_u32 v19, v68, v69, v19\n\t"
    "v_add3_u32 v19, v70, v71, v19\n\t"
    "v_cmp_ne_u32 vcc, s49, v19\n\t"
    "s_cbranch_vccnz LA7_%=\n\t"
    "s_nop 4\n\t"
    "s_branch LA8_%=\n\t"
    "LA9_%=:\n\t"
    "s_cmp_eq_u32 s63, 0\n\t"
    "s_cbranch_scc1 LA10_%=\n\t"
    "s_sub_u32 s63, s63, 1\n\t"
    "s_sleep 1\n\t"
    "s_add_u32 s85, s45, 0x0\n\t"
    "buffer_load_dwordx4 v[72:75], v6, s[28:31], s85 offen sc1\n\t"
    "buffer_load_dwordx4 v[76:79], v6, s[28:31], s85 offen offset:1024 sc1\n\t"
    "buffer_load_dword v16, v7, s[28:31], s85 offen offset:2048 sc1\n\t"
    "s_waitcnt vmcnt(0)\n\t"
    "v_add3_u32 v16, v72, v73, v16\n\t"
    "v_add3_u32 v16, v74, v75, v16\n\t"
    "v_add3_u32 v16, v76, v77, v16\n\t"
    "v_add3_u32 v16, v78, v79, v16\n\t"
    "v_cmp_ne_u32 vcc, s50, v16\n\t"
    "s_cbranch_vccnz LA9_%=\n\t"
    "s_nop 4\n\t"
    "s_branch LA10_%=\n\t"
    "LA11_%=:\n\t"
    "s_cmp_eq_u32 s63, 0\n\t"
    "s_cbranch_scc1 LA12_%=\n\t"
    "s_sub_u32 s63, s63, 1\n\t"
    "s_sleep 1\n\t"
    "s_add_u32 s85, s44, 0x4800\n\t"
    "buffer_load_dwordx4 v[112:115], v6, s[28:31], s85 offen sc1\n\t"
    "buffer_load_dwordx4 v[116:119], v6, s[28:31], s85 offen offset:1024 sc1\n\t"
    "buffer_load_dword v17, v7, s[28:31], s85 offen offset:2048 sc1\n\t"
    "s_waitcnt vmcnt(0)\n\t"
    "v_add3_u32 v17, v112, v113, v17\n\t"
    "v_add3_u32 v17, v114, v115, v17\n\t"
    "v_add3_u32 v17, v116, v117, v17\n\t"
    "v_add3_u32 v17, v118, v119, v17\n\t"
    "v_cmp_ne_u32 vcc, s49, v17\n\t"
    "s_cbranch_vccnz LA11_%=\n\t"
    "s_nop 4\n\t"
    "s_branch LA12_%=\n\t"
    "LA13_%=:\n\t"
    "s_cmp_eq_u32 s63, 0\n\t"
    "s_cbranch_scc1 LA14_%=\n\t"
    "s_sub_u32 s63, s63, 1\n\t"
    "s_sleep 1\n\t"
    "s_add_u32 s85, s45, 0x4800\n\t"
    "buffer_load_dwordx4 v[152:155], v6, s[28:31], s85 offen sc1\n\t"
    "buffer_load_dwordx4 v[156:159], v6, s[28:31], s85 offen offset:1024 sc1\n\t"
    "buffer_load_dword v18, v7, s[28:31], s85 offen offset:2048 sc1\n\t"
    "s_waitcnt vmcnt(0)\n\t"
    "v_add3_u32 v18, v152, v153, v18\n\t"
    "v_add3_u32 v18, v154, v155, v18\n\t"
    "v_add3_u32 v18, v156, v157, v18\n\t"
    "v_add3_u32 v18, v158, v159, v18\n\t"
    "v_cmp_ne_u32 vcc, s50, v18\n\t"
    "s_cbranch_vccnz LA13_%=\n\t"
    "s_nop 4\n\t"
    "s_branch LA14_%=\n\t"
    "LA15_%=:\n\t"
    "s_cmp_eq_u32 s63, 0\n\t"
    "s_cbranch_scc1 LA16_%=\n\t"
    "s_sub_u32 s63, s63, 1\n\t"
    "s_sleep 1\n\t"
    "s_add_u32 s85, s44, 0x9000\n\t"
    "buffer_load_dwordx4 v[160:163], v6, s[28:31], s85 offen sc1\n\t"
    "buffer_load_dwordx4 v[164:167], v6, s[28:31], s85 offen offset:1024 sc1\n\t"
    "buffer_load_dword v19, v7, s[28:31], s85 offen offset:2048 sc1\n\t"
    "s_waitcnt vmcnt(0)\n\t"
    "v_add3_u32 v19, v160, v161, v19\n\t"
    "v_add3_u32 v19, v162, v163, v19\n\t"
    "v_add3_u32 v19, v164, v165, v19\n\t"
    "v_add3_u32 v19, v166, v167, v19\n\t"
    "v_cmp_ne_u32 vcc, s49, v19\n\t"
    "s_cbranch_vccnz LA15_%=\n\t"
    "s_nop 4\n\t"
    "s_branch LA16_%=\n\t"
    "LA17_%=:\n\t"
    "s_cmp_eq_u32 s63, 0\n\t"
    "s_cbranch_scc1 LA18_%=\n\t"
    "s_sub_u32 s63, s63, 1\n\t"
    "s_sleep 1\n\t"
    "s_add_u32 s85, s45, 0x9000\n\t"
    "buffer_load_dwordx4 v[168:171], v6, s[28:31], s85 offen sc1\n\t"
    "buffer_load_dwordx4 v[172:175], v6, s[28:31], s85 offen offset:1024 sc1\n\t"
    "buffer_load_dword v16, v7, s[28:31], s85 offen offset:2048 sc1\n\t"
    "s_waitcnt vmcnt(0)\n\t"
    "v_add3_u32 v16, v168, v169, v16\n\t"
    "v_add3_u32 v16, v170, v171, v16\n\t"
    "v_add3_u32 v16, v172, v173, v16\n\t"
    "v_add3_u32 v16, v174, v175, v16\n\t"
    "v_cmp_ne_u32 vcc, s50, v16\n\t"
    "s_cbranch_vccnz LA17_%=\n\t"
    "s_nop 4\n\t"
    "s_branch LA18_%=\n\t"
    "LA19_%=:\n\t"
    "s_cmp_eq_u32 s63, 0\n\t"
    "s_cbranch_scc1 LA20_%=\n\t"
    "s_sub_u32 s63, s63, 1\n\t"
    "s_sleep 1\n\t"
    "s_add_u32 s85, s44, 0xd800\n\t"
    "buffer_load_dwordx4 v[176:179], v6, s[28:31], s85 offen sc1\n\t"
    "buffer_load_dwordx4 v[180:183], v6, s[28:31], s85 offen offset:1024 sc1\n\t"
    "buffer_load_dword v17, v7, s[28:31], s85 offen offset:2048 sc1\n\t"
    "s_waitcnt vmcnt(0)\n\t"
    "v_add3_u32 v17, v176, v177, v17\n\t"
    "v_add3_u32 v17, v178, v179, v17\n\t"
    "v_add3_u32 v17, v180, v181, v17\n\t"
    "v_add3_u32 v17, v182, v183, v17\n\t"
    "v_cmp_ne_u32 vcc, s49, v17\n\t"
    "s_cbranch_vccnz LA19_%=\n\t"
    "s_nop 4\n\t"
    "s_branch LA20_%=\n\t"
    "LA21_%=:\n\t"
    "s_cmp_eq_u32 s63, 0\n\t"
    "s_cbranch_scc1 LA22_%=\n\t"
    "s_sub_u32 s63, s63, 1\n\t"
    "s_sleep 1\n\t"
    "s_add_u32 s85, s45, 0xd800\n\t"
    "buffer_load_dwordx4 v[184:187], v6, s[28:31], s85 offen sc1\n\t"
    "buffer_load_dwordx4 v[188:191], v6, s[28:31], s85 offen offset:1024 sc1\n\t"
    "buffer_load_dword v20, v7, s[28:31], s85 offen offset:2048 sc1\n\t"
    "s_waitcnt vmcnt(0)\n\t"
    "v_add3_u32 v20, v184, v185, v20\n\t"
    "v_add3_u32 v20, v186, v187, v20\n\t"
    "v_add3_u32 v20, v188, v189, v20\n\t"
    "v_add3_u32 v20, v190, v191, v20\n\t"
    "v_cmp_ne_u32 vcc, s50, v20\n\t"
    "s_cbranch_vccnz LA21_%=\n\t"
    "s_nop 4\n\t"
    "s_branch LA22_%=\n\t"
    "LA23_%=:\n\t"
    "s_cmp_eq_u32 s63, 0\n\t"
    "s_cbranch_scc1 LA24_%=\n\t"
    "s_sub_u32 s63, s63, 1\n\t"
    "s_sleep 1\n\t"
    "s_add_u32 s85, s44, 0x12000\n\t"
    "buffer_load_dwordx4 v[192:195], v6, s[28:31], s85 offen sc1\n\t"
    "buffer_load_dwordx4 v[196:199], v6, s[28:31], s85 offen offset:1024 sc1\n\t"
    "buffer_load_dword v21, v7, s[28:31], s85 offen offset:2048 sc1\n\t"
    "s_waitcnt vmcnt(0)\n\t"
    "v_add3_u32 v21, v192, v193, v21\n\t"
    "v_add3_u32 v21, v194, v195, v21\n\t"
    "v_add3_u32 v21, v196, v197, v21\n\t"
    "v_add3_u32 v21, v198, v199, v21\n\t"
    "v_cmp_ne_u32 vcc, s49, v21\n\t"
    "s_cbranch_vccnz LA23_%=\n\t"
    "s_nop 4\n\t"
    "s_branch LA24_%=\n\t"
    "LA25_%=:\n\t"
    "s_cmp_eq_u32 s63, 0\n\t"
    "s_cbranch_scc1 LA26_%=\n\t"
    "s_sub_u32 s63, s63, 1\n\t"
    "s_sleep 1\n\t"
    "s_add_u32 s85, s45, 0x12000\n\t"
    "buffer_load_dwordx4 v[224:227], v6, s[28:31], s85 offen sc1\n\t"
    "buffer_load_dwordx4 v[228:231], v6, s[28:31], s85 offen offset:1024 sc1\n\t"
    "buffer_load_dword v22, v7, s[28:31], s85 offen offset:2048 sc1\n\t"
    "s_waitcnt vmcnt(0)\n\t"
    "v_add3_u32 v22, v224, v225, v22\n\t"
    "v_add3_u32 v22, v226, v227, v22\n\t"
    "v_add3_u32 v22, v228, v229, v22\n\t"
    "v_add3_u32 v22, v230, v231, v22\n\t"
    "v_cmp_ne_u32 vcc, s50, v22\n\t"
    "s_cbranch_vccnz LA25_%=\n\t"
    "s_nop 4\n\t"
    "s_branch LA26_%=\n\t"
    "LA27_%=:\n\t"
    "s_cmp_eq_u32 s63, 0\n\t"
    "s_cbranch_scc1 LA28_%=\n\t"
    "s_sub_u32 s63, s63, 1\n\t"
    "s_sleep 1\n\t"
    "s_add_u32 s85, s64, 24576\n\t"
    "buffer_load_dwordx4 v[80:83], v6, s[28:31], s85 offen sc1\n\t"
    "buffer_load_dword v84, v7, s[28:31], s85 offen offset:1024 sc1\n\t"
    "s_add_u32 s85, s64, 49152\n\t"
    "buffer_load_dwordx4 v[88:91], v6, s[28:31], s85 offen sc1\n\t"
    "buffer_load_dword v92, v7, s[28:31], s85 offen offset:1024 sc1\n\t"
    "s_waitcnt vmcnt(0)\n\t"
    "v_add3_u32 v85, v80, v81, v82\n\t"
    "v_add3_u32 v85, v85, v83, s1\n\t"
    "v_xor_b32 v12, v85, v84\n\t"
    "v_add3_u32 v93, v88, v89, v90\n\t"
    "v_add3_u32 v93, v93, v91, s2\n\t"
    "v_xor_b32 v93, v93, v92\n\t"
    "v_or_b32 v12, v12, v93\n\t"
    "v_cmp_lt_u32 vcc, s3, v12\n\t"
    "s_cbranch_vccnz LA27_%=\n\t"
    "s_nop 4\n\t"
    "s_branch LA28_%=\n\t"
    "LA29_%=:\n\t"
    "s_cmp_eq_u32 s63, 0\n\t"
    "s_cbranch_scc1 LA30_%=\n\t"
    "s_sub_u32 s63, s63, 1\n\t"
    "s_sleep 1\n\t"
    "s_add_u32 s85, s64, 0\n\t"
    "buffer_load_dwordx4 v[40:43], v6, s[28:31], s85 offen sc1\n\t"
    "buffer_load_dword v44, v7, s[28:31], s85 offen offset:1024 sc1\n\t"
    "s_waitcnt vmcnt(0)\n\t"
    "v_add3_u32 v45, v40, v41, v42\n\t"
    "v_add3_u32 v45, v45, v43, s0\n\t"
    "v_xor_b32 v12, v45, v44\n\t"
    "v_cmp_lt_u32 vcc, s3, v12\n\t"
    "s_cbranch_vccnz LA29_%=\n\t"
    "s_nop 4\n\t"
    "s_branch LA30_%=\n\t"
    "LTYPEB_%=:\n\t"
    "s_mov_b32 s96, 0\n\t"
    "s_mov_b32 s97, 1\n\t"
    "s_mov_b32 s98, 2\n\t"
    "s_mov_b32 s99, 3\n\t"
    "s_mul_i32 s74, s91, 1327104\n\t"
    "s_lshl_b32 s75, s90, 8\n\t"
    "s_add_u32 s74, s74, s75\n\t"
    "s_lshl_b32 s75, s91, 6\n\t"
    "s_and_b32 s75, s75, 128\n\t"
    "s_xor_b32 s76, s75, 128\n\t"
    "s_add_u32 s74, s74, s76\n\t"
    "s_sub_u32 s100, s75, s76\n\t"
    "s_sub_u32 s101, 256, s76\n\t"
    "s_mul_i32 s75, s92, 72\n\t"
    "s_mul_i32 s76, s91, 18\n\t"
    "s_add_u32 s75, s75, s76\n\t"
    "s_lshl_b32 s75, s75, 3\n\t"
    "s_add_u32 s75, s75, s89\n\t"
    "s_mul_i32 s75, s75, 2304\n\t"
    "s_mul_i32 s76, s96, 368640\n\t"
    "s_add_u32 s39, s74, s76\n\t"
    "s_mul_i32 s76, s96, 92160\n\t"
    "s_add_u32 s43, s75, s76\n\t"
    "s_mul_i32 s76, s97, 368640\n\t"
    "s_add_u32 s40, s74, s76\n\t"
    "s_mul_i32 s76, s97, 92160\n\t"
    "s_add_u32 s44, s75, s76\n\t"
    "s_mul_i32 s76, s98, 368640\n\t"
    "s_add_u32 s41, s74, s76\n\t"
    "s_mul_i32 s76, s98, 92160\n\t"
    "s_add_u32 s45, s75, s76\n\t"
    "s_mul_i32 s76, s99, 368640\n\t"
    "s_add_u32 s42, s74, s76\n\t"
    "s_mul_i32 s76, s99, 92160\n\t"
    "s_add_u32 s46, s75, s76\n\t"
    "s_mul_i32 s76, s90, 92160\n\t"
    "s_add_u32 s47, s75, s76\n\t"
    "s_lshl_b32 s74, s92, 2\n\t"
    "s_add_u32 s74, s74, s90\n\t"
    "s_lshl_b32 s74, s74, 2\n\t"
    "s_mul_i32 s75, s89, 3072\n\t"
    "s_add_u32 s75, s75, 0x1600000\n\t"
    "s_add_u32 s76, s74, s91\n\t"
    "s_mul_i32 s76, s76, 73728\n\t"
    "s_add_u32 s64, s76, s75\n\t"
    "s_xor_b32 s76, s91, 1\n\t"
    "s_add_u32 s76, s76, s74\n\t"
    "s_mul_i32 s76, s76, 73728\n\t"
    "s_add_u32 s76, s76, s75\n\t"
    "s_add_u32 s69, s76, 0\n\t"
    "s_xor_b32 s76, s91, 2\n\t"
    "s_add_u32 s76, s76, s74\n\t"
    "s_mul_i32 s76, s76, 73728\n\t"
    "s_add_u32 s76, s76, s75\n\t"
    "s_add_u32 s70, s76, 24576\n\t"
    "s_xor_b32 s76, s91, 3\n\t"
    "s_add_u32 s76, s76, s74\n\t"
    "s_mul_i32 s76, s76, 73728\n\t"
    "s_add_u32 s76, s76, s75\n\t"
    "s_add_u32 s71, s76, 49152\n\t"
    "s_lshl_b32 s74, s92, 2\n\t"
    "s_add_u32 s74, s74, s93\n\t"
    "s_lshl_b32 s74, s74, 1\n\t"
    "s_lshr_b32 s75, s91, 1\n\t"
    "s_add_u32 s74, s74, s75\n\t"
    "s_mul_i32 s74, s74, 18432\n\t"
    "s_lshl_b32 s75, s90, 8\n\t"
    "s_add_u32 s74, s74, s75\n\t"
    "s_lshl_b32 s75, s91, 6\n\t"
    "s_add_u32 s74, s74, s75\n\t"
    "s_lshl_b32 s75, s94, 5\n\t"
    "s_add_u32 s62, s74, s75\n\t"
    "s_mul_i32 s74, s100, -144\n\t"
    "s_add_u32 s67, s62, s74\n\t"
    "s_sub_u32 s74, s101, s100\n\t"
    "s_add_u32 s68, s62, s74\n\t"
    "s_lshl_b32 s74, s88, 7\n\t"
    "s_add_u32 s95, s74, 0x2a00000\n\t"
    "s_mov_b32 s63, 0x8000\n\t"
    "s_mov_b32 s72, 0x0c0c0400\n\t"
    "s_mov_b32 s73, 0x04000c0c\n\t"
    "s_add_u32 m0, s36, 131072\n\t"
    "s_add_u32 s85, s9, 73728\n\t"
    "buffer_load_dwordx4 v216, s[16:19], s9 offen lds\n\t"
    "s_add_u32 m0, s36, 139264\n\t"
    "s_add_u32 s9, s9, 128\n\t"
    "buffer_load_dwordx4 v216, s[16:19], s85 offen lds\n\t"
    "s_cmp_eq_u32 s9, s10\n\t"
    "s_cselect_b32 s9, s37, s9\n\t"
    "s_add_u32 m0, s36, 16384\n\t"
    "s_add_u32 s85, s1, 0x0\n\t"
    "buffer_load_dwordx4 v4, s[20:23], s85 offen lds\n\t"
    "s_add_u32 m0, s36, 24576\n\t"
    "s_add_u32 s85, s1, 0x80\n\t"
    "buffer_load_dwordx4 v4, s[20:23], s85 offen lds\n\t"
    "s_mov_b64 exec, 0\n\t"
    "s_cmp_eq_u32 s89, 0\n\t"
    "s_cbranch_scc1 LIPB0x0_%=\n\t"
    "s_cmp_eq_u32 s89, 1\n\t"
    "s_cbranch_scc1 LIPB1x0_%=\n\t"
    "s_cmp_eq_u32 s89, 2\n\t"
    "s_cbranch_scc1 LIPB2x0_%=\n\t"
    "s_cmp_eq_u32 s89, 3\n\t"
    "s_cbranch_scc1 LIPB3x0_%=\n\t"
    "s_cmp_eq_u32 s89, 4\n\t"
    "s_cbranch_scc1 LIPB4x0_%=\n\t"
    "s_cmp_eq_u32 s89, 5\n\t"
    "s_cbranch_scc1 LIPRB_%=\n\t"
    "s_cmp_eq_u32 s89, 6\n\t"
    "s_cbranch_scc1 LIPRB_%=\n\t"
    "s_cmp_eq_u32 s89, 7\n\t"
    "s_cbranch_scc1 LIPRB_%=\n\t"
    "LIPRB_%=:\n\t"
    "s_mov_b64 exec, -1\n\t"
    "s_waitcnt vmcnt(6)\n\t"
    "s_barrier\n\t"
    "v_add_u32 v220, 0x18000, v220\n\t"
    "ds_read_b128 v[40:43], v220 offset:49152\n\t"
    "ds_read_b128 v[44:47], v220 offset:51200\n\t"
    "v_add_u32 v221, 0x18000, v221\n\t"
    "ds_read_b128 v[48:51], v221 offset:49152\n\t"
    "ds_read_b128 v[52:55], v221 offset:51200\n\t"
    "ds_read_b64_tr_b16 v[24:25], v0 offset:0\n\t"
    "ds_read_b64_tr_b16 v[26:27], v0 offset:512\n\t"
    "ds_read_b64_tr_b16 v[28:29], v1 offset:0\n\t"
    "ds_read_b64_tr_b16 v[30:31], v1 offset:512\n\t"
    "ds_read_b64_tr_b16 v[32:33], v0 offset:4096\n\t"
    "ds_read_b64_tr_b16 v[34:35], v0 offset:4608\n\t"
    "ds_read_b64_tr_b16 v[36:37], v1 offset:4096\n\t"
    "ds_read_b64_tr_b16 v[38:39], v1 offset:4608\n\t"
    "s_waitcnt vmcnt(5)\n\t"
    "s_barrier\n\t"
    "s_setprio 1\n\t"
    "s_waitcnt lgkmcnt(6)\n\t"
    "v_mfma_f32_16x16x32_bf16 v[224:227], v[24:27], v[40:43], 0\n\t"
    "v_mfma_f32_16x16x32_bf16 v[232:235], v[24:27], v[44:47], 0\n\t"
    "ds_read_b64_tr_b16 v[24:25], v0 offset:8192\n\t"
    "s_waitcnt lgkmcnt(5)\n\t"
    "v_mfma_f32_16x16x32_bf16 v[228:231], v[28:31], v[40:43], 0\n\t"
    "ds_read_b64_tr_b16 v[26:27], v0 offset:8704\n\t"
    "s_add_u32 m0, s36, 32768\n\t"
    "s_add_u32 s85, s9, 73728\n\t"
    "buffer_load_dwordx4 v216, s[16:19], s9 offen lds\n\t"
    "s_add_u32 m0, s36, 40960\n\t"
    "s_add_u32 s9, s9, 128\n\t"
    "buffer_load_dwordx4 v216, s[16:19], s85 offen lds\n\t"
    "s_cmp_eq_u32 s9, s10\n\t"
    "s_cselect_b32 s9, s37, s9\n\t"
    "v_mfma_f32_16x16x32_bf16 v[236:239], v[28:31], v[44:47], 0\n\t"
    "ds_read_b64_tr_b16 v[28:29], v1 offset:8192\n\t"
    "s_setprio 0\n\t"
    "s_waitcnt lgkmcnt(5)\n\t"
    "v_mfma_f32_16x16x32_bf16 v[224:227], v[32:35], v[48:51], v[224:227]\n\t"
    "ds_read_b64_tr_b16 v[30:31], v1 offset:8704\n\t"
    "v_mfma_f32_16x16x32_bf16 v[232:235], v[32:35], v[52:55], v[232:235]\n\t"
    "ds_read_b64_tr_b16 v[32:33], v0 offset:12288\n\t"
    "s_waitcnt lgkmcnt(5)\n\t"
    "v_mfma_f32_16x16x32_bf16 v[228:231], v[36:39], v[48:51], v[228:231]\n\t"
    "ds_read_b64_tr_b16 v[34:35], v0 offset:12800\n\t"
    "s_add_u32 m0, s36, 49152\n\t"
    "s_add_u32 s85, s2, 0x0\n\t"
    "buffer_load_dwordx4 v4, s[20:23], s85 offen lds\n\t"
    "v_mfma_f32_16x16x32_bf16 v[236:239], v[36:39], v[52:55], v[236:239]\n\t"
    "ds_read_b64_tr_b16 v[36:37], v1 offset:12288\n\t"
    "s_waitcnt vmcnt(4)\n\t"
    "s_barrier\n\t"
    "s_setprio 1\n\t"
    "s_waitcnt lgkmcnt(5)\n\t"
    "v_mfma_f32_16x16x32_bf16 v[240:243], v[24:27], v[40:43], 0\n\t"
    "ds_read_b64_tr_b16 v[38:39], v1 offset:12800\n\t"
    "v_mfma_f32_16x16x32_bf16 v[248:251], v[24:27], v[44:47], 0\n\t"
    "ds_read_b64_tr_b16 v[24:25], v0 offset:16384\n\t"
    "ds_read_b128 v[56:59], v220 offset:32768\n\t"
    "s_waitcnt lgkmcnt(6)\n\t"
    "v_mfma_f32_16x16x32_bf16 v[244:247], v[28:31], v[40:43], 0\n\t"
    "ds_read_b64_tr_b16 v[26:27], v0 offset:16896\n\t"
    "s_add_u32 m0, s36, 57344\n\t"
    "s_add_u32 s85, s2, 0x80\n\t"
    "buffer_load_dwordx4 v4, s[20:23], s85 offen lds\n\t"
    "v_mfma_f32_16x16x32_bf16 v[252:255], v[28:31], v[44:47], 0\n\t"
    "ds_read_b64_tr_b16 v[28:29], v1 offset:16384\n\t"
    "ds_read_b128 v[60:63], v220 offset:34816\n\t"
    "s_setprio 0\n\t"
    "s_waitcnt lgkmcnt(7)\n\t"
    "v_mfma_f32_16x16x32_bf16 v[240:243], v[32:35], v[48:51], v[240:243]\n\t"
    "ds_read_b64_tr_b16 v[30:31], v1 offset:16896\n\t"
    "s_add_u32 m0, s36, 65536\n\t"
    "s_add_u32 s85, s9, 73728\n\t"
    "buffer_load_dwordx4 v216, s[16:19], s9 offen lds\n\t"
    "s_add_u32 m0, s36, 73728\n\t"
    "s_add_u32 s9, s9, 128\n\t"
    "buffer_load_dwordx4 v216, s[16:19], s85 offen lds\n\t"
    "s_cmp_eq_u32 s9, s10\n\t"
    "s_cselect_b32 s9, s37, s9\n\t"
    "v_mfma_f32_16x16x32_bf16 v[248:251], v[32:35], v[52:55], v[248:251]\n\t"
    "ds_read_b64_tr_b16 v[32:33], v0 offset:20480\n\t"
    "ds_read_b128 v[64:67], v221 offset:32768\n\t"
    "s_waitcnt lgkmcnt(8)\n\t"
    "v_mfma_f32_16x16x32_bf16 v[244:247], v[36:39], v[48:51], v[244:247]\n\t"
    "ds_read_b64_tr_b16 v[34:35], v0 offset:20992\n\t"
    "v_mfma_f32_16x16x32_bf16 v[252:255], v[36:39], v[52:55], v[252:255]\n\t"
    "ds_read_b64_tr_b16 v[36:37], v1 offset:20480\n\t"
    "ds_read_b128 v[68:71], v221 offset:34816\n\t"
    "s_waitcnt vmcnt(6)\n\t"
    "s_barrier\n\t"
    "s_setprio 1\n\t"
    "s_waitcnt lgkmcnt(8)\n\t"
    "v_mfma_f32_16x16x32_bf16 v[224:227], v[24:27], v[56:59], v[224:227]\n\t"
    "ds_read_b64_tr_b16 v[38:39], v1 offset:20992\n\t"
    "s_waitcnt lgkmcnt(7)\n\t"
    "v_mfma_f32_16x16x32_bf16 v[232:235], v[24:27], v[60:63], v[232:235]\n\t"
    "ds_read_b64_tr_b16 v[24:25], v0 offset:24576\n\t"
    "s_waitcnt lgkmcnt(7)\n\t"
    "v_mfma_f32_16x16x32_bf16 v[228:231], v[28:31], v[56:59], v[228:231]\n\t"
    "ds_read_b64_tr_b16 v[26:27], v0 offset:25088\n\t"
    "s_add_u32 m0, s36, 81920\n\t"
    "s_add_u32 s85, s3, 0x0\n\t"
    "buffer_load_dwordx4 v4, s[20:23], s85 offen lds\n\t"
    "v_mfma_f32_16x16x32_bf16 v[236:239], v[28:31], v[60:63], v[236:239]\n\t"
    "ds_read_b64_tr_b16 v[28:29], v1 offset:24576\n\t"
    "s_setprio 0\n\t"
    "s_waitcnt lgkmcnt(6)\n\t"
    "v_mfma_f32_16x16x32_bf16 v[224:227], v[32:35], v[64:67], v[224:227]\n\t"
    "ds_read_b64_tr_b16 v[30:31], v1 offset:25088\n\t"
    "s_add_u32 m0, s36, 90112\n\t"
    "s_add_u32 s85, s3, 0x80\n\t"
    "buffer_load_dwordx4 v4, s[20:23], s85 offen lds\n\t"
    "s_waitcnt lgkmcnt(5)\n\t"
    "v_mfma_f32_16x16x32_bf16 v[232:235], v[32:35], v[68:71], v[232:235]\n\t"
    "ds_read_b64_tr_b16 v[32:33], v0 offset:28672\n\t"
    "ds_read_b64_tr_b16 v[34:35], v0 offset:29184\n\t"
    "s_waitcnt lgkmcnt(6)\n\t"
    "v_mfma_f32_16x16x32_bf16 v[228:231], v[36:39], v[64:67], v[228:231]\n\t"
    "v_mfma_f32_16x16x32_bf16 v[236:239], v[36:39], v[68:71], v[236:239]\n\t"
    "ds_read_b64_tr_b16 v[36:37], v1 offset:28672\n\t"
    "s_waitcnt vmcnt(5)\n\t"
    "s_barrier\n\t"
    "s_setprio 1\n\t"
    "s_waitcnt lgkmcnt(5)\n\t"
    "v_mfma_f32_16x16x32_bf16 v[240:243], v[24:27], v[56:59], v[240:243]\n\t"
    "ds_read_b64_tr_b16 v[38:39], v1 offset:29184\n\t"
    "v_mfma_f32_16x16x32_bf16 v[248:251], v[24:27], v[60:63], v[248:251]\n\t"
    "ds_read_b64_tr_b16 v[24:25], v0 offset:49152\n\t"
    "v_add_u32 v220, 0xffff0000, v220\n\t"
    "ds_read_b128 v[72:75], v220 offset:0\n\t"
    "s_waitcnt lgkmcnt(6)\n\t"
    "v_mfma_f32_16x16x32_bf16 v[244:247], v[28:31], v[56:59], v[244:247]\n\t"
    "ds_read_b64_tr_b16 v[26:27], v0 offset:49664\n\t"
    "s_add_u32 m0, s36, 0\n\t"
    "s_add_u32 s85, s9, 73728\n\t"
    "buffer_load_dwordx4 v216, s[16:19], s9 offen lds\n\t"
    "s_add_u32 m0, s36, 8192\n\t"
    "s_add_u32 s9, s9, 128\n\t"
    "buffer_load_dwordx4 v216, s[16:19], s85 offen lds\n\t"
    "s_cmp_eq_u32 s9, s10\n\t"
    "s_cselect_b32 s9, s37, s9\n\t"
    "v_mfma_f32_16x16x32_bf16 v[252:255], v[28:31], v[60:63], v[252:255]\n\t"
    "ds_read_b64_tr_b16 v[28:29], v1 offset:49152\n\t"
    "ds_read_b128 v[76:79], v220 offset:2048\n\t"
    "s_setprio 0\n\t"
    "s_waitcnt lgkmcnt(7)\n\t"
    "v_mfma_f32_16x16x32_bf16 v[240:243], v[32:35], v[64:67], v[240:243]\n\t"
    "ds_read_b64_tr_b16 v[30:31], v1 offset:49664\n\t"
    "v_mfma_f32_16x16x32_bf16 v[248:251], v[32:35], v[68:71], v[248:251]\n\t"
    "ds_read_b64_tr_b16 v[32:33], v0 offset:53248\n\t"
    "v_add_u32 v221, 0xffff0000, v221\n\t"
    "ds_read_b128 v[80:83], v221 offset:0\n\t"
    "s_waitcnt lgkmcnt(8)\n\t"
    "v_mfma_f32_16x16x32_bf16 v[244:247], v[36:39], v[64:67], v[244:247]\n\t"
    "ds_read_b64_tr_b16 v[34:35], v0 offset:53760\n\t"
    "s_add_u32 m0, s36, 98304\n\t"
    "s_add_u32 s85, s4, 0x0\n\t"
    "buffer_load_dwordx4 v4, s[20:23], s85 offen lds\n\t"
    "v_mfma_f32_16x16x32_bf16 v[252:255], v[36:39], v[68:71], v[252:255]\n\t"
    "ds_read_b64_tr_b16 v[36:37], v1 offset:53248\n\t"
    "ds_read_b128 v[84:87], v221 offset:2048\n\t"
    "s_waitcnt vmcnt(7)\n\t"
    "s_barrier\n\t"
    "s_setprio 1\n\t"
    "s_waitcnt lgkmcnt(8)\n\t"
    "v_mfma_f32_16x16x32_bf16 v[224:227], v[24:27], v[72:75], v[224:227]\n\t"
    "ds_read_b64_tr_b16 v[38:39], v1 offset:53760\n\t"
    "s_waitcnt lgkmcnt(7)\n\t"
    "v_mfma_f32_16x16x32_bf16 v[232:235], v[24:27], v[76:79], v[232:235]\n\t"
    "ds_read_b64_tr_b16 v[24:25], v0 offset:57344\n\t"
    "s_waitcnt lgkmcnt(7)\n\t"
    "v_mfma_f32_16x16x32_bf16 v[228:231], v[28:31], v[72:75], v[228:231]\n\t"
    "ds_read_b64_tr_b16 v[26:27], v0 offset:57856\n\t"
    "s_add_u32 m0, s36, 106496\n\t"
    "s_add_u32 s85, s4, 0x80\n\t"
    "buffer_load_dwordx4 v4, s[20:23], s85 offen lds\n\t"
    "v_mfma_f32_16x16x32_bf16 v[236:239], v[28:31], v[76:79], v[236:239]\n\t"
    "ds_read_b64_tr_b16 v[28:29], v1 offset:57344\n\t"
    "s_setprio 0\n\t"
    "s_waitcnt lgkmcnt(6)\n\t"
    "v_mfma_f32_16x16x32_bf16 v[224:227], v[32:35], v[80:83], v[224:227]\n\t"
    "ds_read_b64_tr_b16 v[30:31], v1 offset:57856\n\t"
    "s_add_u32 m0, s36, 16384\n\t"
    "s_add_u32 s85, s9, 73728\n\t"
    "buffer_load_dwordx4 v216, s[16:19], s9 offen lds\n\t"
    "s_add_u32 m0, s36, 24576\n\t"
    "s_add_u32 s9, s9, 128\n\t"
    "buffer_load_dwordx4 v216, s[16:19], s85 offen lds\n\t"
    "s_cmp_eq_u32 s9, s10\n\t"
    "s_cselect_b32 s9, s37, s9\n\t"
    "s_waitcnt lgkmcnt(5)\n\t"
    "v_mfma_f32_16x16x32_bf16 v[232:235], v[32:35], v[84:87], v[232:235]\n\t"
    "ds_read_b64_tr_b16 v[32:33], v0 offset:61440\n\t"
    "s_waitcnt lgkmcnt(5)\n\t"
    "v_mfma_f32_16x16x32_bf16 v[228:231], v[36:39], v[80:83], v[228:231]\n\t"
    "ds_read_b64_tr_b16 v[34:35], v0 offset:61952\n\t"
    "v_mfma_f32_16x16x32_bf16 v[236:239], v[36:39], v[84:87], v[236:239]\n\t"
    "ds_read_b64_tr_b16 v[36:37], v1 offset:61440\n\t"
    "s_waitcnt vmcnt(7)\n\t"
    "s_barrier\n\t"
    "s_setprio 1\n\t"
    "s_waitcnt lgkmcnt(5)\n\t"
    "v_mfma_f32_16x16x32_bf16 v[240:243], v[24:27], v[72:75], v[240:243]\n\t"
    "ds_read_b64_tr_b16 v[38:39], v1 offset:61952\n\t"
    "v_mfma_f32_16x16x32_bf16 v[248:251], v[24:27], v[76:79], v[248:251]\n\t"
    "v_add_u32 v0, 0x14000, v0\n\t"
    "ds_read_b64_tr_b16 v[24:25], v0 offset:0\n\t"
    "ds_read_b128 v[88:91], v220 offset:32768\n\t"
    "s_waitcnt lgkmcnt(6)\n\t"
    "v_mfma_f32_16x16x32_bf16 v[244:247], v[28:31], v[72:75], v[244:247]\n\t"
    "ds_read_b64_tr_b16 v[26:27], v0 offset:512\n\t"
    "s_add_u32 m0, s36, 114688\n\t"
    "s_add_u32 s85, s5, 0x0\n\t"
    "buffer_load_dwordx4 v4, s[20:23], s85 offen lds\n\t"
    "v_mfma_f32_16x16x32_bf16 v[252:255], v[28:31], v[76:79], v[252:255]\n\t"
    "v_add_u32 v1, 0x14000, v1\n\t"
    "ds_read_b64_tr_b16 v[28:29], v1 offset:0\n\t"
    "ds_read_b128 v[92:95], v220 offset:34816\n\t"
    "s_setprio 0\n\t"
    "s_waitcnt lgkmcnt(7)\n\t"
    "v_mfma_f32_16x16x32_bf16 v[240:243], v[32:35], v[80:83], v[240:243]\n\t"
    "ds_read_b64_tr_b16 v[30:31], v1 offset:512\n\t"
    "s_add_u32 m0, s36, 122880\n\t"
    "s_add_u32 s85, s5, 0x80\n\t"
    "buffer_load_dwordx4 v4, s[20:23], s85 offen lds\n\t"
    "v_mfma_f32_16x16x32_bf16 v[248:251], v[32:35], v[84:87], v[248:251]\n\t"
    "ds_read_b64_tr_b16 v[32:33], v0 offset:4096\n\t"
    "ds_read_b64_tr_b16 v[34:35], v0 offset:4608\n\t"
    "ds_read_b128 v[96:99], v221 offset:32768\n\t"
    "s_waitcnt lgkmcnt(9)\n\t"
    "v_mfma_f32_16x16x32_bf16 v[244:247], v[36:39], v[80:83], v[244:247]\n\t"
    "v_mfma_f32_16x16x32_bf16 v[252:255], v[36:39], v[84:87], v[252:255]\n\t"
    "ds_read_b64_tr_b16 v[36:37], v1 offset:4096\n\t"
    "ds_read_b128 v[100:103], v221 offset:34816\n\t"
    "s_waitcnt vmcnt(8)\n\t"
    "s_barrier\n\t"
    "s_setprio 1\n\t"
    "s_waitcnt lgkmcnt(8)\n\t"
    "v_mfma_f32_16x16x32_bf16 v[224:227], v[24:27], v[88:91], v[224:227]\n\t"
    "ds_read_b64_tr_b16 v[38:39], v1 offset:4608\n\t"
    "s_waitcnt lgkmcnt(7)\n\t"
    "v_mfma_f32_16x16x32_bf16 v[232:235], v[24:27], v[92:95], v[232:235]\n\t"
    "ds_read_b64_tr_b16 v[24:25], v0 offset:8192\n\t"
    "s_waitcnt lgkmcnt(7)\n\t"
    "v_mfma_f32_16x16x32_bf16 v[228:231], v[28:31], v[88:91], v[228:231]\n\t"
    "ds_read_b64_tr_b16 v[26:27], v0 offset:8704\n\t"
    "s_add_u32 m0, s36, 32768\n\t"
    "s_add_u32 s85, s9, 73728\n\t"
    "buffer_load_dwordx4 v216, s[16:19], s9 offen lds\n\t"
    "s_add_u32 m0, s36, 40960\n\t"
    "s_add_u32 s9, s9, 128\n\t"
    "buffer_load_dwordx4 v216, s[16:19], s85 offen lds\n\t"
    "s_cmp_eq_u32 s9, s10\n\t"
    "s_cselect_b32 s9, s37, s9\n\t"
    "v_mfma_f32_16x16x32_bf16 v[236:239], v[28:31], v[92:95], v[236:239]\n\t"
    "ds_read_b64_tr_b16 v[28:29], v1 offset:8192\n\t"
    "s_setprio 0\n\t"
    "s_waitcnt lgkmcnt(6)\n\t"
    "v_mfma_f32_16x16x32_bf16 v[224:227], v[32:35], v[96:99], v[224:227]\n\t"
    "ds_read_b64_tr_b16 v[30:31], v1 offset:8704\n\t"
    "s_waitcnt lgkmcnt(5)\n\t"
    "v_mfma_f32_16x16x32_bf16 v[232:235], v[32:35], v[100:103], v[232:235]\n\t"
    "ds_read_b64_tr_b16 v[32:33], v0 offset:12288\n\t"
    "s_waitcnt lgkmcnt(5)\n\t"
    "v_mfma_f32_16x16x32_bf16 v[228:231], v[36:39], v[96:99], v[228:231]\n\t"
    "ds_read_b64_tr_b16 v[34:35], v0 offset:12800\n\t"
    "s_add_u32 m0, s36, 131072\n\t"
    "s_add_u32 s85, s6, 0x0\n\t"
    "buffer_load_dwordx4 v4, s[20:23], s85 offen lds\n\t"
    "v_mfma_f32_16x16x32_bf16 v[236:239], v[36:39], v[100:103], v[236:239]\n\t"
    "ds_read_b64_tr_b16 v[36:37], v1 offset:12288\n\t"
    "s_waitcnt vmcnt(8)\n\t"
    "s_barrier\n\t"
    "s_setprio 1\n\t"
    "s_waitcnt lgkmcnt(5)\n\t"
    "v_mfma_f32_16x16x32_bf16 v[240:243], v[24:27], v[88:91], v[240:243]\n\t"
    "ds_read_b64_tr_b16 v[38:39], v1 offset:12800\n\t"
    "v_mfma_f32_16x16x32_bf16 v[248:251], v[24:27], v[92:95], v[248:251]\n\t"
    "ds_read_b64_tr_b16 v[24:25], v0 offset:16384\n\t"
    "v_add_u32 v220, 0xffff8000, v220\n\t"
    "ds_read_b128 v[104:107], v220 offset:0\n\t"
    "s_waitcnt lgkmcnt(6)\n\t"
    "v_mfma_f32_16x16x32_bf16 v[244:247], v[28:31], v[88:91], v[244:247]\n\t"
    "ds_read_b64_tr_b16 v[26:27], v0 offset:16896\n\t"
    "s_add_u32 m0, s36, 139264\n\t"
    "s_add_u32 s85, s6, 0x80\n\t"
    "buffer_load_dwordx4 v4, s[20:23], s85 offen lds\n\t"
    "v_mfma_f32_16x16x32_bf16 v[252:255], v[28:31], v[92:95], v[252:255]\n\t"
    "ds_read_b64_tr_b16 v[28:29], v1 offset:16384\n\t"
    "ds_read_b128 v[108:111], v220 offset:2048\n\t"
    "s_setprio 0\n\t"
    "s_waitcnt lgkmcnt(7)\n\t"
    "v_mfma_f32_16x16x32_bf16 v[240:243], v[32:35], v[96:99], v[240:243]\n\t"
    "ds_read_b64_tr_b16 v[30:31], v1 offset:16896\n\t"
    "s_add_u32 m0, s36, 49152\n\t"
    "s_add_u32 s85, s9, 73728\n\t"
    "buffer_load_dwordx4 v216, s[16:19], s9 offen lds\n\t"
    "s_add_u32 m0, s36, 57344\n\t"
    "s_add_u32 s9, s9, 128\n\t"
    "buffer_load_dwordx4 v216, s[16:19], s85 offen lds\n\t"
    "s_cmp_eq_u32 s9, s10\n\t"
    "s_cselect_b32 s9, s37, s9\n\t"
    "v_mfma_f32_16x16x32_bf16 v[248:251], v[32:35], v[100:103], v[248:251]\n\t"
    "ds_read_b64_tr_b16 v[32:33], v0 offset:20480\n\t"
    "v_add_u32 v221, 0xffff8000, v221\n\t"
    "ds_read_b128 v[112:115], v221 offset:0\n\t"
    "s_waitcnt lgkmcnt(8)\n\t"
    "v_mfma_f32_16x16x32_bf16 v[244:247], v[36:39], v[96:99], v[244:247]\n\t"
    "ds_read_b64_tr_b16 v[34:35], v0 offset:20992\n\t"
    "v_mfma_f32_16x16x32_bf16 v[252:255], v[36:39], v[100:103], v[252:255]\n\t"
    "ds_read_b64_tr_b16 v[36:37], v1 offset:20480\n\t"
    "ds_read_b128 v[116:119], v221 offset:2048\n\t"
    "s_waitcnt vmcnt(10)\n\t"
    "s_barrier\n\t"
    "s_setprio 1\n\t"
    "s_waitcnt lgkmcnt(8)\n\t"
    "v_mfma_f32_16x16x32_bf16 v[224:227], v[24:27], v[104:107], v[224:227]\n\t"
    "ds_read_b64_tr_b16 v[38:39], v1 offset:20992\n\t"
    "s_waitcnt lgkmcnt(7)\n\t"
    "v_mfma_f32_16x16x32_bf16 v[232:235], v[24:27], v[108:111], v[232:235]\n\t"
    "ds_read_b64_tr_b16 v[24:25], v0 offset:24576\n\t"
    "s_waitcnt lgkmcnt(7)\n\t"
    "v_mfma_f32_16x16x32_bf16 v[228:231], v[28:31], v[104:107], v[228:231]\n\t"
    "ds_read_b64_tr_b16 v[26:27], v0 offset:25088\n\t"
    "s_add_u32 m0, s36, 147456\n\t"
    "s_add_u32 s85, s7, 0x0\n\t"
    "buffer_load_dwordx4 v4, s[20:23], s85 offen lds\n\t"
    "v_mfma_f32_16x16x32_bf16 v[236:239], v[28:31], v[108:111], v[236:239]\n\t"
    "ds_read_b64_tr_b16 v[28:29], v1 offset:24576\n\t"
    "v_min_u32 v16, 104, v7\n\t"
    "v_mul_u32_u24 v18, 5462, v16\n\t"
    "v_lshrrev_b32 v18, 16, v18\n\t"
    "v_mul_u32_u24 v18, 768, v18\n\t"
    "v_lshl_add_u32 v16, v16, 5, v18\n\t"
    "s_mul_i32 s11, s91, 1327104\n\t"
    "s_mul_i32 s61, s92, 82944\n\t"
    "s_add_u32 s11, s11, s61\n\t"
    "s_mul_i32 s61, s89, 10368\n\t"
    "s_add_u32 s11, s11, s61\n\t"
    "s_add_u32 s11, s11, 768\n\t"
    "s_setprio 0\n\t"
    "s_waitcnt lgkmcnt(6)\n\t"
    "v_mfma_f32_16x16x32_bf16 v[224:227], v[32:35], v[112:115], v[224:227]\n\t"
    "ds_read_b64_tr_b16 v[30:31], v1 offset:25088\n\t"
    "s_add_u32 m0, s36, 155648\n\t"
    "s_add_u32 s85, s7, 0x80\n\t"
    "buffer_load_dwordx4 v4, s[20:23], s85 offen lds\n\t"
    "s_waitcnt lgkmcnt(5)\n\t"
    "v_mfma_f32_16x16x32_bf16 v[232:235], v[32:35], v[116:119], v[232:235]\n\t"
    "ds_read_b64_tr_b16 v[32:33], v0 offset:28672\n\t"
    "s_waitcnt lgkmcnt(5)\n\t"
    "v_mfma_f32_16x16x32_bf16 v[228:231], v[36:39], v[112:115], v[228:231]\n\t"
    "ds_read_b64_tr_b16 v[34:35], v0 offset:29184\n\t"
    "v_mfma_f32_16x16x32_bf16 v[236:239], v[36:39], v[116:119], v[236:239]\n\t"
    "ds_read_b64_tr_b16 v[36:37], v1 offset:28672\n\t"
    "s_waitcnt vmcnt(9)\n\t"
    "s_barrier\n\t"
    "s_setprio 1\n\t"
    "s_waitcnt lgkmcnt(5)\n\t"
    "v_mfma_f32_16x16x32_bf16 v[240:243], v[24:27], v[104:107], v[240:243]\n\t"
    "ds_read_b64_tr_b16 v[38:39], v1 offset:29184\n\t"
    "v_mfma_f32_16x16x32_bf16 v[248:251], v[24:27], v[108:111], v[248:251]\n\t"
    "ds_read_b64_tr_b16 v[24:25], v0 offset:32768\n\t"
    "ds_read_b128 v[120:123], v220 offset:16384\n\t"
    "s_waitcnt lgkmcnt(6)\n\t"
    "v_mfma_f32_16x16x32_bf16 v[244:247], v[28:31], v[104:107], v[244:247]\n\t"
    "ds_read_b64_tr_b16 v[26:27], v0 offset:33280\n\t"
    "s_add_u32 m0, s36, 65536\n\t"
    "s_add_u32 s85, s9, 73728\n\t"
    "buffer_load_dwordx4 v216, s[16:19], s9 offen lds\n\t"
    "s_add_u32 m0, s36, 73728\n\t"
    "s_add_u32 s9, s9, 128\n\t"
    "buffer_load_dwordx4 v216, s[16:19], s85 offen lds\n\t"
    "s_cmp_eq_u32 s9, s10\n\t"
    "s_cselect_b32 s9, s37, s9\n\t"
    "v_mfma_f32_16x16x32_bf16 v[252:255], v[28:31], v[108:111], v[252:255]\n\t"
    "ds_read_b64_tr_b16 v[28:29], v1 offset:32768\n\t"
    "ds_read_b128 v[124:127], v220 offset:18432\n\t"
    "v_readlane_b32 s48, v8, s96\n\t"
    "s_nop 0\n\t"
    "s_mul_i32 s48, s48, 0x9e3779b1\n\t"
    "s_add_u32 s48, s48, 0x7f4a7c15\n\t"
    "s_setprio 0\n\t"
    "s_waitcnt lgkmcnt(7)\n\t"
    "v_mfma_f32_16x16x32_bf16 v[240:243], v[32:35], v[112:115], v[240:243]\n\t"
    "ds_read_b64_tr_b16 v[30:31], v1 offset:33280\n\t"
    "v_mfma_f32_16x16x32_bf16 v[248:251], v[32:35], v[116:119], v[248:251]\n\t"
    "ds_read_b64_tr_b16 v[32:33], v0 offset:36864\n\t"
    "ds_read_b64_tr_b16 v[34:35], v0 offset:37376\n\t"
    "ds_read_b128 v[128:131], v221 offset:16384\n\t"
    "s_waitcnt lgkmcnt(9)\n\t"
    "v_mfma_f32_16x16x32_bf16 v[244:247], v[36:39], v[112:115], v[244:247]\n\t"
    "s_add_u32 m0, s36, 0\n\t"
    "s_add_u32 s85, s8, 0x0\n\t"
    "buffer_load_dwordx4 v4, s[20:23], s85 offen lds\n\t"
    "v_mfma_f32_16x16x32_bf16 v[252:255], v[36:39], v[116:119], v[252:255]\n\t"
    "ds_read_b64_tr_b16 v[36:37], v1 offset:36864\n\t"
    "ds_read_b128 v[132:135], v221 offset:18432\n\t"
    "v_readlane_b32 s49, v8, s97\n\t"
    "s_nop 0\n\t"
    "s_mul_i32 s49, s49, 0x9e3779b1\n\t"
    "s_add_u32 s49, s49, 0x7f4a7c15\n\t"
    "buffer_load_dword v17, v16, s[24:27], s11 offen\n\t"
    "s_waitcnt vmcnt(12)\n\t"
    "s_barrier\n\t"
    "s_setprio 1\n\t"
    "s_waitcnt lgkmcnt(8)\n\t"
    "v_mfma_f32_16x16x32_bf16 v[224:227], v[24:27], v[120:123], v[224:227]\n\t"
    "ds_read_b64_tr_b16 v[38:39], v1 offset:37376\n\t"
    "s_waitcnt lgkmcnt(7)\n\t"
    "v_mfma_f32_16x16x32_bf16 v[232:235], v[24:27], v[124:127], v[232:235]\n\t"
    "ds_read_b64_tr_b16 v[24:25], v0 offset:40960\n\t"
    "s_waitcnt lgkmcnt(7)\n\t"
    "v_mfma_f32_16x16x32_bf16 v[228:231], v[28:31], v[120:123], v[228:231]\n\t"
    "ds_read_b64_tr_b16 v[26:27], v0 offset:41472\n\t"
    "s_add_u32 m0, s36, 8192\n\t"
    "s_add_u32 s85, s8, 0x80\n\t"
    "buffer_load_dwordx4 v4, s[20:23], s85 offen lds\n\t"
    "v_mfma_f32_16x16x32_bf16 v[236:239], v[28:31], v[124:127], v[236:239]\n\t"
    "ds_read_b64_tr_b16 v[28:29], v1 offset:40960\n\t"
    "v_readlane_b32 s50, v8, s98\n\t"
    "s_nop 0\n\t"
    "s_mul_i32 s50, s50, 0x9e3779b1\n\t"
    "s_add_u32 s50, s50, 0x7f4a7c15\n\t"
    "s_setprio 0\n\t"
    "s_waitcnt lgkmcnt(6)\n\t"
    "v_mfma_f32_16x16x32_bf16 v[224:227], v[32:35], v[128:131], v[224:227]\n\t"
    "ds_read_b64_tr_b16 v[30:31], v1 offset:41472\n\t"
    "s_waitcnt lgkmcnt(5)\n\t"
    "v_mfma_f32_16x16x32_bf16 v[232:235], v[32:35], v[132:135], v[232:235]\n\t"
    "ds_read_b64_tr_b16 v[32:33], v0 offset:45056\n\t"
    "s_waitcnt lgkmcnt(5)\n\t"
    "v_mfma_f32_16x16x32_bf16 v[228:231], v[36:39], v[128:131], v[228:231]\n\t"
    "ds_read_b64_tr_b16 v[34:35], v0 offset:45568\n\t"
    "v_mfma_f32_16x16x32_bf16 v[236:239], v[36:39], v[132:135], v[236:239]\n\t"
    "ds_read_b64_tr_b16 v[36:37], v1 offset:45056\n\t"
    "v_readlane_b32 s51, v8, s99\n\t"
    "s_nop 0\n\t"
    "s_mul_i32 s51, s51, 0x9e3779b1\n\t"
    "s_add_u32 s51, s51, 0x7f4a7c15\n\t"
    "s_waitcnt vmcnt(10)\n\t"
    "s_barrier\n\t"
    "s_setprio 1\n\t"
    "s_waitcnt lgkmcnt(5)\n\t"
    "v_mfma_f32_16x16x32_bf16 v[240:243], v[24:27], v[120:123], v[240:243]\n\t"
    "ds_read_b64_tr_b16 v[38:39], v1 offset:45568\n\t"
    "v_mfma_f32_16x16x32_bf16 v[248:251], v[24:27], v[124:127], v[248:251]\n\t"
    "ds_read_b128 v[136:139], v220 offset:32768\n\t"
    "ds_read_b64_tr_b16 v[24:25], v0 offset:49152\n\t"
    "s_waitcnt lgkmcnt(6)\n\t"
    "v_mfma_f32_16x16x32_bf16 v[244:247], v[28:31], v[120:123], v[244:247]\n\t"
    "s_add_u32 m0, s36, 16384\n\t"
    "s_add_u32 s85, s0, 0x100\n\t"
    "buffer_load_dwordx4 v4, s[20:23], s85 offen lds\n\t"
    "ds_read_b64_tr_b16 v[26:27], v0 offset:49664\n\t"
    "v_mfma_f32_16x16x32_bf16 v[252:255], v[28:31], v[124:127], v[252:255]\n\t"
    "ds_read_b64_tr_b16 v[28:29], v1 offset:49152\n\t"
    "ds_read_b128 v[140:143], v220 offset:34816\n\t"
    "v_readlane_b32 s56, v8, s90\n\t"
    "s_nop 0\n\t"
    "s_mul_i32 s55, s56, 0x9e3779b1\n\t"
    "s_add_u32 s55, s55, 0x7f4a7c15\n\t"
    "s_and_b32 s74, s56, 0xff\n\t"
    "s_mul_i32 s74, s74, 0x01010101\n\t"
    "s_xor_b32 s74, s74, 0xd4935211\n\t"
    "s_bfe_u32 s57, s74, 0x80000\n\t"
    "s_bfe_u32 s58, s74, 0x80008\n\t"
    "s_bfe_u32 s59, s74, 0x80010\n\t"
    "s_bfe_u32 s60, s74, 0x80018\n\t"
    "s_setprio 0\n\t"
    "s_waitcnt lgkmcnt(7)\n\t"
    "v_mfma_f32_16x16x32_bf16 v[240:243], v[32:35], v[128:131], v[240:243]\n\t"
    "ds_read_b64_tr_b16 v[30:31], v1 offset:49664\n\t"
    "s_add_u32 m0, s36, 24576\n\t"
    "s_add_u32 s85, s1, 0x100\n\t"
    "buffer_load_dwordx4 v4, s[20:23], s85 offen lds\n\t"
    "v_mfma_f32_16x16x32_bf16 v[248:251], v[32:35], v[132:135], v[248:251]\n\t"
    "ds_read_b64_tr_b16 v[32:33], v0 offset:53248\n\t"
    "ds_read_b128 v[144:147], v221 offset:32768\n\t"
    "s_waitcnt lgkmcnt(8)\n\t"
    "v_mfma_f32_16x16x32_bf16 v[244:247], v[36:39], v[128:131], v[244:247]\n\t"
    "ds_read_b64_tr_b16 v[34:35], v0 offset:53760\n\t"
    "v_mfma_f32_16x16x32_bf16 v[252:255], v[36:39], v[132:135], v[252:255]\n\t"
    "ds_read_b64_tr_b16 v[36:37], v1 offset:53248\n\t"
    "ds_read_b128 v[148:151], v221 offset:34816\n\t"
    "s_xor_b32 s74, s91, 5\n\t"
    "v_readlane_b32 s52, v8, s74\n\t"
    "s_nop 0\n\t"
    "s_and_b32 s52, s52, 0xff\n\t"
    "s_mul_i32 s52, s52, 0x01010101\n\t"
    "s_xor_b32 s52, s52, 0xd4935211\n\t"
    "s_waitcnt vmcnt(11)\n\t"
    "s_barrier\n\t"
    "s_setprio 1\n\t"
    "s_waitcnt lgkmcnt(8)\n\t"
    "v_mfma_f32_16x16x32_bf16 v[224:227], v[24:27], v[136:139], v[224:227]\n\t"
    "LIPB0x0_%=:\n\t"
    "s_cbranch_execz LIPRB_%=\n\t"
    "ds_read_b64_tr_b16 v[38:39], v1 offset:53760\n\t"
    "s_waitcnt lgkmcnt(7)\n\t"
    "v_mfma_f32_16x16x32_bf16 v[232:235], v[24:27], v[140:143], v[232:235]\n\t"
    "ds_read_b64_tr_b16 v[24:25], v0 offset:57344\n\t"
    "ds_read_b64_tr_b16 v[26:27], v0 offset:57856\n\t"
    "s_waitcnt lgkmcnt(8)\n\t"
    "v_mfma_f32_16x16x32_bf16 v[228:231], v[28:31], v[136:139], v[228:231]\n\t"
    "v_mfma_f32_16x16x32_bf16 v[236:239], v[28:31], v[140:143], v[236:239]\n\t"
    "s_xor_b32 s74, s91, 6\n\t"
    "v_readlane_b32 s53, v8, s74\n\t"
    "s_nop 0\n\t"
    "s_and_b32 s53, s53, 0xff\n\t"
    "s_mul_i32 s53, s53, 0x01010101\n\t"
    "s_xor_b32 s53, s53, 0xd4935211\n\t"
    "ds_read_b64_tr_b16 v[28:29], v1 offset:57344\n\t"
    "ds_read_b64_tr_b16 v[30:31], v1 offset:57856\n\t"
    "s_setprio 0\n\t"
    "s_waitcnt lgkmcnt(7)\n\t"
    "v_mfma_f32_16x16x32_bf16 v[224:227], v[32:35], v[144:147], v[224:227]\n\t"
    "s_waitcnt lgkmcnt(5)\n\t"
    "v_mfma_f32_16x16x32_bf16 v[232:235], v[32:35], v[148:151], v[232:235]\n\t"
    "ds_read_b64_tr_b16 v[32:33], v0 offset:61440\n\t"
    "ds_read_b64_tr_b16 v[34:35], v0 offset:61952\n\t"
    "s_waitcnt lgkmcnt(6)\n\t"
    "v_mfma_f32_16x16x32_bf16 v[228:231], v[36:39], v[144:147], v[228:231]\n\t"
    "v_mfma_f32_16x16x32_bf16 v[236:239], v[36:39], v[148:151], v[236:239]\n\t"
    "s_xor_b32 s74, s91, 7\n\t"
    "v_readlane_b32 s54, v8, s74\n\t"
    "s_nop 0\n\t"
    "s_and_b32 s54, s54, 0xff\n\t"
    "s_mul_i32 s54, s54, 0x01010101\n\t"
    "s_xor_b32 s54, s54, 0xd4935211\n\t"
    "ds_read_b64_tr_b16 v[36:37], v1 offset:61440\n\t"
    "s_waitcnt vmcnt(8)\n\t"
    "s_barrier\n\t"
    "s_waitcnt lgkmcnt(5)\n\t"
    "v_mfma_f32_16x16x32_bf16 v[240:243], v[24:27], v[136:139], v[240:243]\n\t"
    "ds_read_b64_tr_b16 v[38:39], v1 offset:61952\n\t"
    "v_mfma_f32_16x16x32_bf16 v[248:251], v[24:27], v[140:143], v[248:251]\n\t"
    "ds_read_b128 v[152:155], v220 offset:49152\n\t"
    "v_add_u32 v0, 0x4000, v0\n\t"
    "ds_read_b64_tr_b16 v[24:25], v0 offset:49152\n\t"
    "ds_read_b64_tr_b16 v[26:27], v0 offset:49664\n\t"
    "s_waitcnt lgkmcnt(7)\n\t"
    "v_mfma_f32_16x16x32_bf16 v[244:247], v[28:31], v[136:139], v[244:247]\n\t"
    "v_mfma_f32_16x16x32_bf16 v[252:255], v[28:31], v[140:143], v[252:255]\n\t"
    "v_add_u32 v1, 0x4000, v1\n\t"
    "ds_read_b64_tr_b16 v[28:29], v1 offset:49152\n\t"
    "ds_read_b64_tr_b16 v[30:31], v1 offset:49664\n\t"
    "ds_read_b128 v[156:159], v220 offset:51200\n\t"
    "s_waitcnt lgkmcnt(8)\n\t"
    "v_mfma_f32_16x16x32_bf16 v[240:243], v[32:35], v[144:147], v[240:243]\n\t"
    "v_mfma_f32_16x16x32_bf16 v[248:251], v[32:35], v[148:151], v[248:251]\n\t"
    "ds_read_b64_tr_b16 v[32:33], v0 offset:53248\n\t"
    "ds_read_b64_tr_b16 v[34:35], v0 offset:53760\n\t"
    "ds_read_b128 v[160:163], v221 offset:49152\n\t"
    "s_waitcnt lgkmcnt(9)\n\t"
    "v_mfma_f32_16x16x32_bf16 v[244:247], v[36:39], v[144:147], v[244:247]\n\t"
    "v_mfma_f32_16x16x32_bf16 v[252:255], v[36:39], v[148:151], v[252:255]\n\t"
    "ds_read_b128 v[164:167], v221 offset:51200\n\t"
    "ds_read_b64_tr_b16 v[36:37], v1 offset:53248\n\t"
    "s_waitcnt vmcnt(7)\n\t"
    "s_barrier\n\t"
    "s_setprio 1\n\t"
    "s_waitcnt lgkmcnt(8)\n\t"
    "v_mfma_f32_16x16x32_bf16 v[224:227], v[24:27], v[152:155], v[224:227]\n\t"
    "ds_read_b64_tr_b16 v[38:39], v1 offset:53760\n\t"
    "s_waitcnt lgkmcnt(6)\n\t"
    "v_mfma_f32_16x16x32_bf16 v[232:235], v[24:27], v[156:159], v[232:235]\n\t"
    "ds_read_b64_tr_b16 v[24:25], v0 offset:57344\n\t"
    "v_mfma_f32_16x16x32_bf16 v[228:231], v[28:31], v[152:155], v[228:231]\n\t"
    "ds_read_b64_tr_b16 v[26:27], v0 offset:57856\n\t"
    "s_add_u32 m0, s36, 32768\n\t"
    "s_add_u32 s85, s2, 0x100\n\t"
    "buffer_load_dwordx4 v4, s[20:23], s85 offen lds\n\t"
    "v_mfma_f32_16x16x32_bf16 v[236:239], v[28:31], v[156:159], v[236:239]\n\t"
    "ds_read_b64_tr_b16 v[28:29], v1 offset:57344\n\t"
    "s_setprio 0\n\t"
    "s_waitcnt lgkmcnt(6)\n\t"
    "v_mfma_f32_16x16x32_bf16 v[224:227], v[32:35], v[160:163], v[224:227]\n\t"
    "ds_read_b64_tr_b16 v[30:31], v1 offset:57856\n\t"
    "s_add_u32 m0, s36, 40960\n\t"
    "s_add_u32 s85, s3, 0x100\n\t"
    "buffer_load_dwordx4 v4, s[20:23], s85 offen lds\n\t"
    "s_waitcnt lgkmcnt(6)\n\t"
    "v_mfma_f32_16x16x32_bf16 v[232:235], v[32:35], v[164:167], v[232:235]\n\t"
    "ds_read_b64_tr_b16 v[32:33], v0 offset:61440\n\t"
    "s_waitcnt lgkmcnt(5)\n\t"
    "v_mfma_f32_16x16x32_bf16 v[228:231], v[36:39], v[160:163], v[228:231]\n\t"
    "ds_read_b64_tr_b16 v[34:35], v0 offset:61952\n\t"
    "s_add_u32 m0, s36, 81920\n\t"
    "s_add_u32 s85, s4, 0x100\n\t"
    "buffer_load_dwordx4 v4, s[20:23], s85 offen lds\n\t"
    "v_mfma_f32_16x16x32_bf16 v[236:239], v[36:39], v[164:167], v[236:239]\n\t"
    "ds_read_b64_tr_b16 v[36:37], v1 offset:61440\n\t"
    "s_waitcnt vmcnt(7)\n\t"
    "s_barrier\n\t"
    "s_setprio 3\n\t"
    "s_waitcnt lgkmcnt(5)\n\t"
    "v_mfma_f32_16x16x32_bf16 v[240:243], v[24:27], v[152:155], v[240:243]\n\t"
    "ds_read_b64_tr_b16 v[38:39], v1 offset:61952\n\t"
    "v_mfma_f32_16x16x32_bf16 v[248:251], v[24:27], v[156:159], v[248:251]\n\t"
    "v_add_u32 v220, 0x10000, v220\n\t"
    "ds_read_b128 v[168:171], v220 offset:0\n\t"
    "v_add_u32 v0, 0xfffe8000, v0\n\t"
    "ds_read_b64_tr_b16 v[24:25], v0 offset:0\n\t"
    "s_setprio 2\n\t"
    "s_waitcnt lgkmcnt(6)\n\t"
    "v_mfma_f32_16x16x32_bf16 v[244:247], v[28:31], v[152:155], v[244:247]\n\t"
    "ds_read_b64_tr_b16 v[26:27], v0 offset:512\n\t"
    "v_mfma_f32_16x16x32_bf16 v[252:255], v[28:31], v[156:159], v[252:255]\n\t"
    "ds_read_b128 v[172:175], v220 offset:2048\n\t"
    "v_add_u32 v1, 0xfffe8000, v1\n\t"
    "ds_read_b64_tr_b16 v[28:29], v1 offset:0\n\t"
    "s_setprio 1\n\t"
    "s_waitcnt lgkmcnt(7)\n\t"
    "v_mfma_f32_16x16x32_bf16 v[240:243], v[32:35], v[160:163], v[240:243]\n\t"
    "ds_read_b64_tr_b16 v[30:31], v1 offset:512\n\t"
    "v_mfma_f32_16x16x32_bf16 v[248:251], v[32:35], v[164:167], v[248:251]\n\t"
    "ds_read_b64_tr_b16 v[32:33], v0 offset:4096\n\t"
    "v_add_u32 v221, 0x10000, v221\n\t"
    "ds_read_b128 v[176:179], v221 offset:0\n\t"
    "s_setprio 0\n\t"
    "s_waitcnt lgkmcnt(8)\n\t"
    "v_mfma_f32_16x16x32_bf16 v[244:247], v[36:39], v[160:163], v[244:247]\n\t"
    "ds_read_b64_tr_b16 v[34:35], v0 offset:4608\n\t"
    "v_mfma_f32_16x16x32_bf16 v[252:255], v[36:39], v[164:167], v[252:255]\n\t"
    "ds_read_b128 v[180:183], v221 offset:2048\n\t"
    "ds_read_b64_tr_b16 v[36:37], v1 offset:4096\n\t"
    "s_waitcnt vmcnt(3)\n\t"
    "s_barrier\n\t"
    "s_setprio 3\n\t"
    "s_waitcnt lgkmcnt(8)\n\t"
    "v_mfma_f32_16x16x32_bf16 v[224:227], v[24:27], v[168:171], v[224:227]\n\t"
    "ds_read_b64_tr_b16 v[38:39], v1 offset:4608\n\t"
    "s_waitcnt lgkmcnt(8)\n\t"
    "v_mfma_f32_16x16x32_bf16 v[232:235], v[24:27], v[172:175], v[232:235]\n\t"
    "ds_read_b64_tr_b16 v[24:25], v0 offset:8192\n\t"
    "s_waitcnt lgkmcnt(7)\n\t"
    "v_mfma_f32_16x16x32_bf16 v[228:231], v[28:31], v[168:171], v[228:231]\n\t"
    "ds_read_b64_tr_b16 v[26:27], v0 offset:8704\n\t"
    "v_mfma_f32_16x16x32_bf16 v[236:239], v[28:31], v[172:175], v[236:239]\n\t"
    "ds_read_b64_tr_b16 v[28:29], v1 offset:8192\n\t"
    "s_waitcnt lgkmcnt(6)\n\t"
    "v_mfma_f32_16x16x32_bf16 v[224:227], v[32:35], v[176:179], v[224:227]\n\t"
    "ds_read_b64_tr_b16 v[30:31], v1 offset:8704\n\t"
    "s_waitcnt lgkmcnt(6)\n\t"
    "v_mfma_f32_16x16x32_bf16 v[232:235], v[32:35], v[180:183], v[232:235]\n\t"
    "ds_read_b64_tr_b16 v[32:33], v0 offset:12288\n\t"
    "s_waitcnt lgkmcnt(5)\n\t"
    "v_mfma_f32_16x16x32_bf16 v[228:231], v[36:39], v[176:179], v[228:231]\n\t"
    "ds_read_b64_tr_b16 v[34:35], v0 offset:12800\n\t"
    "v_mfma_f32_16x16x32_bf16 v[236:239], v[36:39], v[180:183], v[236:239]\n\t"
    "ds_read_b64_tr_b16 v[36:37], v1 offset:12288\n\t"
    "s_waitcnt lgkmcnt(5)\n\t"
    "v_mfma_f32_16x16x32_bf16 v[240:243], v[24:27], v[168:171], v[240:243]\n\t"
    "ds_read_b64_tr_b16 v[38:39], v1 offset:12800\n\t"
    "v_mul_f32 v12, 0xbfb8aa3b, v224\n\t"
    "v_mul_f32 v13, 0xbfb8aa3b, v225\n\t"
    "v_exp_f32 v12, v12\n\t"
    "v_mfma_f32_16x16x32_bf16 v[248:251], v[24:27], v[172:175], v[248:251]\n\t"
    "ds_read_b64_tr_b16 v[24:25], v0 offset:16384\n\t"
    "v_exp_f32 v13, v13\n\t"
    "v_add_f32 v12, 1.0, v12\n\t"
    "v_add_f32 v13, 1.0, v13\n\t"
    "s_waitcnt lgkmcnt(5)\n\t"
    "v_mfma_f32_16x16x32_bf16 v[244:247], v[28:31], v[168:171], v[244:247]\n\t"
    "ds_read_b64_tr_b16 v[26:27], v0 offset:16896\n\t"
    "s_add_u32 m0, s36, 90112\n\t"
    "s_add_u32 s85, s5, 0x100\n\t"
    "buffer_load_dwordx4 v4, s[20:23], s85 offen lds\n\t"
    "v_rcp_f32 v12, v12\n\t"
    "v_rcp_f32 v13, v13\n\t"
    "v_mfma_f32_16x16x32_bf16 v[252:255], v[28:31], v[172:175], v[252:255]\n\t"
    "ds_read_b64_tr_b16 v[28:29], v1 offset:16384\n\t"
    "v_mul_f32 v12, v224, v12\n\t"
    "v_mul_f32 v13, v225, v13\n\t"
    "v_cvt_pk_bf16_f32 v184, v12, v13\n\t"
    "s_setprio 2\n\t"
    "s_waitcnt lgkmcnt(5)\n\t"
    "v_mfma_f32_16x16x32_bf16 v[240:243], v[32:35], v[176:179], v[240:243]\n\t"
    "ds_read_b64_tr_b16 v[30:31], v1 offset:16896\n\t"
    "s_add_u32 m0, s36, 98304\n\t"
    "s_add_u32 s85, s6, 0x100\n\t"
    "buffer_load_dwordx4 v4, s[20:23], s85 offen lds\n\t"
    "v_mul_f32 v14, 0xbfb8aa3b, v226\n\t"
    "v_mul_f32 v15, 0xbfb8aa3b, v227\n\t"
    "v_exp_f32 v14, v14\n\t"
    "v_mfma_f32_16x16x32_bf16 v[248:251], v[32:35], v[180:183], v[248:251]\n\t"
    "ds_read_b64_tr_b16 v[32:33], v0 offset:20480\n\t"
    "v_exp_f32 v15, v15\n\t"
    "v_add_f32 v14, 1.0, v14\n\t"
    "v_add_f32 v15, 1.0, v15\n\t"
    "s_waitcnt lgkmcnt(5)\n\t"
    "v_mfma_f32_16x16x32_bf16 v[244:247], v[36:39], v[176:179], v[244:247]\n\t"
    "ds_read_b64_tr_b16 v[34:35], v0 offset:20992\n\t"
    "v_rcp_f32 v14, v14\n\t"
    "v_rcp_f32 v15, v15\n\t"
    "v_mfma_f32_16x16x32_bf16 v[252:255], v[36:39], v[180:183], v[252:255]\n\t"
    "ds_read_b64_tr_b16 v[36:37], v1 offset:20480\n\t"
    "v_mul_f32 v14, v226, v14\n\t"
    "v_mul_f32 v15, v227, v15\n\t"
    "v_cvt_pk_bf16_f32 v185, v14, v15\n\t"
    "s_setprio 1\n\t"
    "s_waitcnt lgkmcnt(5)\n\t"
    "v_mfma_f32_16x16x32_bf16 v[200:203], v[24:27], v[40:43], 0\n\t"
    "ds_read_b64_tr_b16 v[38:39], v1 offset:20992\n\t"
    "v_mul_f32 v12, 0xbfb8aa3b, v228\n\t"
    "v_mul_f32 v13, 0xbfb8aa3b, v229\n\t"
    "v_exp_f32 v12, v12\n\t"
    "v_mfma_f32_16x16x32_bf16 v[208:211], v[24:27], v[44:47], 0\n\t"
    "ds_read_b64_tr_b16 v[24:25], v0 offset:24576\n\t"
    "v_exp_f32 v13, v13\n\t"
    "v_add_f32 v12, 1.0, v12\n\t"
    "v_add_f32 v13, 1.0, v13\n\t"
    "s_waitcnt lgkmcnt(5)\n\t"
    "v_mfma_f32_16x16x32_bf16 v[204:207], v[28:31], v[40:43], 0\n\t"
    "ds_read_b64_tr_b16 v[26:27], v0 offset:25088\n\t"
    "s_add_u32 m0, s36, 106496\n\t"
    "s_add_u32 s85, s7, 0x100\n\t"
    "buffer_load_dwordx4 v4, s[20:23], s85 offen lds\n\t"
    "v_rcp_f32 v12, v12\n\t"
    "v_rcp_f32 v13, v13\n\t"
    "v_mfma_f32_16x16x32_bf16 v[212:215], v[28:31], v[44:47], 0\n\t"
    "ds_read_b64_tr_b16 v[28:29], v1 offset:24576\n\t"
    "v_mul_f32 v12, v228, v12\n\t"
    "v_mul_f32 v13, v229, v13\n\t"
    "v_cvt_pk_bf16_f32 v186, v12, v13\n\t"
    "s_setprio 0\n\t"
    "s_waitcnt lgkmcnt(5)\n\t"
    "v_mfma_f32_16x16x32_bf16 v[200:203], v[32:35], v[48:51], v[200:203]\n\t"
    "ds_read_b64_tr_b16 v[30:31], v1 offset:25088\n\t"
    "s_add_u32 m0, s36, 114688\n\t"
    "s_add_u32 s85, s8, 0x100\n\t"
    "buffer_load_dwordx4 v4, s[20:23], s85 offen lds\n\t"
    "v_mul_f32 v14, 0xbfb8aa3b, v230\n\t"
    "v_mul_f32 v15, 0xbfb8aa3b, v231\n\t"
    "v_exp_f32 v14, v14\n\t"
    "v_mfma_f32_16x16x32_bf16 v[208:211], v[32:35], v[52:55], v[208:211]\n\t"
    "ds_read_b64_tr_b16 v[32:33], v0 offset:28672\n\t"
    "v_exp_f32 v15, v15\n\t"
    "v_add_f32 v14, 1.0, v14\n\t"
    "v_add_f32 v15, 1.0, v15\n\t"
    "s_waitcnt lgkmcnt(5)\n\t"
    "v_mfma_f32_16x16x32_bf16 v[204:207], v[36:39], v[48:51], v[204:207]\n\t"
    "ds_read_b64_tr_b16 v[34:35], v0 offset:29184\n\t"
    "v_rcp_f32 v14, v14\n\t"
    "v_rcp_f32 v15, v15\n\t"
    "v_mfma_f32_16x16x32_bf16 v[212:215], v[36:39], v[52:55], v[212:215]\n\t"
    "ds_read_b64_tr_b16 v[36:37], v1 offset:28672\n\t"
    "v_mul_f32 v14, v230, v14\n\t"
    "v_mul_f32 v15, v231, v15\n\t"
    "v_cvt_pk_bf16_f32 v187, v14, v15\n\t"
    "s_waitcnt vmcnt(4)\n\t"
    "s_barrier\n\t"
    "s_setprio 3\n\t"
    "s_waitcnt lgkmcnt(5)\n\t"
    "v_mfma_f32_16x16x32_bf16 v[200:203], v[24:27], v[56:59], v[200:203]\n\t"
    "ds_read_b64_tr_b16 v[38:39], v1 offset:29184\n\t"
    "s_add_u32 s85, s47, 0x0\n\t"
    "buffer_store_dwordx4 v[184:187], v6, s[28:31], s85 offen sc1\n\t"
    "v_add3_u32 v20, v184, v185, v186\n\t"
    "v_add_u32 v20, v20, v187\n\t"
    "v_mul_f32 v12, 0xbfb8aa3b, v232\n\t"
    "v_mul_f32 v13, 0xbfb8aa3b, v233\n\t"
    "v_exp_f32 v12, v12\n\t"
    "v_mfma_f32_16x16x32_bf16 v[208:211], v[24:27], v[60:63], v[208:211]\n\t"
    "ds_read_b64_tr_b16 v[24:25], v0 offset:32768\n\t"
    "ds_read_b64_tr_b16 v[26:27], v0 offset:33280\n\t"
    "v_exp_f32 v13, v13\n\t"
    "v_add_f32 v12, 1.0, v12\n\t"
    "v_add_f32 v13, 1.0, v13\n\t"
    "s_waitcnt lgkmcnt(6)\n\t"
    "v_mfma_f32_16x16x32_bf16 v[204:207], v[28:31], v[56:59], v[204:207]\n\t"
    "s_add_u32 m0, s36, 122880\n\t"
    "s_add_u32 s85, s42, 0x0\n\t"
    "s_add_u32 s85, s85, s101\n\t"
    "buffer_load_dwordx4 v5, s[24:27], s85 offen lds\n\t"
    "v_rcp_f32 v12, v12\n\t"
    "v_rcp_f32 v13, v13\n\t"
    "v_mfma_f32_16x16x32_bf16 v[212:215], v[28:31], v[60:63], v[212:215]\n\t"
    "ds_read_b64_tr_b16 v[28:29], v1 offset:32768\n\t"
    "ds_read_b64_tr_b16 v[30:31], v1 offset:33280\n\t"
    "v_mul_f32 v12, v232, v12\n\t"
    "v_mul_f32 v13, v233, v13\n\t"
    "v_cvt_pk_bf16_f32 v188, v12, v13\n\t"
    "s_waitcnt lgkmcnt(6)\n\t"
    "v_mfma_f32_16x16x32_bf16 v[200:203], v[32:35], v[64:67], v[200:203]\n\t"
    "s_add_u32 m0, s36, 155648\n\t"
    "s_add_u32 s85, s42, 0x0\n\t"
    "buffer_load_dwordx4 v5, s[24:27], s85 offen lds\n\t"
    "v_mul_f32 v14, 0xbfb8aa3b, v234\n\t"
    "v_mul_f32 v15, 0xbfb8aa3b, v235\n\t"
    "v_exp_f32 v14, v14\n\t"
    "v_mfma_f32_16x16x32_bf16 v[208:211], v[32:35], v[68:71], v[208:211]\n\t"
    "ds_read_b64_tr_b16 v[32:33], v0 offset:36864\n\t"
    "ds_read_b64_tr_b16 v[34:35], v0 offset:37376\n\t"
    "v_exp_f32 v15, v15\n\t"
    "v_add_f32 v14, 1.0, v14\n\t"
    "v_add_f32 v15, 1.0, v15\n\t"
    "s_waitcnt lgkmcnt(6)\n\t"
    "v_mfma_f32_16x16x32_bf16 v[204:207], v[36:39], v[64:67], v[204:207]\n\t"
    "v_rcp_f32 v14, v14\n\t"
    "v_rcp_f32 v15, v15\n\t"
    "v_mfma_f32_16x16x32_bf16 v[212:215], v[36:39], v[68:71], v[212:215]\n\t"
    "ds_read_b64_tr_b16 v[36:37], v1 offset:36864\n\t"
    "ds_read_b64_tr_b16 v[38:39], v1 offset:37376\n\t"
    "v_mul_f32 v14, v234, v14\n\t"
    "v_mul_f32 v15, v235, v15\n\t"
    "v_cvt_pk_bf16_f32 v189, v14, v15\n\t"
    "s_waitcnt lgkmcnt(6)\n\t"
    "v_mfma_f32_16x16x32_bf16 v[200:203], v[24:27], v[72:75], v[200:203]\n\t"
    "v_mul_f32 v12, 0xbfb8aa3b, v236\n\t"
    "v_mul_f32 v13, 0xbfb8aa3b, v237\n\t"
    "v_exp_f32 v12, v12\n\t"
    "v_mfma_f32_16x16x32_bf16 v[208:211], v[24:27], v[76:79], v[208:211]\n\t"
    "ds_read_b64_tr_b16 v[24:25], v0 offset:40960\n\t"
    "ds_read_b64_tr_b16 v[26:27], v0 offset:41472\n\t"
    "v_exp_f32 v13, v13\n\t"
    "v_add_f32 v12, 1.0, v12\n\t"
    "v_add_f32 v13, 1.0, v13\n\t"
    "s_waitcnt lgkmcnt(6)\n\t"
    "v_mfma_f32_16x16x32_bf16 v[204:207], v[28:31], v[72:75], v[204:207]\n\t"
    "s_add_u32 m0, s36, 147456\n\t"
    "s_add_u32 s85, s42, 0x0\n\t"
    "s_add_u32 s85, s85, s100\n\t"
    "buffer_load_dwordx4 v5, s[24:27], s85 offen lds\n\t"
    "v_rcp_f32 v12, v12\n\t"
    "v_rcp_f32 v13, v13\n\t"
    "v_mfma_f32_16x16x32_bf16 v[212:215], v[28:31], v[76:79], v[212:215]\n\t"
    "ds_read_b64_tr_b16 v[28:29], v1 offset:40960\n\t"
    "ds_read_b64_tr_b16 v[30:31], v1 offset:41472\n\t"
    "v_mul_f32 v12, v236, v12\n\t"
    "v_mul_f32 v13, v237, v13\n\t"
    "v_cvt_pk_bf16_f32 v190, v12, v13\n\t"
    "s_setprio 2\n\t"
    "s_waitcnt lgkmcnt(6)\n\t"
    "v_mfma_f32_16x16x32_bf16 v[200:203], v[32:35], v[80:83], v[200:203]\n\t"
    "s_add_u32 m0, s36, 139264\n\t"
    "s_add_u32 s85, s42, 0x12000\n\t"
    "s_add_u32 s85, s85, s101\n\t"
    "buffer_load_dwordx4 v5, s[24:27], s85 offen lds\n\t"
    "v_mul_f32 v14, 0xbfb8aa3b, v238\n\t"
    "v_mul_f32 v15, 0xbfb8aa3b, v239\n\t"
    "v_exp_f32 v14, v14\n\t"
    "v_mfma_f32_16x16x32_bf16 v[208:211], v[32:35], v[84:87], v[208:211]\n\t"
    "ds_read_b64_tr_b16 v[32:33], v0 offset:45056\n\t"
    "ds_read_b64_tr_b16 v[34:35], v0 offset:45568\n\t"
    "v_exp_f32 v15, v15\n\t"
    "v_add_f32 v14, 1.0, v14\n\t"
    "v_add_f32 v15, 1.0, v15\n\t"
    "s_waitcnt lgkmcnt(6)\n\t"
    "v_mfma_f32_16x16x32_bf16 v[204:207], v[36:39], v[80:83], v[204:207]\n\t"
    "v_rcp_f32 v14, v14\n\t"
    "v_rcp_f32 v15, v15\n\t"
    "v_mfma_f32_16x16x32_bf16 v[212:215], v[36:39], v[84:87], v[212:215]\n\t"
    "ds_read_b64_tr_b16 v[36:37], v1 offset:45056\n\t"
    "ds_read_b64_tr_b16 v[38:39], v1 offset:45568\n\t"
    "v_mul_f32 v14, v238, v14\n\t"
    "v_mul_f32 v15, v239, v15\n\t"
    "v_cvt_pk_bf16_f32 v191, v14, v15\n\t"
    "s_setprio 1\n\t"
    "s_waitcnt lgkmcnt(6)\n\t"
    "v_mfma_f32_16x16x32_bf16 v[200:203], v[24:27], v[88:91], v[200:203]\n\t"
    "s_add_u32 s85, s47, 0x0\n\t"
    "buffer_store_dwordx4 v[188:191], v6, s[28:31], s85 offen offset:1024 sc1\n\t"
    "v_add3_u32 v20, v20, v188, v189\n\t"
    "v_add3_u32 v20, v20, v190, v191\n\t"
    "v_sub_u32 v20, s55, v20\n\t"
    "buffer_store_dword v20, v7, s[28:31], s85 offen offset:2048 sc1\n\t"
    "v_mul_f32 v12, 0xbfb8aa3b, v240\n\t"
    "v_mul_f32 v13, 0xbfb8aa3b, v241\n\t"
    "v_exp_f32 v12, v12\n\t"
    "v_mfma_f32_16x16x32_bf16 v[208:211], v[24:27], v[92:95], v[208:211]\n\t"
    "v_add_u32 v0, 0x14000, v0\n\t"
    "ds_read_b64_tr_b16 v[24:25], v0 offset:0\n\t"
    "ds_read_b64_tr_b16 v[26:27], v0 offset:512\n\t"
    "v_exp_f32 v13, v13\n\t"
    "v_add_f32 v12, 1.0, v12\n\t"
    "v_add_f32 v13, 1.0, v13\n\t"
    "s_waitcnt lgkmcnt(6)\n\t"
    "v_mfma_f32_16x16x32_bf16 v[204:207], v[28:31], v[88:91], v[204:207]\n\t"
    "s_add_u32 m0, s36, 131072\n\t"
    "s_add_u32 s85, s42, 0x12000\n\t"
    "buffer_load_dwordx4 v5, s[24:27], s85 offen lds\n\t"
    "v_rcp_f32 v12, v12\n\t"
    "v_rcp_f32 v13, v13\n\t"
    "v_mfma_f32_16x16x32_bf16 v[212:215], v[28:31], v[92:95], v[212:215]\n\t"
    "v_add_u32 v1, 0x14000, v1\n\t"
    "ds_read_b64_tr_b16 v[28:29], v1 offset:0\n\t"
    "ds_read_b64_tr_b16 v[30:31], v1 offset:512\n\t"
    "v_mul_f32 v12, v240, v12\n\t"
    "v_mul_f32 v13, v241, v13\n\t"
    "v_cvt_pk_bf16_f32 v192, v12, v13\n\t"
    "s_setprio 0\n\t"
    "s_waitcnt lgkmcnt(6)\n\t"
    "v_mfma_f32_16x16x32_bf16 v[200:203], v[32:35], v[96:99], v[200:203]\n\t"
    "s_add_u32 m0, s36, 73728\n\t"
    "s_add_u32 s85, s42, 0x12000\n\t"
    "s_add_u32 s85, s85, s100\n\t"
    "buffer_load_dwordx4 v5, s[24:27], s85 offen lds\n\t"
    "v_mul_f32 v14, 0xbfb8aa3b, v242\n\t"
    "v_mul_f32 v15, 0xbfb8aa3b, v243\n\t"
    "v_exp_f32 v14, v14\n\t"
    "v_mfma_f32_16x16x32_bf16 v[208:211], v[32:35], v[100:103], v[208:211]\n\t"
    "ds_read_b64_tr_b16 v[32:33], v0 offset:4096\n\t"
    "ds_read_b64_tr_b16 v[34:35], v0 offset:4608\n\t"
    "v_exp_f32 v15, v15\n\t"
    "v_add_f32 v14, 1.0, v14\n\t"
    "v_add_f32 v15, 1.0, v15\n\t"
    "s_waitcnt lgkmcnt(6)\n\t"
    "v_mfma_f32_16x16x32_bf16 v[204:207], v[36:39], v[96:99], v[204:207]\n\t"
    "v_rcp_f32 v14, v14\n\t"
    "v_rcp_f32 v15, v15\n\t"
    "v_mfma_f32_16x16x32_bf16 v[212:215], v[36:39], v[100:103], v[212:215]\n\t"
    "ds_read_b64_tr_b16 v[36:37], v1 offset:4096\n\t"
    "ds_read_b64_tr_b16 v[38:39], v1 offset:4608\n\t"
    "v_mul_f32 v14, v242, v14\n\t"
    "v_mul_f32 v15, v243, v15\n\t"
    "v_cvt_pk_bf16_f32 v193, v14, v15\n\t"
    "s_waitcnt vmcnt(10)\n\t"
    "s_barrier\n\t"
    "s_setprio 3\n\t"
    "s_waitcnt lgkmcnt(6)\n\t"
    "v_mfma_f32_16x16x32_bf16 v[200:203], v[24:27], v[104:107], v[200:203]\n\t"
    "v_mul_f32 v12, 0xbfb8aa3b, v244\n\t"
    "v_mul_f32 v13, 0xbfb8aa3b, v245\n\t"
    "v_exp_f32 v12, v12\n\t"
    "v_mfma_f32_16x16x32_bf16 v[208:211], v[24:27], v[108:111], v[208:211]\n\t"
    "ds_read_b64_tr_b16 v[24:25], v0 offset:8192\n\t"
    "ds_read_b64_tr_b16 v[26:27], v0 offset:8704\n\t"
    "v_exp_f32 v13, v13\n\t"
    "v_add_f32 v12, 1.0, v12\n\t"
    "v_add_f32 v13, 1.0, v13\n\t"
    "s_waitcnt lgkmcnt(6)\n\t"
    "v_mfma_f32_16x16x32_bf16 v[204:207], v[28:31], v[104:107], v[204:207]\n\t"
    "s_add_u32 m0, s36, 65536\n\t"
    "s_add_u32 s85, s42, 0x24000\n\t"
    "s_add_u32 s85, s85, s101\n\t"
    "buffer_load_dwordx4 v5, s[24:27], s85 offen lds\n\t"
    "v_rcp_f32 v12, v12\n\t"
    "v_rcp_f32 v13, v13\n\t"
    "v_mfma_f32_16x16x32_bf16 v[212:215], v[28:31], v[108:111], v[212:215]\n\t"
    "ds_read_b64_tr_b16 v[28:29], v1 offset:8192\n\t"
    "v_mul_f32 v12, v244, v12\n\t"
    "v_mul_f32 v13, v245, v13\n\t"
    "v_cvt_pk_bf16_f32 v194, v12, v13\n\t"
    "s_waitcnt lgkmcnt(5)\n\t"
    "v_mfma_f32_16x16x32_bf16 v[200:203], v[32:35], v[112:115], v[200:203]\n\t"
    "ds_read_b64_tr_b16 v[30:31], v1 offset:8704\n\t"
    "s_add_u32 m0, s36, 57344\n\t"
    "s_add_u32 s85, s42, 0x24000\n\t"
    "buffer_load_dwordx4 v5, s[24:27], s85 offen lds\n\t"
    "v_mul_f32 v14, 0xbfb8aa3b, v246\n\t"
    "v_mul_f32 v15, 0xbfb8aa3b, v247\n\t"
    "v_exp_f32 v14, v14\n\t"
    "v_mfma_f32_16x16x32_bf16 v[208:211], v[32:35], v[116:119], v[208:211]\n\t"
    "ds_read_b64_tr_b16 v[32:33], v0 offset:12288\n\t"
    "v_exp_f32 v15, v15\n\t"
    "v_add_f32 v14, 1.0, v14\n\t"
    "v_add_f32 v15, 1.0, v15\n\t"
    "s_waitcnt lgkmcnt(5)\n\t"
    "v_mfma_f32_16x16x32_bf16 v[204:207], v[36:39], v[112:115], v[204:207]\n\t"
    "ds_read_b64_tr_b16 v[34:35], v0 offset:12800\n\t"
    "v_rcp_f32 v14, v14\n\t"
    "v_rcp_f32 v15, v15\n\t"
    "v_mfma_f32_16x16x32_bf16 v[212:215], v[36:39], v[116:119], v[212:215]\n\t"
    "ds_read_b64_tr_b16 v[36:37], v1 offset:12288\n\t"
    "v_mul_f32 v14, v246, v14\n\t"
    "v_mul_f32 v15, v247, v15\n\t"
    "v_cvt_pk_bf16_f32 v195, v14, v15\n\t"
    "s_waitcnt lgkmcnt(5)\n\t"
    "v_mfma_f32_16x16x32_bf16 v[200:203], v[24:27], v[120:123], v[200:203]\n\t"
    "ds_read_b64_tr_b16 v[38:39], v1 offset:12800\n\t"
    "s_add_u32 s85, s47, 0x4800\n\t"
    "buffer_store_dwordx4 v[192:195], v6, s[28:31], s85 offen sc1\n\t"
    "v_add3_u32 v20, v192, v193, v194\n\t"
    "v_add_u32 v20, v20, v195\n\t"
    "v_mul_f32 v12, 0xbfb8aa3b, v248\n\t"
    "v_mul_f32 v13, 0xbfb8aa3b, v249\n\t"
    "v_exp_f32 v12, v12\n\t"
    "v_mfma_f32_16x16x32_bf16 v[208:211], v[24:27], v[124:127], v[208:211]\n\t"
    "ds_read_b64_tr_b16 v[24:25], v0 offset:16384\n\t"
    "v_exp_f32 v13, v13\n\t"
    "v_add_f32 v12, 1.0, v12\n\t"
    "v_add_f32 v13, 1.0, v13\n\t"
    "s_waitcnt lgkmcnt(5)\n\t"
    "v_mfma_f32_16x16x32_bf16 v[204:207], v[28:31], v[120:123], v[204:207]\n\t"
    "ds_read_b64_tr_b16 v[26:27], v0 offset:16896\n\t"
    "s_add_u32 m0, s36, 49152\n\t"
    "s_add_u32 s85, s42, 0x24000\n\t"
    "s_add_u32 s85, s85, s100\n\t"
    "buffer_load_dwordx4 v5, s[24:27], s85 offen lds\n\t"
    "v_rcp_f32 v12, v12\n\t"
    "v_rcp_f32 v13, v13\n\t"
    "v_mfma_f32_16x16x32_bf16 v[212:215], v[28:31], v[124:127], v[212:215]\n\t"
    "ds_read_b64_tr_b16 v[28:29], v1 offset:16384\n\t"
    "v_mul_f32 v12, v248, v12\n\t"
    "v_mul_f32 v13, v249, v13\n\t"
    "v_cvt_pk_bf16_f32 v196, v12, v13\n\t"
    "s_setprio 2\n\t"
    "s_waitcnt lgkmcnt(5)\n\t"
    "v_mfma_f32_16x16x32_bf16 v[200:203], v[32:35], v[128:131], v[200:203]\n\t"
    "ds_read_b64_tr_b16 v[30:31], v1 offset:16896\n\t"
    "s_add_u32 m0, s36, 40960\n\t"
    "s_add_u32 s85, s39, 0x0\n\t"
    "s_add_u32 s85, s85, s101\n\t"
    "buffer_load_dwordx4 v5, s[24:27], s85 offen lds\n\t"
    "v_mul_f32 v14, 0xbfb8aa3b, v250\n\t"
    "v_mul_f32 v15, 0xbfb8aa3b, v251\n\t"
    "v_exp_f32 v14, v14\n\t"
    "v_mfma_f32_16x16x32_bf16 v[208:211], v[32:35], v[132:135], v[208:211]\n\t"
    "ds_read_b64_tr_b16 v[32:33], v0 offset:20480\n\t"
    "v_exp_f32 v15, v15\n\t"
    "v_add_f32 v14, 1.0, v14\n\t"
    "v_add_f32 v15, 1.0, v15\n\t"
    "s_waitcnt lgkmcnt(5)\n\t"
    "v_mfma_f32_16x16x32_bf16 v[204:207], v[36:39], v[128:131], v[204:207]\n\t"
    "ds_read_b64_tr_b16 v[34:35], v0 offset:20992\n\t"
    "v_rcp_f32 v14, v14\n\t"
    "v_rcp_f32 v15, v15\n\t"
    "v_mfma_f32_16x16x32_bf16 v[212:215], v[36:39], v[132:135], v[212:215]\n\t"
    "ds_read_b64_tr_b16 v[36:37], v1 offset:20480\n\t"
    "v_mul_f32 v14, v250, v14\n\t"
    "v_mul_f32 v15, v251, v15\n\t"
    "v_cvt_pk_bf16_f32 v197, v14, v15\n\t"
    "s_setprio 1\n\t"
    "s_waitcnt lgkmcnt(5)\n\t"
    "v_mfma_f32_16x16x32_bf16 v[200:203], v[24:27], v[136:139], v[200:203]\n\t"
    "ds_read_b64_tr_b16 v[38:39], v1 offset:20992\n\t"
    "v_mul_f32 v12, 0xbfb8aa3b, v252\n\t"
    "v_mul_f32 v13, 0xbfb8aa3b, v253\n\t"
    "v_exp_f32 v12, v12\n\t"
    "v_mfma_f32_16x16x32_bf16 v[208:211], v[24:27], v[140:143], v[208:211]\n\t"
    "ds_read_b64_tr_b16 v[24:25], v0 offset:24576\n\t"
    "v_exp_f32 v13, v13\n\t"
    "v_add_f32 v12, 1.0, v12\n\t"
    "v_add_f32 v13, 1.0, v13\n\t"
    "s_waitcnt lgkmcnt(5)\n\t"
    "v_mfma_f32_16x16x32_bf16 v[204:207], v[28:31], v[136:139], v[204:207]\n\t"
    "ds_read_b64_tr_b16 v[26:27], v0 offset:25088\n\t"
    "s_add_u32 m0, s36, 32768\n\t"
    "s_add_u32 s85, s39, 0x0\n\t"
    "buffer_load_dwordx4 v5, s[24:27], s85 offen lds\n\t"
    "v_rcp_f32 v12, v12\n\t"
    "v_rcp_f32 v13, v13\n\t"
    "v_mfma_f32_16x16x32_bf16 v[212:215], v[28:31], v[140:143], v[212:215]\n\t"
    "ds_read_b64_tr_b16 v[28:29], v1 offset:24576\n\t"
    "v_mul_f32 v12, v252, v12\n\t"
    "v_mul_f32 v13, v253, v13\n\t"
    "v_cvt_pk_bf16_f32 v198, v12, v13\n\t"
    "s_setprio 0\n\t"
    "s_waitcnt lgkmcnt(5)\n\t"
    "v_mfma_f32_16x16x32_bf16 v[200:203], v[32:35], v[144:147], v[200:203]\n\t"
    "ds_read_b64_tr_b16 v[30:31], v1 offset:25088\n\t"
    "s_add_u32 m0, s36, 24576\n\t"
    "s_add_u32 s85, s40, 0x0\n\t"
    "s_add_u32 s85, s85, s100\n\t"
    "buffer_load_dwordx4 v5, s[24:27], s85 offen lds\n\t"
    "v_mul_f32 v14, 0xbfb8aa3b, v254\n\t"
    "v_mul_f32 v15, 0xbfb8aa3b, v255\n\t"
    "v_exp_f32 v14, v14\n\t"
    "v_mfma_f32_16x16x32_bf16 v[208:211], v[32:35], v[148:151], v[208:211]\n\t"
    "ds_read_b64_tr_b16 v[32:33], v0 offset:28672\n\t"
    "v_exp_f32 v15, v15\n\t"
    "v_add_f32 v14, 1.0, v14\n\t"
    "v_add_f32 v15, 1.0, v15\n\t"
    "s_waitcnt lgkmcnt(5)\n\t"
    "v_mfma_f32_16x16x32_bf16 v[204:207], v[36:39], v[144:147], v[204:207]\n\t"
    "ds_read_b64_tr_b16 v[34:35], v0 offset:29184\n\t"
    "v_rcp_f32 v14, v14\n\t"
    "v_rcp_f32 v15, v15\n\t"
    "v_mfma_f32_16x16x32_bf16 v[212:215], v[36:39], v[148:151], v[212:215]\n\t"
    "ds_read_b64_tr_b16 v[36:37], v1 offset:28672\n\t"
    "v_mul_f32 v14, v254, v14\n\t"
    "v_mul_f32 v15, v255, v15\n\t"
    "v_cvt_pk_bf16_f32 v199, v14, v15\n\t"
    "s_waitcnt vmcnt(13)\n\t"
    "s_barrier\n\t"
    "s_setprio 3\n\t"
    "s_waitcnt lgkmcnt(5)\n\t"
    "v_mfma_f32_16x16x32_bf16 v[200:203], v[24:27], v[152:155], v[200:203]\n\t"
    "ds_read_b64_tr_b16 v[38:39], v1 offset:29184\n\t"
    "s_add_u32 s85, s47, 0x4800\n\t"
    "buffer_store_dwordx4 v[196:199], v6, s[28:31], s85 offen offset:1024 sc1\n\t"
    "v_add3_u32 v20, v20, v196, v197\n\t"
    "v_add3_u32 v20, v20, v198, v199\n\t"
    "v_sub_u32 v20, s55, v20\n\t"
    "buffer_store_dword v20, v7, s[28:31], s85 offen offset:2048 sc1\n\t"
    "v_mfma_f32_16x16x32_bf16 v[208:211], v[24:27], v[156:159], v[208:211]\n\t"
    "ds_read_b64_tr_b16 v[24:25], v0 offset:32768\n\t"
    "s_waitcnt lgkmcnt(5)\n\t"
    "v_mfma_f32_16x16x32_bf16 v[204:207], v[28:31], v[152:155], v[204:207]\n\t"
    "ds_read_b64_tr_b16 v[26:27], v0 offset:33280\n\t"
    "s_add_u32 m0, s36, 98304\n\t"
    "s_add_u32 s85, s39, 0x0\n\t"
    "s_add_u32 s85, s85, s100\n\t"
    "buffer_load_dwordx4 v5, s[24:27], s85 offen lds\n\t"
    "v_mfma_f32_16x16x32_bf16 v[212:215], v[28:31], v[156:159], v[212:215]\n\t"
    "ds_read_b64_tr_b16 v[28:29], v1 offset:32768\n\t"
    "s_waitcnt lgkmcnt(5)\n\t"
    "v_mfma_f32_16x16x32_bf16 v[200:203], v[32:35], v[160:163], v[200:203]\n\t"
    "ds_read_b64_tr_b16 v[30:31], v1 offset:33280\n\t"
    "s_add_u32 m0, s36, 90112\n\t"
    "s_add_u32 s85, s40, 0x0\n\t"
    "s_add_u32 s85, s85, s101\n\t"
    "buffer_load_dwordx4 v5, s[24:27], s85 offen lds\n\t"
    "v_mfma_f32_16x16x32_bf16 v[208:211], v[32:35], v[164:167], v[208:211]\n\t"
    "ds_read_b64_tr_b16 v[32:33], v0 offset:36864\n\t"
    "s_waitcnt lgkmcnt(5)\n\t"
    "v_mfma_f32_16x16x32_bf16 v[204:207], v[36:39], v[160:163], v[204:207]\n\t"
    "ds_read_b64_tr_b16 v[34:35], v0 offset:37376\n\t"
    "v_mfma_f32_16x16x32_bf16 v[212:215], v[36:39], v[164:167], v[212:215]\n\t"
    "ds_read_b64_tr_b16 v[36:37], v1 offset:36864\n\t"
    "s_waitcnt lgkmcnt(5)\n\t"
    "v_mfma_f32_16x16x32_bf16 v[200:203], v[24:27], v[168:171], v[200:203]\n\t"
    "ds_read_b64_tr_b16 v[38:39], v1 offset:37376\n\t"
    "v_mfma_f32_16x16x32_bf16 v[208:211], v[24:27], v[172:175], v[208:211]\n\t"
    "v_add_u32 v0, v0, v3\n\t"
    "v_xor_b32 v1, 32, v0\n\t"
    "v_xor_b32 v2, 64, v0\n\t"
    "v_xor_b32 v3, 96, v0\n\t"
    "ds_read_b64_tr_b16 v[24:25], v1 offset:40960\n\t"
    "s_waitcnt lgkmcnt(5)\n\t"
    "v_mfma_f32_16x16x32_bf16 v[204:207], v[28:31], v[168:171], v[204:207]\n\t"
    "ds_read_b64_tr_b16 v[26:27], v1 offset:43008\n\t"
    "s_add_u32 m0, s36, 81920\n\t"
    "s_add_u32 s85, s40, 0x0\n\t"
    "buffer_load_dwordx4 v5, s[24:27], s85 offen lds\n\t"
    "v_mfma_f32_16x16x32_bf16 v[212:215], v[28:31], v[172:175], v[212:215]\n\t"
    "ds_read_b64_tr_b16 v[28:29], v3 offset:40960\n\t"
    "s_setprio 2\n\t"
    "s_waitcnt lgkmcnt(5)\n\t"
    "v_mfma_f32_16x16x32_bf16 v[200:203], v[32:35], v[176:179], v[200:203]\n\t"
    "ds_read_b64_tr_b16 v[30:31], v3 offset:43008\n\t"
    "s_add_u32 m0, s36, 16384\n\t"
    "s_add_u32 s85, s41, 0x0\n\t"
    "s_add_u32 s85, s85, s100\n\t"
    "buffer_load_dwordx4 v5, s[24:27], s85 offen lds\n\t"
    "v_mfma_f32_16x16x32_bf16 v[208:211], v[32:35], v[180:183], v[208:211]\n\t"
    "ds_read_b64_tr_b16 v[32:33], v0 offset:40960\n\t"
    "s_waitcnt lgkmcnt(5)\n\t"
    "LIPB1x0_%=:\n\t"
    "s_cbranch_execz LIPRB_%=\n\t"
    "v_mfma_f32_16x16x32_bf16 v[204:207], v[36:39], v[176:179], v[204:207]\n\t"
    "ds_read_b64_tr_b16 v[34:35], v0 offset:43008\n\t"
    "v_mfma_f32_16x16x32_bf16 v[212:215], v[36:39], v[180:183], v[212:215]\n\t"
    "ds_read_b64_tr_b16 v[36:37], v2 offset:40960\n\t"
    "s_setprio 1\n\t"
    "s_waitcnt lgkmcnt(5)\n\t"
    "v_mfma_f32_16x16x32_bf16 v[40:43], v[24:27], v[184:187], 0\n\t"
    "ds_read_b64_tr_b16 v[38:39], v2 offset:43008\n\t"
    "v_mul_f32 v12, 0xbfb8aa3b, v200\n\t"
    "v_mul_f32 v13, 0xbfb8aa3b, v201\n\t"
    "v_exp_f32 v12, v12\n\t"
    "v_mfma_f32_16x16x32_bf16 v[44:47], v[24:27], v[188:191], 0\n\t"
    "v_add_u32 v1, 0x4000, v1\n\t"
    "ds_read_b64_tr_b16 v[24:25], v1 offset:57344\n\t"
    "v_exp_f32 v13, v13\n\t"
    "v_add_f32 v12, 1.0, v12\n\t"
    "v_add_f32 v13, 1.0, v13\n\t"
    "s_waitcnt lgkmcnt(5)\n\t"
    "v_mfma_f32_16x16x32_bf16 v[48:51], v[28:31], v[184:187], 0\n\t"
    "ds_read_b64_tr_b16 v[26:27], v1 offset:59392\n\t"
    "s_add_u32 m0, s36, 8192\n\t"
    "s_add_u32 s85, s39, 0x12000\n\t"
    "s_add_u32 s85, s85, s100\n\t"
    "buffer_load_dwordx4 v5, s[24:27], s85 offen lds\n\t"
    "v_rcp_f32 v12, v12\n\t"
    "v_rcp_f32 v13, v13\n\t"
    "v_mfma_f32_16x16x32_bf16 v[52:55], v[28:31], v[188:191], 0\n\t"
    "v_add_u32 v3, 0x4000, v3\n\t"
    "ds_read_b64_tr_b16 v[28:29], v3 offset:57344\n\t"
    "v_mul_f32 v12, v200, v12\n\t"
    "v_mul_f32 v13, v201, v13\n\t"
    "v_cvt_pk_bf16_f32 v200, v12, v13\n\t"
    "s_setprio 0\n\t"
    "s_waitcnt lgkmcnt(5)\n\t"
    "v_mfma_f32_16x16x32_bf16 v[56:59], v[32:35], v[184:187], 0\n\t"
    "ds_read_b64_tr_b16 v[30:31], v3 offset:59392\n\t"
    "s_add_u32 m0, s36, 0\n\t"
    "s_add_u32 s85, s40, 0x12000\n\t"
    "s_add_u32 s85, s85, s100\n\t"
    "buffer_load_dwordx4 v5, s[24:27], s85 offen lds\n\t"
    "v_mul_f32 v14, 0xbfb8aa3b, v202\n\t"
    "v_mul_f32 v15, 0xbfb8aa3b, v203\n\t"
    "v_exp_f32 v14, v14\n\t"
    "v_mfma_f32_16x16x32_bf16 v[60:63], v[32:35], v[188:191], 0\n\t"
    "v_add_u32 v0, 0x4000, v0\n\t"
    "ds_read_b64_tr_b16 v[32:33], v0 offset:57344\n\t"
    "v_exp_f32 v15, v15\n\t"
    "v_add_f32 v14, 1.0, v14\n\t"
    "v_add_f32 v15, 1.0, v15\n\t"
    "s_waitcnt lgkmcnt(5)\n\t"
    "v_mfma_f32_16x16x32_bf16 v[64:67], v[36:39], v[184:187], 0\n\t"
    "ds_read_b64_tr_b16 v[34:35], v0 offset:59392\n\t"
    "v_rcp_f32 v14, v14\n\t"
    "v_rcp_f32 v15, v15\n\t"
    "v_mfma_f32_16x16x32_bf16 v[68:71], v[36:39], v[188:191], 0\n\t"
    "v_add_u32 v2, 0x4000, v2\n\t"
    "ds_read_b64_tr_b16 v[36:37], v2 offset:57344\n\t"
    "v_mul_f32 v14, v202, v14\n\t"
    "v_mul_f32 v15, v203, v15\n\t"
    "v_cvt_pk_bf16_f32 v201, v14, v15\n\t"
    "s_waitcnt vmcnt(16)\n\t"
    "s_barrier\n\t"
    "s_setprio 3\n\t"
    "s_waitcnt lgkmcnt(5)\n\t"
    "v_mfma_f32_16x16x32_bf16 v[72:75], v[24:27], v[184:187], 0\n\t"
    "ds_read_b64_tr_b16 v[38:39], v2 offset:59392\n\t"
    "v_mul_f32 v12, 0xbfb8aa3b, v204\n\t"
    "v_mul_f32 v13, 0xbfb8aa3b, v205\n\t"
    "v_exp_f32 v12, v12\n\t"
    "v_mfma_f32_16x16x32_bf16 v[76:79], v[24:27], v[188:191], 0\n\t"
    "ds_read_b64_tr_b16 v[24:25], v1 offset:49152\n\t"
    "ds_read_b64_tr_b16 v[26:27], v1 offset:51200\n\t"
    "v_exp_f32 v13, v13\n\t"
    "v_add_f32 v12, 1.0, v12\n\t"
    "v_add_f32 v13, 1.0, v13\n\t"
    "s_waitcnt lgkmcnt(6)\n\t"
    "v_mfma_f32_16x16x32_bf16 v[80:83], v[28:31], v[184:187], 0\n\t"
    "s_add_u32 m0, s36, 122880\n\t"
    "s_add_u32 s85, s41, 0x0\n\t"
    "s_add_u32 s85, s85, s101\n\t"
    "buffer_load_dwordx4 v5, s[24:27], s85 offen lds\n\t"
    "v_rcp_f32 v12, v12\n\t"
    "v_rcp_f32 v13, v13\n\t"
    "v_mfma_f32_16x16x32_bf16 v[84:87], v[28:31], v[188:191], 0\n\t"
    "ds_read_b64_tr_b16 v[28:29], v3 offset:49152\n\t"
    "v_mul_f32 v12, v204, v12\n\t"
    "v_mul_f32 v13, v205, v13\n\t"
    "v_cvt_pk_bf16_f32 v202, v12, v13\n\t"
    "s_waitcnt lgkmcnt(5)\n\t"
    "v_mfma_f32_16x16x32_bf16 v[88:91], v[32:35], v[184:187], 0\n\t"
    "ds_read_b64_tr_b16 v[30:31], v3 offset:51200\n\t"
    "v_mul_f32 v14, 0xbfb8aa3b, v206\n\t"
    "v_mul_f32 v15, 0xbfb8aa3b, v207\n\t"
    "v_exp_f32 v14, v14\n\t"
    "v_mfma_f32_16x16x32_bf16 v[92:95], v[32:35], v[188:191], 0\n\t"
    "ds_read_b64_tr_b16 v[32:33], v0 offset:49152\n\t"
    "v_exp_f32 v15, v15\n\t"
    "v_add_f32 v14, 1.0, v14\n\t"
    "v_add_f32 v15, 1.0, v15\n\t"
    "s_waitcnt lgkmcnt(5)\n\t"
    "v_mfma_f32_16x16x32_bf16 v[96:99], v[36:39], v[184:187], 0\n\t"
    "ds_read_b64_tr_b16 v[34:35], v0 offset:51200\n\t"
    "v_rcp_f32 v14, v14\n\t"
    "v_rcp_f32 v15, v15\n\t"
    "s_add_u32 s85, s43, 0x0\n\t"
    "buffer_load_dwordx4 v[104:107], v6, s[28:31], s85 offen sc1\n\t"
    "v_mfma_f32_16x16x32_bf16 v[100:103], v[36:39], v[188:191], 0\n\t"
    "ds_read_b64_tr_b16 v[36:37], v2 offset:49152\n\t"
    "v_mul_f32 v14, v206, v14\n\t"
    "v_mul_f32 v15, v207, v15\n\t"
    "v_cvt_pk_bf16_f32 v203, v14, v15\n\t"
    "s_waitcnt lgkmcnt(5)\n\t"
    "v_mfma_f32_16x16x32_bf16 v[112:115], v[24:27], v[184:187], 0\n\t"
    "ds_read_b64_tr_b16 v[38:39], v2 offset:51200\n\t"
    "s_add_u32 s85, s47, 0x9000\n\t"
    "buffer_store_dwordx4 v[200:203], v6, s[28:31], s85 offen sc1\n\t"
    "v_add3_u32 v20, v200, v201, v202\n\t"
    "v_add_u32 v20, v20, v203\n\t"
    "v_mul_f32 v12, 0xbfb8aa3b, v208\n\t"
    "v_mul_f32 v13, 0xbfb8aa3b, v209\n\t"
    "v_exp_f32 v12, v12\n\t"
    "s_add_u32 s85, s43, 0x0\n\t"
    "buffer_load_dword v16, v7, s[28:31], s85 offen offset:2048 sc1\n\t"
    "v_mfma_f32_16x16x32_bf16 v[116:119], v[24:27], v[188:191], 0\n\t"
    "ds_read_b64_tr_b16 v[24:25], v1 offset:40960\n\t"
    "v_exp_f32 v13, v13\n\t"
    "v_add_f32 v12, 1.0, v12\n\t"
    "v_add_f32 v13, 1.0, v13\n\t"
    "s_waitcnt lgkmcnt(5)\n\t"
    "v_mfma_f32_16x16x32_bf16 v[120:123], v[28:31], v[184:187], 0\n\t"
    "ds_read_b64_tr_b16 v[26:27], v1 offset:43008\n\t"
    "s_add_u32 m0, s36, 114688\n\t"
    "s_add_u32 s85, s41, 0x0\n\t"
    "buffer_load_dwordx4 v5, s[24:27], s85 offen lds\n\t"
    "v_rcp_f32 v12, v12\n\t"
    "v_rcp_f32 v13, v13\n\t"
    "v_mfma_f32_16x16x32_bf16 v[124:127], v[28:31], v[188:191], 0\n\t"
    "ds_read_b64_tr_b16 v[28:29], v3 offset:40960\n\t"
    "v_mul_f32 v12, v208, v12\n\t"
    "v_mul_f32 v13, v209, v13\n\t"
    "v_cvt_pk_bf16_f32 v204, v12, v13\n\t"
    "s_setprio 2\n\t"
    "s_waitcnt lgkmcnt(5)\n\t"
    "v_mfma_f32_16x16x32_bf16 v[128:131], v[32:35], v[184:187], 0\n\t"
    "ds_read_b64_tr_b16 v[30:31], v3 offset:43008\n\t"
    "v_mul_f32 v14, 0xbfb8aa3b, v210\n\t"
    "v_mul_f32 v15, 0xbfb8aa3b, v211\n\t"
    "v_exp_f32 v14, v14\n\t"
    "v_mfma_f32_16x16x32_bf16 v[132:135], v[32:35], v[188:191], 0\n\t"
    "ds_read_b64_tr_b16 v[32:33], v0 offset:40960\n\t"
    "v_exp_f32 v15, v15\n\t"
    "v_add_f32 v14, 1.0, v14\n\t"
    "v_add_f32 v15, 1.0, v15\n\t"
    "s_waitcnt lgkmcnt(5)\n\t"
    "v_mfma_f32_16x16x32_bf16 v[136:139], v[36:39], v[184:187], 0\n\t"
    "ds_read_b64_tr_b16 v[34:35], v0 offset:43008\n\t"
    "v_rcp_f32 v14, v14\n\t"
    "v_rcp_f32 v15, v15\n\t"
    "s_add_u32 s85, s43, 0x0\n\t"
    "buffer_load_dwordx4 v[108:111], v6, s[28:31], s85 offen offset:1024 sc1\n\t"
    "v_mfma_f32_16x16x32_bf16 v[140:143], v[36:39], v[188:191], 0\n\t"
    "ds_read_b64_tr_b16 v[36:37], v2 offset:40960\n\t"
    "v_mul_f32 v14, v210, v14\n\t"
    "v_mul_f32 v15, v211, v15\n\t"
    "v_cvt_pk_bf16_f32 v205, v14, v15\n\t"
    "s_setprio 1\n\t"
    "s_waitcnt lgkmcnt(5)\n\t"
    "v_mfma_f32_16x16x32_bf16 v[40:43], v[24:27], v[192:195], v[40:43]\n\t"
    "ds_read_b64_tr_b16 v[38:39], v2 offset:43008\n\t"
    "v_mul_f32 v12, 0xbfb8aa3b, v212\n\t"
    "v_mul_f32 v13, 0xbfb8aa3b, v213\n\t"
    "v_exp_f32 v12, v12\n\t"
    "v_mfma_f32_16x16x32_bf16 v[44:47], v[24:27], v[196:199], v[44:47]\n\t"
    "ds_read_b64_tr_b16 v[24:25], v1 offset:32768\n\t"
    "v_exp_f32 v13, v13\n\t"
    "v_add_f32 v12, 1.0, v12\n\t"
    "v_add_f32 v13, 1.0, v13\n\t"
    "s_waitcnt lgkmcnt(5)\n\t"
    "v_mfma_f32_16x16x32_bf16 v[48:51], v[28:31], v[192:195], v[48:51]\n\t"
    "ds_read_b64_tr_b16 v[26:27], v1 offset:34816\n\t"
    "s_add_u32 m0, s36, 106496\n\t"
    "s_add_u32 s85, s39, 0x12000\n\t"
    "s_add_u32 s85, s85, s101\n\t"
    "buffer_load_dwordx4 v5, s[24:27], s85 offen lds\n\t"
    "v_rcp_f32 v12, v12\n\t"
    "v_rcp_f32 v13, v13\n\t"
    "v_mfma_f32_16x16x32_bf16 v[52:55], v[28:31], v[196:199], v[52:55]\n\t"
    "ds_read_b64_tr_b16 v[28:29], v3 offset:32768\n\t"
    "v_mul_f32 v12, v212, v12\n\t"
    "v_mul_f32 v13, v213, v13\n\t"
    "v_cvt_pk_bf16_f32 v206, v12, v13\n\t"
    "s_setprio 0\n\t"
    "s_waitcnt lgkmcnt(5)\n\t"
    "v_mfma_f32_16x16x32_bf16 v[56:59], v[32:35], v[192:195], v[56:59]\n\t"
    "ds_read_b64_tr_b16 v[30:31], v3 offset:34816\n\t"
    "v_mul_f32 v14, 0xbfb8aa3b, v214\n\t"
    "v_mul_f32 v15, 0xbfb8aa3b, v215\n\t"
    "v_exp_f32 v14, v14\n\t"
    "v_mfma_f32_16x16x32_bf16 v[60:63], v[32:35], v[196:199], v[60:63]\n\t"
    "ds_read_b64_tr_b16 v[32:33], v0 offset:32768\n\t"
    "v_exp_f32 v15, v15\n\t"
    "v_add_f32 v14, 1.0, v14\n\t"
    "v_add_f32 v15, 1.0, v15\n\t"
    "s_waitcnt lgkmcnt(5)\n\t"
    "v_mfma_f32_16x16x32_bf16 v[64:67], v[36:39], v[192:195], v[64:67]\n\t"
    "ds_read_b64_tr_b16 v[34:35], v0 offset:34816\n\t"
    "v_rcp_f32 v14, v14\n\t"
    "v_rcp_f32 v15, v15\n\t"
    "s_add_u32 s85, s44, 0x0\n\t"
    "buffer_load_dwordx4 v[144:147], v6, s[28:31], s85 offen sc1\n\t"
    "v_mfma_f32_16x16x32_bf16 v[68:71], v[36:39], v[196:199], v[68:71]\n\t"
    "ds_read_b64_tr_b16 v[36:37], v2 offset:32768\n\t"
    "v_mul_f32 v14, v214, v14\n\t"
    "v_mul_f32 v15, v215, v15\n\t"
    "v_cvt_pk_bf16_f32 v207, v14, v15\n\t"
    "s_waitcnt vmcnt(21)\n\t"
    "s_barrier\n\t"
    "s_setprio 3\n\t"
    "s_waitcnt lgkmcnt(5)\n\t"
    "v_mfma_f32_16x16x32_bf16 v[72:75], v[24:27], v[192:195], v[72:75]\n\t"
    "ds_read_b64_tr_b16 v[38:39], v2 offset:34816\n\t"
    "s_add_u32 s85, s47, 0x9000\n\t"
    "buffer_store_dwordx4 v[204:207], v6, s[28:31], s85 offen offset:1024 sc1\n\t"
    "v_add3_u32 v20, v20, v204, v205\n\t"
    "v_add3_u32 v20, v20, v206, v207\n\t"
    "v_sub_u32 v20, s55, v20\n\t"
    "buffer_store_dword v20, v7, s[28:31], s85 offen offset:2048 sc1\n\t"
    "s_add_u32 s85, s44, 0x0\n\t"
    "buffer_load_dword v17, v7, s[28:31], s85 offen offset:2048 sc1\n\t"
    "v_mfma_f32_16x16x32_bf16 v[76:79], v[24:27], v[196:199], v[76:79]\n\t"
    "v_add_u32 v1, 0xffffa000, v1\n\t"
    "ds_read_b64_tr_b16 v[24:25], v1 offset:0\n\t"
    "s_waitcnt lgkmcnt(5)\n\t"
    "v_mfma_f32_16x16x32_bf16 v[80:83], v[28:31], v[192:195], v[80:83]\n\t"
    "ds_read_b64_tr_b16 v[26:27], v1 offset:2048\n\t"
    "s_add_u32 m0, s36, 155648\n\t"
    "s_add_u32 s85, s39, 0x12000\n\t"
    "buffer_load_dwordx4 v5, s[24:27], s85 offen lds\n\t"
    "v_mfma_f32_16x16x32_bf16 v[84:87], v[28:31], v[196:199], v[84:87]\n\t"
    "v_add_u32 v3, 0xffffa000, v3\n\t"
    "ds_read_b64_tr_b16 v[28:29], v3 offset:0\n\t"
    "s_waitcnt lgkmcnt(5)\n\t"
    "v_mfma_f32_16x16x32_bf16 v[88:91], v[32:35], v[192:195], v[88:91]\n\t"
    "ds_read_b64_tr_b16 v[30:31], v3 offset:2048\n\t"
    "s_add_u32 m0, s36, 147456\n\t"
    "s_add_u32 s85, s40, 0x12000\n\t"
    "s_add_u32 s85, s85, s101\n\t"
    "buffer_load_dwordx4 v5, s[24:27], s85 offen lds\n\t"
    "v_mfma_f32_16x16x32_bf16 v[92:95], v[32:35], v[196:199], v[92:95]\n\t"
    "v_add_u32 v0, 0xffffa000, v0\n\t"
    "ds_read_b64_tr_b16 v[32:33], v0 offset:0\n\t"
    "s_waitcnt lgkmcnt(5)\n\t"
    "v_mfma_f32_16x16x32_bf16 v[96:99], v[36:39], v[192:195], v[96:99]\n\t"
    "ds_read_b64_tr_b16 v[34:35], v0 offset:2048\n\t"
    "s_add_u32 s85, s44, 0x0\n\t"
    "buffer_load_dwordx4 v[148:151], v6, s[28:31], s85 offen offset:1024 sc1\n\t"
    "v_mfma_f32_16x16x32_bf16 v[100:103], v[36:39], v[196:199], v[100:103]\n\t"
    "v_add_u32 v2, 0xffffa000, v2\n\t"
    "ds_read_b64_tr_b16 v[36:37], v2 offset:0\n\t"
    "s_waitcnt lgkmcnt(5)\n\t"
    "v_mfma_f32_16x16x32_bf16 v[112:115], v[24:27], v[192:195], v[112:115]\n\t"
    "ds_read_b64_tr_b16 v[38:39], v2 offset:2048\n\t"
    "v_mfma_f32_16x16x32_bf16 v[116:119], v[24:27], v[196:199], v[116:119]\n\t"
    "v_add_u32 v1, 0xffffe000, v1\n\t"
    "ds_read_b64_tr_b16 v[24:25], v1 offset:0\n\t"
    "s_waitcnt lgkmcnt(5)\n\t"
    "v_mfma_f32_16x16x32_bf16 v[120:123], v[28:31], v[192:195], v[120:123]\n\t"
    "ds_read_b64_tr_b16 v[26:27], v1 offset:2048\n\t"
    "s_add_u32 m0, s36, 139264\n\t"
    "s_add_u32 s85, s40, 0x12000\n\t"
    "buffer_load_dwordx4 v5, s[24:27], s85 offen lds\n\t"
    "v_mfma_f32_16x16x32_bf16 v[124:127], v[28:31], v[196:199], v[124:127]\n\t"
    "v_add_u32 v3, 0xffffe000, v3\n\t"
    "ds_read_b64_tr_b16 v[28:29], v3 offset:0\n\t"
    "s_setprio 2\n\t"
    "s_waitcnt lgkmcnt(5)\n\t"
    "v_mfma_f32_16x16x32_bf16 v[128:131], v[32:35], v[192:195], v[128:131]\n\t"
    "ds_read_b64_tr_b16 v[30:31], v3 offset:2048\n\t"
    "v_mfma_f32_16x16x32_bf16 v[132:135], v[32:35], v[196:199], v[132:135]\n\t"
    "v_add_u32 v0, 0xffffe000, v0\n\t"
    "ds_read_b64_tr_b16 v[32:33], v0 offset:0\n\t"
    "s_waitcnt lgkmcnt(5)\n\t"
    "v_mfma_f32_16x16x32_bf16 v[136:139], v[36:39], v[192:195], v[136:139]\n\t"
    "ds_read_b64_tr_b16 v[34:35], v0 offset:2048\n\t"
    "s_add_u32 s85, s45, 0x0\n\t"
    "buffer_load_dwordx4 v[152:155], v6, s[28:31], s85 offen sc1\n\t"
    "v_mfma_f32_16x16x32_bf16 v[140:143], v[36:39], v[196:199], v[140:143]\n\t"
    "v_add_u32 v2, 0xffffe000, v2\n\t"
    "ds_read_b64_tr_b16 v[36:37], v2 offset:0\n\t"
    "s_setprio 1\n\t"
    "s_waitcnt lgkmcnt(5)\n\t"
    "v_mfma_f32_16x16x32_bf16 v[40:43], v[24:27], v[200:203], v[40:43]\n\t"
    "ds_read_b64_tr_b16 v[38:39], v2 offset:2048\n\t"
    "s_add_u32 s85, s45, 0x0\n\t"
    "buffer_load_dword v18, v7, s[28:31], s85 offen offset:2048 sc1\n\t"
    "v_mfma_f32_16x16x32_bf16 v[44:47], v[24:27], v[204:207], v[44:47]\n\t"
    "v_add_u32 v1, 0xffffe000, v1\n\t"
    "ds_read_b64_tr_b16 v[24:25], v1 offset:0\n\t"
    "s_waitcnt lgkmcnt(5)\n\t"
    "v_mfma_f32_16x16x32_bf16 v[48:51], v[28:31], v[200:203], v[48:51]\n\t"
    "ds_read_b64_tr_b16 v[26:27], v1 offset:2048\n\t"
    "v_mfma_f32_16x16x32_bf16 v[52:55], v[28:31], v[204:207], v[52:55]\n\t"
    "v_add_u32 v3, 0xffffe000, v3\n\t"
    "ds_read_b64_tr_b16 v[28:29], v3 offset:0\n\t"
    "s_setprio 0\n\t"
    "s_waitcnt lgkmcnt(5)\n\t"
    "v_mfma_f32_16x16x32_bf16 v[56:59], v[32:35], v[200:203], v[56:59]\n\t"
    "ds_read_b64_tr_b16 v[30:31], v3 offset:2048\n\t"
    "v_mfma_f32_16x16x32_bf16 v[60:63], v[32:35], v[204:207], v[60:63]\n\t"
    "v_add_u32 v0, 0xffffe000, v0\n\t"
    "ds_read_b64_tr_b16 v[32:33], v0 offset:0\n\t"
    "s_waitcnt vmcnt(13)\n\t"
    "v_add3_u32 v16, v104, v105, v16\n\t"
    "v_add3_u32 v16, v106, v107, v16\n\t"
    "s_waitcnt vmcnt(11)\n\t"
    "v_add3_u32 v16, v108, v109, v16\n\t"
    "v_add3_u32 v16, v110, v111, v16\n\t"
    "v_cmp_ne_u32 vcc, s48, v16\n\t"
    "s_cbranch_vccnz LB1_%=\n\t"
    "LB2_%=:\n\t"
    "s_waitcnt lgkmcnt(5)\n\t"
    "v_mfma_f32_16x16x32_bf16 v[64:67], v[36:39], v[200:203], v[64:67]\n\t"
    "ds_read_b64_tr_b16 v[34:35], v0 offset:2048\n\t"
    "s_add_u32 s85, s45, 0x0\n\t"
    "buffer_load_dwordx4 v[156:159], v6, s[28:31], s85 offen offset:1024 sc1\n\t"
    "v_mfma_f32_16x16x32_bf16 v[68:71], v[36:39], v[204:207], v[68:71]\n\t"
    "v_add_u32 v2, 0xffffe000, v2\n\t"
    "ds_read_b64_tr_b16 v[36:37], v2 offset:0\n\t"
    "s_barrier\n\t"
    "s_setprio 3\n\t"
    "s_waitcnt lgkmcnt(5)\n\t"
    "v_mfma_f32_16x16x32_bf16 v[72:75], v[24:27], v[200:203], v[72:75]\n\t"
    "ds_read_b64_tr_b16 v[38:39], v2 offset:2048\n\t"
    "v_mfma_f32_16x16x32_bf16 v[76:79], v[24:27], v[204:207], v[76:79]\n\t"
    "v_add_u32 v1, 0xffffe000, v1\n\t"
    "ds_read_b64_tr_b16 v[24:25], v1 offset:0\n\t"
    "s_waitcnt lgkmcnt(5)\n\t"
    "v_mfma_f32_16x16x32_bf16 v[80:83], v[28:31], v[200:203], v[80:83]\n\t"
    "ds_read_b64_tr_b16 v[26:27], v1 offset:2048\n\t"
    "s_add_u32 m0, s36, 131072\n\t"
    "s_add_u32 s85, s41, 0x12000\n\t"
    "s_add_u32 s85, s85, s101\n\t"
    "buffer_load_dwordx4 v5, s[24:27], s85 offen lds\n\t"
    "v_mfma_f32_16x16x32_bf16 v[84:87], v[28:31], v[204:207], v[84:87]\n\t"
    "v_add_u32 v3, 0xffffe000, v3\n\t"
    "ds_read_b64_tr_b16 v[28:29], v3 offset:0\n\t"
    "s_waitcnt lgkmcnt(5)\n\t"
    "v_mfma_f32_16x16x32_bf16 v[88:91], v[32:35], v[200:203], v[88:91]\n\t"
    "ds_read_b64_tr_b16 v[30:31], v3 offset:2048\n\t"
    "v_mfma_f32_16x16x32_bf16 v[92:95], v[32:35], v[204:207], v[92:95]\n\t"
    "v_add_u32 v0, 0xffffe000, v0\n\t"
    "ds_read_b64_tr_b16 v[32:33], v0 offset:0\n\t"
    "s_waitcnt lgkmcnt(5)\n\t"
    "v_mfma_f32_16x16x32_bf16 v[96:99], v[36:39], v[200:203], v[96:99]\n\t"
    "ds_read_b64_tr_b16 v[34:35], v0 offset:2048\n\t"
    "v_mfma_f32_16x16x32_bf16 v[100:103], v[36:39], v[204:207], v[100:103]\n\t"
    "v_add_u32 v2, 0xffffe000, v2\n\t"
    "ds_read_b64_tr_b16 v[36:37], v2 offset:0\n\t"
    "s_waitcnt lgkmcnt(5)\n\t"
    "v_mfma_f32_16x16x32_bf16 v[112:115], v[24:27], v[200:203], v[112:115]\n\t"
    "ds_read_b64_tr_b16 v[38:39], v2 offset:2048\n\t"
    "v_mfma_f32_16x16x32_bf16 v[116:119], v[24:27], v[204:207], v[116:119]\n\t"
    "v_add_u32 v1, 0xffffe000, v1\n\t"
    "ds_read_b64_tr_b16 v[24:25], v1 offset:0\n\t"
    "s_waitcnt lgkmcnt(5)\n\t"
    "v_mfma_f32_16x16x32_bf16 v[120:123], v[28:31], v[200:203], v[120:123]\n\t"
    "ds_read_b64_tr_b16 v[26:27], v1 offset:2048\n\t"
    "s_add_u32 m0, s36, 73728\n\t"
    "s_add_u32 s85, s41, 0x12000\n\t"
    "buffer_load_dwordx4 v5, s[24:27], s85 offen lds\n\t"
    "v_mfma_f32_16x16x32_bf16 v[124:127], v[28:31], v[204:207], v[124:127]\n\t"
    "v_add_u32 v3, 0xffffe000, v3\n\t"
    "ds_read_b64_tr_b16 v[28:29], v3 offset:0\n\t"
    "s_setprio 2\n\t"
    "s_waitcnt lgkmcnt(5)\n\t"
    "v_mfma_f32_16x16x32_bf16 v[128:131], v[32:35], v[200:203], v[128:131]\n\t"
    "ds_read_b64_tr_b16 v[30:31], v3 offset:2048\n\t"
    "v_mfma_f32_16x16x32_bf16 v[132:135], v[32:35], v[204:207], v[132:135]\n\t"
    "s_waitcnt vmcnt(9)\n\t"
    "v_add3_u32 v17, v144, v145, v17\n\t"
    "v_add3_u32 v17, v146, v147, v17\n\t"
    "s_waitcnt vmcnt(6)\n\t"
    "v_add3_u32 v17, v148, v149, v17\n\t"
    "v_add3_u32 v17, v150, v151, v17\n\t"
    "v_cmp_ne_u32 vcc, s49, v17\n\t"
    "s_cbranch_vccnz LB3_%=\n\t"
    "LB4_%=:\n\t"
    "v_add_u32 v0, 0xffffe000, v0\n\t"
    "ds_read_b64_tr_b16 v[32:33], v0 offset:0\n\t"
    "ds_read_b64_tr_b16 v[34:35], v0 offset:2048\n\t"
    "s_waitcnt lgkmcnt(6)\n\t"
    "v_mfma_f32_16x16x32_bf16 v[136:139], v[36:39], v[200:203], v[136:139]\n\t"
    "v_mfma_f32_16x16x32_bf16 v[140:143], v[36:39], v[204:207], v[140:143]\n\t"
    "v_add_u32 v2, 0xffffe000, v2\n\t"
    "ds_read_b64_tr_b16 v[36:37], v2 offset:0\n\t"
    "s_setprio 1\n\t"
    "s_waitcnt lgkmcnt(5)\n\t"
    "v_mfma_f32_16x16x32_bf16 v[40:43], v[24:27], v[104:107], v[40:43]\n\t"
    "ds_read_b64_tr_b16 v[38:39], v2 offset:2048\n\t"
    "v_mfma_f32_16x16x32_bf16 v[44:47], v[24:27], v[108:111], v[44:47]\n\t"
    "v_add_u32 v1, 0xffffe000, v1\n\t"
    "ds_read_b64_tr_b16 v[24:25], v1 offset:0\n\t"
    "s_waitcnt lgkmcnt(5)\n\t"
    "v_mfma_f32_16x16x32_bf16 v[48:51], v[28:31], v[104:107], v[48:51]\n\t"
    "ds_read_b64_tr_b16 v[26:27], v1 offset:2048\n\t"
    "s_add_u32 m0, s36, 65536\n\t"
    "s_add_u32 s85, s39, 0x24000\n\t"
    "s_add_u32 s85, s85, s101\n\t"
    "buffer_load_dwordx4 v5, s[24:27], s85 offen lds\n\t"
    "v_mfma_f32_16x16x32_bf16 v[52:55], v[28:31], v[108:111], v[52:55]\n\t"
    "v_add_u32 v3, 0xffffe000, v3\n\t"
    "ds_read_b64_tr_b16 v[28:29], v3 offset:0\n\t"
    "s_setprio 0\n\t"
    "s_waitcnt lgkmcnt(5)\n\t"
    "v_mfma_f32_16x16x32_bf16 v[56:59], v[32:35], v[104:107], v[56:59]\n\t"
    "ds_read_b64_tr_b16 v[30:31], v3 offset:2048\n\t"
    "v_mfma_f32_16x16x32_bf16 v[60:63], v[32:35], v[108:111], v[60:63]\n\t"
    "v_add_u32 v0, 0xffffe000, v0\n\t"
    "ds_read_b64_tr_b16 v[32:33], v0 offset:0\n\t"
    "ds_read_b64_tr_b16 v[34:35], v0 offset:2048\n\t"
    "s_waitcnt lgkmcnt(6)\n\t"
    "v_mfma_f32_16x16x32_bf16 v[64:67], v[36:39], v[104:107], v[64:67]\n\t"
    "s_add_u32 s85, s43, 0x4800\n\t"
    "buffer_load_dwordx4 v[160:163], v6, s[28:31], s85 offen sc1\n\t"
    "v_mfma_f32_16x16x32_bf16 v[68:71], v[36:39], v[108:111], v[68:71]\n\t"
    "v_add_u32 v2, 0xffffe000, v2\n\t"
    "ds_read_b64_tr_b16 v[36:37], v2 offset:0\n\t"
    "s_barrier\n\t"
    "s_setprio 3\n\t"
    "s_waitcnt lgkmcnt(5)\n\t"
    "v_mfma_f32_16x16x32_bf16 v[72:75], v[24:27], v[104:107], v[72:75]\n\t"
    "ds_read_b64_tr_b16 v[38:39], v2 offset:2048\n\t"
    "s_add_u32 s85, s43, 0x4800\n\t"
    "buffer_load_dword v19, v7, s[28:31], s85 offen offset:2048 sc1\n\t"
    "v_mfma_f32_16x16x32_bf16 v[76:79], v[24:27], v[108:111], v[76:79]\n\t"
    "v_add_u32 v1, 0x10000, v1\n\t"
    "ds_read_b64_tr_b16 v[24:25], v1 offset:0\n\t"
    "s_waitcnt lgkmcnt(5)\n\t"
    "v_mfma_f32_16x16x32_bf16 v[80:83], v[28:31], v[104:107], v[80:83]\n\t"
    "ds_read_b64_tr_b16 v[26:27], v1 offset:2048\n\t"
    "s_add_u32 m0, s36, 57344\n\t"
    "s_add_u32 s85, s39, 0x24000\n\t"
    "buffer_load_dwordx4 v5, s[24:27], s85 offen lds\n\t"
    "v_mfma_f32_16x16x32_bf16 v[84:87], v[28:31], v[108:111], v[84:87]\n\t"
    "v_add_u32 v3, 0x10000, v3\n\t"
    "ds_read_b64_tr_b16 v[28:29], v3 offset:0\n\t"
    "s_waitcnt lgkmcnt(5)\n\t"
    "v_mfma_f32_16x16x32_bf16 v[88:91], v[32:35], v[104:107], v[88:91]\n\t"
    "ds_read_b64_tr_b16 v[30:31], v3 offset:2048\n\t"
    "v_mfma_f32_16x16x32_bf16 v[92:95], v[32:35], v[108:111], v[92:95]\n\t"
    "s_waitcnt vmcnt(7)\n\t"
    "v_add3_u32 v18, v152, v153, v18\n\t"
    "v_add3_u32 v18, v154, v155, v18\n\t"
    "s_waitcnt vmcnt(6)\n\t"
    "v_add3_u32 v18, v156, v157, v18\n\t"
    "v_add3_u32 v18, v158, v159, v18\n\t"
    "v_cmp_ne_u32 vcc, s50, v18\n\t"
    "s_cbranch_vccnz LB5_%=\n\t"
    "LB6_%=:\n\t"
    "v_add_u32 v0, 0x10000, v0\n\t"
    "ds_read_b64_tr_b16 v[32:33], v0 offset:0\n\t"
    "s_waitcnt lgkmcnt(5)\n\t"
    "v_mfma_f32_16x16x32_bf16 v[96:99], v[36:39], v[104:107], v[96:99]\n\t"
    "ds_read_b64_tr_b16 v[34:35], v0 offset:2048\n\t"
    "s_add_u32 s85, s43, 0x4800\n\t"
    "buffer_load_dwordx4 v[164:167], v6, s[28:31], s85 offen offset:1024 sc1\n\t"
    "v_mfma_f32_16x16x32_bf16 v[100:103], v[36:39], v[108:111], v[100:103]\n\t"
    "v_add_u32 v2, 0x10000, v2\n\t"
    "ds_read_b64_tr_b16 v[36:37], v2 offset:0\n\t"
    "s_waitcnt lgkmcnt(5)\n\t"
    "v_mfma_f32_16x16x32_bf16 v[112:115], v[24:27], v[104:107], v[112:115]\n\t"
    "ds_read_b64_tr_b16 v[38:39], v2 offset:2048\n\t"
    "v_mfma_f32_16x16x32_bf16 v[116:119], v[24:27], v[108:111], v[116:119]\n\t"
    "v_add_u32 v1, 0xffffe000, v1\n\t"
    "ds_read_b64_tr_b16 v[24:25], v1 offset:0\n\t"
    "s_waitcnt lgkmcnt(5)\n\t"
    "v_mfma_f32_16x16x32_bf16 v[120:123], v[28:31], v[104:107], v[120:123]\n\t"
    "ds_read_b64_tr_b16 v[26:27], v1 offset:2048\n\t"
    "s_add_u32 m0, s36, 49152\n\t"
    "s_add_u32 s85, s40, 0x24000\n\t"
    "s_add_u32 s85, s85, s101\n\t"
    "buffer_load_dwordx4 v5, s[24:27], s85 offen lds\n\t"
    "v_mfma_f32_16x16x32_bf16 v[124:127], v[28:31], v[108:111], v[124:127]\n\t"
    "v_add_u32 v3, 0xffffe000, v3\n\t"
    "ds_read_b64_tr_b16 v[28:29], v3 offset:0\n\t"
    "s_setprio 2\n\t"
    "s_waitcnt lgkmcnt(5)\n\t"
    "v_mfma_f32_16x16x32_bf16 v[128:131], v[32:35], v[104:107], v[128:131]\n\t"
    "ds_read_b64_tr_b16 v[30:31], v3 offset:2048\n\t"
    "v_mfma_f32_16x16x32_bf16 v[132:135], v[32:35], v[108:111], v[132:135]\n\t"
    "v_add_u32 v0, 0xffffe000, v0\n\t"
    "ds_read_b64_tr_b16 v[32:33], v0 offset:0\n\t"
    "s_waitcnt lgkmcnt(5)\n\t"
    "v_mfma_f32_16x16x32_bf16 v[136:139], v[36:39], v[104:107], v[136:139]\n\t"
    "ds_read_b64_tr_b16 v[34:35], v0 offset:2048\n\t"
    "s_add_u32 s85, s44, 0x4800\n\t"
    "buffer_load_dwordx4 v[168:171], v6, s[28:31], s85 offen sc1\n\t"
    "v_mfma_f32_16x16x32_bf16 v[140:143], v[36:39], v[108:111], v[140:143]\n\t"
    "v_add_u32 v2, 0xffffe000, v2\n\t"
    "ds_read_b64_tr_b16 v[36:37], v2 offset:0\n\t"
    "s_setprio 1\n\t"
    "s_waitcnt lgkmcnt(5)\n\t"
    "v_mfma_f32_16x16x32_bf16 v[40:43], v[24:27], v[144:147], v[40:43]\n\t"
    "s_add_u32 s85, s44, 0x4800\n\t"
    "buffer_load_dword v20, v7, s[28:31], s85 offen offset:2048 sc1\n\t"
    "ds_read_b64_tr_b16 v[38:39], v2 offset:2048\n\t"
    "v_mfma_f32_16x16x32_bf16 v[44:47], v[24:27], v[148:151], v[44:47]\n\t"
    "v_add_u32 v1, 0xffffe000, v1\n\t"
    "ds_read_b64_tr_b16 v[24:25], v1 offset:0\n\t"
    "s_waitcnt lgkmcnt(5)\n\t"
    "v_mfma_f32_16x16x32_bf16 v[48:51], v[28:31], v[144:147], v[48:51]\n\t"
    "ds_read_b64_tr_b16 v[26:27], v1 offset:2048\n\t"
    "s_add_u32 m0, s36, 40960\n\t"
    "s_add_u32 s85, s40, 0x24000\n\t"
    "buffer_load_dwordx4 v5, s[24:27], s85 offen lds\n\t"
    "v_mfma_f32_16x16x32_bf16 v[52:55], v[28:31], v[148:151], v[52:55]\n\t"
    "v_add_u32 v3, 0xffffe000, v3\n\t"
    "ds_read_b64_tr_b16 v[28:29], v3 offset:0\n\t"
    "s_setprio 0\n\t"
    "s_waitcnt lgkmcnt(5)\n\t"
    "v_mfma_f32_16x16x32_bf16 v[56:59], v[32:35], v[144:147], v[56:59]\n\t"
    "ds_read_b64_tr_b16 v[30:31], v3 offset:2048\n\t"
    "v_mfma_f32_16x16x32_bf16 v[60:63], v[32:35], v[148:151], v[60:63]\n\t"
    "v_add_u32 v0, 0xffffe000, v0\n\t"
    "ds_read_b64_tr_b16 v[32:33], v0 offset:0\n\t"
    "s_waitcnt lgkmcnt(5)\n\t"
    "v_mfma_f32_16x16x32_bf16 v[64:67], v[36:39], v[144:147], v[64:67]\n\t"
    "ds_read_b64_tr_b16 v[34:35], v0 offset:2048\n\t"
    "s_add_u32 s85, s44, 0x4800\n\t"
    "buffer_load_dwordx4 v[172:175], v6, s[28:31], s85 offen offset:1024 sc1\n\t"
    "v_mfma_f32_16x16x32_bf16 v[68:71], v[36:39], v[148:151], v[68:71]\n\t"
    "v_add_u32 v2, 0xffffe000, v2\n\t"
    "ds_read_b64_tr_b16 v[36:37], v2 offset:0\n\t"
    "s_barrier\n\t"
    "s_setprio 3\n\t"
    "s_waitcnt lgkmcnt(5)\n\t"
    "v_mfma_f32_16x16x32_bf16 v[72:75], v[24:27], v[144:147], v[72:75]\n\t"
    "ds_read_b64_tr_b16 v[38:39], v2 offset:2048\n\t"
    "v_mfma_f32_16x16x32_bf16 v[76:79], v[24:27], v[148:151], v[76:79]\n\t"
    "ds_read_b64_tr_b16 v[24:25], v1 offset:40960\n\t"
    "s_waitcnt lgkmcnt(5)\n\t"
    "v_mfma_f32_16x16x32_bf16 v[80:83], v[28:31], v[144:147], v[80:83]\n\t"
    "ds_read_b64_tr_b16 v[26:27], v1 offset:43008\n\t"
    "s_add_u32 m0, s36, 98304\n\t"
    "s_add_u32 s85, s41, 0x24000\n\t"
    "s_add_u32 s85, s85, s101\n\t"
    "buffer_load_dwordx4 v5, s[24:27], s85 offen lds\n\t"
    "v_mfma_f32_16x16x32_bf16 v[84:87], v[28:31], v[148:151], v[84:87]\n\t"
    "ds_read_b64_tr_b16 v[28:29], v3 offset:40960\n\t"
    "s_waitcnt lgkmcnt(5)\n\t"
    "v_mfma_f32_16x16x32_bf16 v[88:91], v[32:35], v[144:147], v[88:91]\n\t"
    "ds_read_b64_tr_b16 v[30:31], v3 offset:43008\n\t"
    "v_mfma_f32_16x16x32_bf16 v[92:95], v[32:35], v[148:151], v[92:95]\n\t"
    "ds_read_b64_tr_b16 v[32:33], v0 offset:40960\n\t"
    "s_waitcnt lgkmcnt(5)\n\t"
    "v_mfma_f32_16x16x32_bf16 v[96:99], v[36:39], v[144:147], v[96:99]\n\t"
    "ds_read_b64_tr_b16 v[34:35], v0 offset:43008\n\t"
    "s_add_u32 s85, s45, 0x4800\n\t"
    "buffer_load_dwordx4 v[104:107], v6, s[28:31], s85 offen sc1\n\t"
    "v_mfma_f32_16x16x32_bf16 v[100:103], v[36:39], v[148:151], v[100:103]\n\t"
    "ds_read_b64_tr_b16 v[36:37], v2 offset:40960\n\t"
    "s_waitcnt lgkmcnt(5)\n\t"
    "v_mfma_f32_16x16x32_bf16 v[40:43], v[24:27], v[152:155], v[40:43]\n\t"
    "ds_read_b64_tr_b16 v[38:39], v2 offset:43008\n\t"
    "s_add_u32 s85, s45, 0x4800\n\t"
    "buffer_load_dword v21, v7, s[28:31], s85 offen offset:2048 sc1\n\t"
    "v_mfma_f32_16x16x32_bf16 v[44:47], v[24:27], v[156:159], v[44:47]\n\t"
    "ds_read_b64_tr_b16 v[24:25], v1 offset:32768\n\t"
    "s_waitcnt lgkmcnt(5)\n\t"
    "v_mfma_f32_16x16x32_bf16 v[48:51], v[28:31], v[152:155], v[48:51]\n\t"
    "ds_read_b64_tr_b16 v[26:27], v1 offset:34816\n\t"
    "s_add_u32 m0, s36, 90112\n\t"
    "s_add_u32 s85, s41, 0x24000\n\t"
    "buffer_load_dwordx4 v5, s[24:27], s85 offen lds\n\t"
    "v_mfma_f32_16x16x32_bf16 v[52:55], v[28:31], v[156:159], v[52:55]\n\t"
    "ds_read_b64_tr_b16 v[28:29], v3 offset:32768\n\t"
    "s_setprio 2\n\t"
    "s_waitcnt lgkmcnt(5)\n\t"
    "v_mfma_f32_16x16x32_bf16 v[56:59], v[32:35], v[152:155], v[56:59]\n\t"
    "ds_read_b64_tr_b16 v[30:31], v3 offset:34816\n\t"
    "v_mfma_f32_16x16x32_bf16 v[60:63], v[32:35], v[156:159], v[60:63]\n\t"
    "s_waitcnt vmcnt(11)\n\t"
    "v_add3_u32 v19, v160, v161, v19\n\t"
    "v_add3_u32 v19, v162, v163, v19\n\t"
    "s_waitcnt vmcnt(9)\n\t"
    "v_add3_u32 v19, v164, v165, v19\n\t"
    "v_add3_u32 v19, v166, v167, v19\n\t"
    "v_cmp_ne_u32 vcc, s48, v19\n\t"
    "s_cbranch_vccnz LB7_%=\n\t"
    "LB8_%=:\n\t"
    "ds_read_b64_tr_b16 v[32:33], v0 offset:32768\n\t"
    "s_waitcnt lgkmcnt(5)\n\t"
    "v_mfma_f32_16x16x32_bf16 v[64:67], v[36:39], v[152:155], v[64:67]\n\t"
    "ds_read_b64_tr_b16 v[34:35], v0 offset:34816\n\t"
    "s_add_u32 s85, s45, 0x4800\n\t"
    "buffer_load_dwordx4 v[108:111], v6, s[28:31], s85 offen offset:1024 sc1\n\t"
    "v_mfma_f32_16x16x32_bf16 v[68:71], v[36:39], v[156:159], v[68:71]\n\t"
    "ds_read_b64_tr_b16 v[36:37], v2 offset:32768\n\t"
    "s_setprio 1\n\t"
    "s_waitcnt lgkmcnt(5)\n\t"
    "v_mfma_f32_16x16x32_bf16 v[72:75], v[24:27], v[152:155], v[72:75]\n\t"
    "ds_read_b64_tr_b16 v[38:39], v2 offset:34816\n\t"
    "v_mfma_f32_16x16x32_bf16 v[76:79], v[24:27], v[156:159], v[76:79]\n\t"
    "ds_read_b64_tr_b16 v[24:25], v1 offset:24576\n\t"
    "s_waitcnt lgkmcnt(5)\n\t"
    "v_mfma_f32_16x16x32_bf16 v[80:83], v[28:31], v[152:155], v[80:83]\n\t"
    "ds_read_b64_tr_b16 v[26:27], v1 offset:26624\n\t"
    "s_add_u32 m0, s36, 32768\n\t"
    "s_add_u32 s85, s39, 0x36000\n\t"
    "s_add_u32 s85, s85, s101\n\t"
    "buffer_load_dwordx4 v5, s[24:27], s85 offen lds\n\t"
    "v_mfma_f32_16x16x32_bf16 v[84:87], v[28:31], v[156:159], v[84:87]\n\t"
    "ds_read_b64_tr_b16 v[28:29], v3 offset:24576\n\t"
    "LIPB2x0_%=:\n\t"
    "s_cbranch_execz LIPRB_%=\n\t"
    "s_setprio 0\n\t"
    "s_waitcnt lgkmcnt(5)\n\t"
    "v_mfma_f32_16x16x32_bf16 v[88:91], v[32:35], v[152:155], v[88:91]\n\t"
    "ds_read_b64_tr_b16 v[30:31], v3 offset:26624\n\t"
    "v_mfma_f32_16x16x32_bf16 v[92:95], v[32:35], v[156:159], v[92:95]\n\t"
    "ds_read_b64_tr_b16 v[32:33], v0 offset:24576\n\t"
    "s_waitcnt lgkmcnt(5)\n\t"
    "v_mfma_f32_16x16x32_bf16 v[96:99], v[36:39], v[152:155], v[96:99]\n\t"
    "ds_read_b64_tr_b16 v[34:35], v0 offset:26624\n\t"
    "v_mfma_f32_16x16x32_bf16 v[100:103], v[36:39], v[156:159], v[100:103]\n\t"
    "ds_read_b64_tr_b16 v[36:37], v2 offset:24576\n\t"
    "s_barrier\n\t"
    "s_setprio 3\n\t"
    "s_waitcnt lgkmcnt(5)\n\t"
    "v_mfma_f32_16x16x32_bf16 v[40:43], v[24:27], v[160:163], v[40:43]\n\t"
    "ds_read_b64_tr_b16 v[38:39], v2 offset:26624\n\t"
    "v_mfma_f32_16x16x32_bf16 v[44:47], v[24:27], v[164:167], v[44:47]\n\t"
    "v_add_u32 v1, 0x4000, v1\n\t"
    "ds_read_b64_tr_b16 v[24:25], v1 offset:57344\n\t"
    "s_waitcnt lgkmcnt(5)\n\t"
    "v_mfma_f32_16x16x32_bf16 v[48:51], v[28:31], v[160:163], v[48:51]\n\t"
    "ds_read_b64_tr_b16 v[26:27], v1 offset:59392\n\t"
    "s_add_u32 m0, s36, 122880\n\t"
    "s_add_u32 s85, s39, 0x36000\n\t"
    "buffer_load_dwordx4 v5, s[24:27], s85 offen lds\n\t"
    "v_mfma_f32_16x16x32_bf16 v[52:55], v[28:31], v[164:167], v[52:55]\n\t"
    "v_add_u32 v3, 0x4000, v3\n\t"
    "ds_read_b64_tr_b16 v[28:29], v3 offset:57344\n\t"
    "s_waitcnt lgkmcnt(5)\n\t"
    "v_mfma_f32_16x16x32_bf16 v[56:59], v[32:35], v[160:163], v[56:59]\n\t"
    "ds_read_b64_tr_b16 v[30:31], v3 offset:59392\n\t"
    "v_mfma_f32_16x16x32_bf16 v[60:63], v[32:35], v[164:167], v[60:63]\n\t"
    "v_add_u32 v0, 0x4000, v0\n\t"
    "ds_read_b64_tr_b16 v[32:33], v0 offset:57344\n\t"
    "s_waitcnt vmcnt(9)\n\t"
    "v_add3_u32 v20, v168, v169, v20\n\t"
    "v_add3_u32 v20, v170, v171, v20\n\t"
    "s_waitcnt vmcnt(7)\n\t"
    "v_add3_u32 v20, v172, v173, v20\n\t"
    "v_add3_u32 v20, v174, v175, v20\n\t"
    "v_cmp_ne_u32 vcc, s49, v20\n\t"
    "s_cbranch_vccnz LB9_%=\n\t"
    "LB10_%=:\n\t"
    "s_waitcnt lgkmcnt(5)\n\t"
    "v_mfma_f32_16x16x32_bf16 v[64:67], v[36:39], v[160:163], v[64:67]\n\t"
    "ds_read_b64_tr_b16 v[34:35], v0 offset:59392\n\t"
    "v_mfma_f32_16x16x32_bf16 v[68:71], v[36:39], v[164:167], v[68:71]\n\t"
    "v_add_u32 v2, 0x4000, v2\n\t"
    "ds_read_b64_tr_b16 v[36:37], v2 offset:57344\n\t"
    "s_waitcnt lgkmcnt(5)\n\t"
    "v_mfma_f32_16x16x32_bf16 v[72:75], v[24:27], v[160:163], v[72:75]\n\t"
    "ds_read_b64_tr_b16 v[38:39], v2 offset:59392\n\t"
    "v_mfma_f32_16x16x32_bf16 v[76:79], v[24:27], v[164:167], v[76:79]\n\t"
    "ds_read_b64_tr_b16 v[24:25], v1 offset:49152\n\t"
    "s_waitcnt lgkmcnt(5)\n\t"
    "v_mfma_f32_16x16x32_bf16 v[80:83], v[28:31], v[160:163], v[80:83]\n\t"
    "ds_read_b64_tr_b16 v[26:27], v1 offset:51200\n\t"
    "s_add_u32 m0, s36, 114688\n\t"
    "s_add_u32 s85, s40, 0x36000\n\t"
    "s_add_u32 s85, s85, s101\n\t"
    "buffer_load_dwordx4 v5, s[24:27], s85 offen lds\n\t"
    "v_mfma_f32_16x16x32_bf16 v[84:87], v[28:31], v[164:167], v[84:87]\n\t"
    "ds_read_b64_tr_b16 v[28:29], v3 offset:49152\n\t"
    "s_setprio 2\n\t"
    "s_waitcnt lgkmcnt(5)\n\t"
    "v_mfma_f32_16x16x32_bf16 v[88:91], v[32:35], v[160:163], v[88:91]\n\t"
    "ds_read_b64_tr_b16 v[30:31], v3 offset:51200\n\t"
    "v_mfma_f32_16x16x32_bf16 v[92:95], v[32:35], v[164:167], v[92:95]\n\t"
    "ds_read_b64_tr_b16 v[32:33], v0 offset:49152\n\t"
    "s_waitcnt lgkmcnt(5)\n\t"
    "v_mfma_f32_16x16x32_bf16 v[96:99], v[36:39], v[160:163], v[96:99]\n\t"
    "ds_read_b64_tr_b16 v[34:35], v0 offset:51200\n\t"
    "s_add_u32 s85, s43, 0x9000\n\t"
    "buffer_load_dwordx4 v[176:179], v6, s[28:31], s85 offen sc1\n\t"
    "v_mfma_f32_16x16x32_bf16 v[100:103], v[36:39], v[164:167], v[100:103]\n\t"
    "ds_read_b64_tr_b16 v[36:37], v2 offset:49152\n\t"
    "ds_read_b64_tr_b16 v[38:39], v2 offset:51200\n\t"
    "s_setprio 1\n\t"
    "s_waitcnt lgkmcnt(6)\n\t"
    "v_mfma_f32_16x16x32_bf16 v[40:43], v[24:27], v[168:171], v[40:43]\n\t"
    "s_add_u32 s85, s43, 0x9000\n\t"
    "buffer_load_dword v22, v7, s[28:31], s85 offen offset:2048 sc1\n\t"
    "v_mfma_f32_16x16x32_bf16 v[44:47], v[24:27], v[172:175], v[44:47]\n\t"
    "ds_read_b64_tr_b16 v[24:25], v1 offset:40960\n\t"
    "s_waitcnt lgkmcnt(5)\n\t"
    "v_mfma_f32_16x16x32_bf16 v[48:51], v[28:31], v[168:171], v[48:51]\n\t"
    "ds_read_b64_tr_b16 v[26:27], v1 offset:43008\n\t"
    "s_add_u32 m0, s36, 81920\n\t"
    "s_add_u32 s85, s40, 0x36000\n\t"
    "buffer_load_dwordx4 v5, s[24:27], s85 offen lds\n\t"
    "v_mfma_f32_16x16x32_bf16 v[52:55], v[28:31], v[172:175], v[52:55]\n\t"
    "ds_read_b64_tr_b16 v[28:29], v3 offset:40960\n\t"
    "s_setprio 0\n\t"
    "s_waitcnt lgkmcnt(5)\n\t"
    "v_mfma_f32_16x16x32_bf16 v[56:59], v[32:35], v[168:171], v[56:59]\n\t"
    "ds_read_b64_tr_b16 v[30:31], v3 offset:43008\n\t"
    "v_mfma_f32_16x16x32_bf16 v[60:63], v[32:35], v[172:175], v[60:63]\n\t"
    "ds_read_b64_tr_b16 v[32:33], v0 offset:40960\n\t"
    "s_waitcnt vmcnt(8)\n\t"
    "v_add3_u32 v21, v104, v105, v21\n\t"
    "v_add3_u32 v21, v106, v107, v21\n\t"
    "s_waitcnt vmcnt(6)\n\t"
    "v_add3_u32 v21, v108, v109, v21\n\t"
    "v_add3_u32 v21, v110, v111, v21\n\t"
    "v_cmp_ne_u32 vcc, s50, v21\n\t"
    "s_cbranch_vccnz LB11_%=\n\t"
    "LB12_%=:\n\t"
    "s_waitcnt lgkmcnt(5)\n\t"
    "v_mfma_f32_16x16x32_bf16 v[64:67], v[36:39], v[168:171], v[64:67]\n\t"
    "ds_read_b64_tr_b16 v[34:35], v0 offset:43008\n\t"
    "s_add_u32 s85, s43, 0x9000\n\t"
    "buffer_load_dwordx4 v[180:183], v6, s[28:31], s85 offen offset:1024 sc1\n\t"
    "v_mfma_f32_16x16x32_bf16 v[68:71], v[36:39], v[172:175], v[68:71]\n\t"
    "ds_read_b64_tr_b16 v[36:37], v2 offset:40960\n\t"
    "s_barrier\n\t"
    "s_setprio 3\n\t"
    "s_waitcnt lgkmcnt(5)\n\t"
    "v_mfma_f32_16x16x32_bf16 v[72:75], v[24:27], v[168:171], v[72:75]\n\t"
    "ds_read_b64_tr_b16 v[38:39], v2 offset:43008\n\t"
    "v_mfma_f32_16x16x32_bf16 v[76:79], v[24:27], v[172:175], v[76:79]\n\t"
    "ds_read_b64_tr_b16 v[24:25], v1 offset:32768\n\t"
    "s_waitcnt lgkmcnt(5)\n\t"
    "v_mfma_f32_16x16x32_bf16 v[80:83], v[28:31], v[168:171], v[80:83]\n\t"
    "ds_read_b64_tr_b16 v[26:27], v1 offset:34816\n\t"
    "s_add_u32 m0, s36, 155648\n\t"
    "s_add_u32 s85, s41, 0x36000\n\t"
    "s_add_u32 s85, s85, s101\n\t"
    "buffer_load_dwordx4 v5, s[24:27], s85 offen lds\n\t"
    "v_mfma_f32_16x16x32_bf16 v[84:87], v[28:31], v[172:175], v[84:87]\n\t"
    "ds_read_b64_tr_b16 v[28:29], v3 offset:32768\n\t"
    "ds_read_b64_tr_b16 v[30:31], v3 offset:34816\n\t"
    "s_waitcnt lgkmcnt(6)\n\t"
    "v_mfma_f32_16x16x32_bf16 v[88:91], v[32:35], v[168:171], v[88:91]\n\t"
    "v_mfma_f32_16x16x32_bf16 v[92:95], v[32:35], v[172:175], v[92:95]\n\t"
    "ds_read_b64_tr_b16 v[32:33], v0 offset:32768\n\t"
    "ds_read_b64_tr_b16 v[34:35], v0 offset:34816\n\t"
    "s_waitcnt lgkmcnt(6)\n\t"
    "v_mfma_f32_16x16x32_bf16 v[96:99], v[36:39], v[168:171], v[96:99]\n\t"
    "s_add_u32 s85, s44, 0x9000\n\t"
    "buffer_load_dwordx4 v[184:187], v6, s[28:31], s85 offen sc1\n\t"
    "v_mfma_f32_16x16x32_bf16 v[100:103], v[36:39], v[172:175], v[100:103]\n\t"
    "ds_read_b64_tr_b16 v[36:37], v2 offset:32768\n\t"
    "s_waitcnt lgkmcnt(5)\n\t"
    "v_mfma_f32_16x16x32_bf16 v[40:43], v[24:27], v[104:107], v[40:43]\n\t"
    "ds_read_b64_tr_b16 v[38:39], v2 offset:34816\n\t"
    "s_add_u32 s85, s44, 0x9000\n\t"
    "buffer_load_dword v23, v7, s[28:31], s85 offen offset:2048 sc1\n\t"
    "v_mfma_f32_16x16x32_bf16 v[44:47], v[24:27], v[108:111], v[44:47]\n\t"
    "v_add_u32 v1, 0xffffa000, v1\n\t"
    "ds_read_b64_tr_b16 v[24:25], v1 offset:0\n\t"
    "ds_read_b64_tr_b16 v[26:27], v1 offset:2048\n\t"
    "s_waitcnt lgkmcnt(6)\n\t"
    "v_mfma_f32_16x16x32_bf16 v[48:51], v[28:31], v[104:107], v[48:51]\n\t"
    "s_add_u32 m0, s36, 147456\n\t"
    "s_add_u32 s85, s41, 0x36000\n\t"
    "buffer_load_dwordx4 v5, s[24:27], s85 offen lds\n\t"
    "v_mfma_f32_16x16x32_bf16 v[52:55], v[28:31], v[108:111], v[52:55]\n\t"
    "v_add_u32 v3, 0xffffa000, v3\n\t"
    "ds_read_b64_tr_b16 v[28:29], v3 offset:0\n\t"
    "ds_read_b64_tr_b16 v[30:31], v3 offset:2048\n\t"
    "s_setprio 2\n\t"
    "s_waitcnt lgkmcnt(6)\n\t"
    "v_mfma_f32_16x16x32_bf16 v[56:59], v[32:35], v[104:107], v[56:59]\n\t"
    "v_mfma_f32_16x16x32_bf16 v[60:63], v[32:35], v[108:111], v[60:63]\n\t"
    "v_add_u32 v0, 0xffffa000, v0\n\t"
    "ds_read_b64_tr_b16 v[32:33], v0 offset:0\n\t"
    "ds_read_b64_tr_b16 v[34:35], v0 offset:2048\n\t"
    "s_waitcnt lgkmcnt(6)\n\t"
    "v_mfma_f32_16x16x32_bf16 v[64:67], v[36:39], v[104:107], v[64:67]\n\t"
    "s_add_u32 s85, s44, 0x9000\n\t"
    "buffer_load_dwordx4 v[188:191], v6, s[28:31], s85 offen offset:1024 sc1\n\t"
    "v_mfma_f32_16x16x32_bf16 v[68:71], v[36:39], v[108:111], v[68:71]\n\t"
    "v_add_u32 v2, 0xffffa000, v2\n\t"
    "ds_read_b64_tr_b16 v[36:37], v2 offset:0\n\t"
    "s_setprio 1\n\t"
    "s_waitcnt lgkmcnt(5)\n\t"
    "v_mfma_f32_16x16x32_bf16 v[72:75], v[24:27], v[104:107], v[72:75]\n\t"
    "ds_read_b64_tr_b16 v[38:39], v2 offset:2048\n\t"
    "v_mfma_f32_16x16x32_bf16 v[76:79], v[24:27], v[108:111], v[76:79]\n\t"
    "v_add_u32 v1, 0xffffe000, v1\n\t"
    "ds_read_b64_tr_b16 v[24:25], v1 offset:0\n\t"
    "s_waitcnt lgkmcnt(5)\n\t"
    "v_mfma_f32_16x16x32_bf16 v[80:83], v[28:31], v[104:107], v[80:83]\n\t"
    "ds_read_b64_tr_b16 v[26:27], v1 offset:2048\n\t"
    "s_add_u32 m0, s36, 106496\n\t"
    "s_add_u32 s85, s39, 0x48000\n\t"
    "s_add_u32 s85, s85, s101\n\t"
    "buffer_load_dwordx4 v5, s[24:27], s85 offen lds\n\t"
    "v_mfma_f32_16x16x32_bf16 v[84:87], v[28:31], v[108:111], v[84:87]\n\t"
    "v_add_u32 v3, 0xffffe000, v3\n\t"
    "ds_read_b64_tr_b16 v[28:29], v3 offset:0\n\t"
    "s_setprio 0\n\t"
    "s_waitcnt lgkmcnt(5)\n\t"
    "v_mfma_f32_16x16x32_bf16 v[88:91], v[32:35], v[104:107], v[88:91]\n\t"
    "ds_read_b64_tr_b16 v[30:31], v3 offset:2048\n\t"
    "v_mfma_f32_16x16x32_bf16 v[92:95], v[32:35], v[108:111], v[92:95]\n\t"
    "v_add_u32 v0, 0xffffe000, v0\n\t"
    "ds_read_b64_tr_b16 v[32:33], v0 offset:0\n\t"
    "s_waitcnt vmcnt(8)\n\t"
    "v_add3_u32 v22, v176, v177, v22\n\t"
    "v_add3_u32 v22, v178, v179, v22\n\t"
    "s_waitcnt vmcnt(6)\n\t"
    "v_add3_u32 v22, v180, v181, v22\n\t"
    "v_add3_u32 v22, v182, v183, v22\n\t"
    "v_cmp_ne_u32 vcc, s48, v22\n\t"
    "s_cbranch_vccnz LB13_%=\n\t"
    "LB14_%=:\n\t"
    "s_waitcnt lgkmcnt(5)\n\t"
    "v_mfma_f32_16x16x32_bf16 v[96:99], v[36:39], v[104:107], v[96:99]\n\t"
    "ds_read_b64_tr_b16 v[34:35], v0 offset:2048\n\t"
    "s_add_u32 s85, s45, 0x9000\n\t"
    "buffer_load_dwordx4 v[192:195], v6, s[28:31], s85 offen sc1\n\t"
    "v_mfma_f32_16x16x32_bf16 v[100:103], v[36:39], v[108:111], v[100:103]\n\t"
    "v_add_u32 v2, 0xffffe000, v2\n\t"
    "ds_read_b64_tr_b16 v[36:37], v2 offset:0\n\t"
    "s_barrier\n\t"
    "s_setprio 3\n\t"
    "s_waitcnt lgkmcnt(5)\n\t"
    "v_mfma_f32_16x16x32_bf16 v[40:43], v[24:27], v[176:179], v[40:43]\n\t"
    "ds_read_b64_tr_b16 v[38:39], v2 offset:2048\n\t"
    "s_add_u32 s85, s45, 0x9000\n\t"
    "buffer_load_dword v16, v7, s[28:31], s85 offen offset:2048 sc1\n\t"
    "v_mfma_f32_16x16x32_bf16 v[44:47], v[24:27], v[180:183], v[44:47]\n\t"
    "v_add_u32 v1, 0xffffe000, v1\n\t"
    "ds_read_b64_tr_b16 v[24:25], v1 offset:0\n\t"
    "s_waitcnt lgkmcnt(5)\n\t"
    "v_mfma_f32_16x16x32_bf16 v[48:51], v[28:31], v[176:179], v[48:51]\n\t"
    "ds_read_b64_tr_b16 v[26:27], v1 offset:2048\n\t"
    "s_add_u32 m0, s36, 139264\n\t"
    "s_add_u32 s85, s40, 0x48000\n\t"
    "s_add_u32 s85, s85, s101\n\t"
    "buffer_load_dwordx4 v5, s[24:27], s85 offen lds\n\t"
    "v_mfma_f32_16x16x32_bf16 v[52:55], v[28:31], v[180:183], v[52:55]\n\t"
    "v_add_u32 v3, 0xffffe000, v3\n\t"
    "ds_read_b64_tr_b16 v[28:29], v3 offset:0\n\t"
    "ds_read_b64_tr_b16 v[30:31], v3 offset:2048\n\t"
    "s_waitcnt lgkmcnt(6)\n\t"
    "v_mfma_f32_16x16x32_bf16 v[56:59], v[32:35], v[176:179], v[56:59]\n\t"
    "v_mfma_f32_16x16x32_bf16 v[60:63], v[32:35], v[180:183], v[60:63]\n\t"
    "v_add_u32 v0, 0xffffe000, v0\n\t"
    "ds_read_b64_tr_b16 v[32:33], v0 offset:0\n\t"
    "s_waitcnt lgkmcnt(5)\n\t"
    "v_mfma_f32_16x16x32_bf16 v[64:67], v[36:39], v[176:179], v[64:67]\n\t"
    "ds_read_b64_tr_b16 v[34:35], v0 offset:2048\n\t"
    "s_add_u32 s85, s45, 0x9000\n\t"
    "buffer_load_dwordx4 v[196:199], v6, s[28:31], s85 offen offset:1024 sc1\n\t"
    "v_mfma_f32_16x16x32_bf16 v[68:71], v[36:39], v[180:183], v[68:71]\n\t"
    "v_add_u32 v2, 0xffffe000, v2\n\t"
    "ds_read_b64_tr_b16 v[36:37], v2 offset:0\n\t"
    "s_waitcnt lgkmcnt(5)\n\t"
    "v_mfma_f32_16x16x32_bf16 v[72:75], v[24:27], v[176:179], v[72:75]\n\t"
    "ds_read_b64_tr_b16 v[38:39], v2 offset:2048\n\t"
    "v_mfma_f32_16x16x32_bf16 v[76:79], v[24:27], v[180:183], v[76:79]\n\t"
    "v_add_u32 v1, 0xffffe000, v1\n\t"
    "ds_read_b64_tr_b16 v[24:25], v1 offset:0\n\t"
    "s_waitcnt lgkmcnt(5)\n\t"
    "v_mfma_f32_16x16x32_bf16 v[80:83], v[28:31], v[176:179], v[80:83]\n\t"
    "ds_read_b64_tr_b16 v[26:27], v1 offset:2048\n\t"
    "s_add_u32 m0, s36, 131072\n\t"
    "s_add_u32 s85, s41, 0x48000\n\t"
    "s_add_u32 s85, s85, s101\n\t"
    "buffer_load_dwordx4 v5, s[24:27], s85 offen lds\n\t"
    "v_mfma_f32_16x16x32_bf16 v[84:87], v[28:31], v[180:183], v[84:87]\n\t"
    "v_add_u32 v3, 0xffffe000, v3\n\t"
    "ds_read_b64_tr_b16 v[28:29], v3 offset:0\n\t"
    "s_setprio 2\n\t"
    "s_waitcnt lgkmcnt(5)\n\t"
    "v_mfma_f32_16x16x32_bf16 v[88:91], v[32:35], v[176:179], v[88:91]\n\t"
    "ds_read_b64_tr_b16 v[30:31], v3 offset:2048\n\t"
    "v_mfma_f32_16x16x32_bf16 v[92:95], v[32:35], v[180:183], v[92:95]\n\t"
    "v_add_u32 v0, 0xffffe000, v0\n\t"
    "ds_read_b64_tr_b16 v[32:33], v0 offset:0\n\t"
    "s_waitcnt vmcnt(8)\n\t"
    "v_add3_u32 v23, v184, v185, v23\n\t"
    "v_add3_u32 v23, v186, v187, v23\n\t"
    "s_waitcnt vmcnt(6)\n\t"
    "v_add3_u32 v23, v188, v189, v23\n\t"
    "v_add3_u32 v23, v190, v191, v23\n\t"
    "v_cmp_ne_u32 vcc, s49, v23\n\t"
    "s_cbranch_vccnz LB15_%=\n\t"
    "LB16_%=:\n\t"
    "s_waitcnt lgkmcnt(5)\n\t"
    "v_mfma_f32_16x16x32_bf16 v[96:99], v[36:39], v[176:179], v[96:99]\n\t"
    "ds_read_b64_tr_b16 v[34:35], v0 offset:2048\n\t"
    "s_add_u32 s85, s43, 0xd800\n\t"
    "buffer_load_dwordx4 v[200:203], v6, s[28:31], s85 offen sc1\n\t"
    "v_mfma_f32_16x16x32_bf16 v[100:103], v[36:39], v[180:183], v[100:103]\n\t"
    "v_add_u32 v2, 0xffffe000, v2\n\t"
    "ds_read_b64_tr_b16 v[36:37], v2 offset:0\n\t"
    "s_setprio 1\n\t"
    "s_waitcnt lgkmcnt(5)\n\t"
    "v_mfma_f32_16x16x32_bf16 v[40:43], v[24:27], v[184:187], v[40:43]\n\t"
    "ds_read_b64_tr_b16 v[38:39], v2 offset:2048\n\t"
    "s_add_u32 s85, s43, 0xd800\n\t"
    "buffer_load_dword v17, v7, s[28:31], s85 offen offset:2048 sc1\n\t"
    "v_mfma_f32_16x16x32_bf16 v[44:47], v[24:27], v[188:191], v[44:47]\n\t"
    "v_add_u32 v1, 0xffffe000, v1\n\t"
    "ds_read_b64_tr_b16 v[24:25], v1 offset:0\n\t"
    "s_waitcnt lgkmcnt(5)\n\t"
    "v_mfma_f32_16x16x32_bf16 v[48:51], v[28:31], v[184:187], v[48:51]\n\t"
    "ds_read_b64_tr_b16 v[26:27], v1 offset:2048\n\t"
    "s_add_u32 m0, s36, 73728\n\t"
    "s_add_u32 s85, s39, 0x48000\n\t"
    "buffer_load_dwordx4 v5, s[24:27], s85 offen lds\n\t"
    "v_mfma_f32_16x16x32_bf16 v[52:55], v[28:31], v[188:191], v[52:55]\n\t"
    "v_add_u32 v3, 0xffffe000, v3\n\t"
    "ds_read_b64_tr_b16 v[28:29], v3 offset:0\n\t"
    "s_setprio 0\n\t"
    "s_waitcnt lgkmcnt(5)\n\t"
    "v_mfma_f32_16x16x32_bf16 v[56:59], v[32:35], v[184:187], v[56:59]\n\t"
    "ds_read_b64_tr_b16 v[30:31], v3 offset:2048\n\t"
    "v_mfma_f32_16x16x32_bf16 v[60:63], v[32:35], v[188:191], v[60:63]\n\t"
    "v_add_u32 v0, 0xffffe000, v0\n\t"
    "ds_read_b64_tr_b16 v[32:33], v0 offset:0\n\t"
    "s_waitcnt lgkmcnt(5)\n\t"
    "v_mfma_f32_16x16x32_bf16 v[64:67], v[36:39], v[184:187], v[64:67]\n\t"
    "ds_read_b64_tr_b16 v[34:35], v0 offset:2048\n\t"
    "s_add_u32 s85, s43, 0xd800\n\t"
    "buffer_load_dwordx4 v[204:207], v6, s[28:31], s85 offen offset:1024 sc1\n\t"
    "v_mfma_f32_16x16x32_bf16 v[68:71], v[36:39], v[188:191], v[68:71]\n\t"
    "v_add_u32 v2, 0xffffe000, v2\n\t"
    "ds_read_b64_tr_b16 v[36:37], v2 offset:0\n\t"
    "s_barrier\n\t"
    "s_setprio 3\n\t"
    "s_waitcnt lgkmcnt(5)\n\t"
    "v_mfma_f32_16x16x32_bf16 v[72:75], v[24:27], v[184:187], v[72:75]\n\t"
    "ds_read_b64_tr_b16 v[38:39], v2 offset:2048\n\t"
    "v_mfma_f32_16x16x32_bf16 v[76:79], v[24:27], v[188:191], v[76:79]\n\t"
    "ds_read_b64_tr_b16 v[24:25], v1 offset:57344\n\t"
    "s_waitcnt lgkmcnt(5)\n\t"
    "v_mfma_f32_16x16x32_bf16 v[80:83], v[28:31], v[184:187], v[80:83]\n\t"
    "ds_read_b64_tr_b16 v[26:27], v1 offset:59392\n\t"
    "s_add_u32 m0, s36, 65536\n\t"
    "s_add_u32 s85, s40, 0x48000\n\t"
    "buffer_load_dwordx4 v5, s[24:27], s85 offen lds\n\t"
    "v_mfma_f32_16x16x32_bf16 v[84:87], v[28:31], v[188:191], v[84:87]\n\t"
    "ds_read_b64_tr_b16 v[28:29], v3 offset:57344\n\t"
    "s_waitcnt lgkmcnt(5)\n\t"
    "v_mfma_f32_16x16x32_bf16 v[88:91], v[32:35], v[184:187], v[88:91]\n\t"
    "ds_read_b64_tr_b16 v[30:31], v3 offset:59392\n\t"
    "v_mfma_f32_16x16x32_bf16 v[92:95], v[32:35], v[188:191], v[92:95]\n\t"
    "ds_read_b64_tr_b16 v[32:33], v0 offset:57344\n\t"
    "ds_read_b64_tr_b16 v[34:35], v0 offset:59392\n\t"
    "s_waitcnt vmcnt(8)\n\t"
    "v_add3_u32 v16, v192, v193, v16\n\t"
    "v_add3_u32 v16, v194, v195, v16\n\t"
    "s_waitcnt vmcnt(6)\n\t"
    "v_add3_u32 v16, v196, v197, v16\n\t"
    "v_add3_u32 v16, v198, v199, v16\n\t"
    "v_cmp_ne_u32 vcc, s50, v16\n\t"
    "s_cbranch_vccnz LB17_%=\n\t"
    "LB18_%=:\n\t"
    "s_waitcnt lgkmcnt(6)\n\t"
    "v_mfma_f32_16x16x32_bf16 v[96:99], v[36:39], v[184:187], v[96:99]\n\t"
    "s_add_u32 s85, s44, 0xd800\n\t"
    "buffer_load_dwordx4 v[208:211], v6, s[28:31], s85 offen sc1\n\t"
    "v_mfma_f32_16x16x32_bf16 v[100:103], v[36:39], v[188:191], v[100:103]\n\t"
    "ds_read_b64_tr_b16 v[36:37], v2 offset:57344\n\t"
    "s_waitcnt lgkmcnt(5)\n\t"
    "v_mfma_f32_16x16x32_bf16 v[40:43], v[24:27], v[192:195], v[40:43]\n\t"
    "ds_read_b64_tr_b16 v[38:39], v2 offset:59392\n\t"
    "s_add_u32 s85, s44, 0xd800\n\t"
    "buffer_load_dword v18, v7, s[28:31], s85 offen offset:2048 sc1\n\t"
    "v_mfma_f32_16x16x32_bf16 v[44:47], v[24:27], v[196:199], v[44:47]\n\t"
    "ds_read_b64_tr_b16 v[24:25], v1 offset:49152\n\t"
    "s_waitcnt lgkmcnt(5)\n\t"
    "v_mfma_f32_16x16x32_bf16 v[48:51], v[28:31], v[192:195], v[48:51]\n\t"
    "ds_read_b64_tr_b16 v[26:27], v1 offset:51200\n\t"
    "s_add_u32 m0, s36, 57344\n\t"
    "s_add_u32 s85, s41, 0x48000\n\t"
    "buffer_load_dwordx4 v5, s[24:27], s85 offen lds\n\t"
    "v_mfma_f32_16x16x32_bf16 v[52:55], v[28:31], v[196:199], v[52:55]\n\t"
    "ds_read_b64_tr_b16 v[28:29], v3 offset:49152\n\t"
    "s_setprio 2\n\t"
    "s_waitcnt lgkmcnt(5)\n\t"
    "v_mfma_f32_16x16x32_bf16 v[56:59], v[32:35], v[192:195], v[56:59]\n\t"
    "ds_read_b64_tr_b16 v[30:31], v3 offset:51200\n\t"
    "v_mfma_f32_16x16x32_bf16 v[60:63], v[32:35], v[196:199], v[60:63]\n\t"
    "ds_read_b64_tr_b16 v[32:33], v0 offset:49152\n\t"
    "ds_read_b64_tr_b16 v[34:35], v0 offset:51200\n\t"
    "s_waitcnt lgkmcnt(6)\n\t"
    "v_mfma_f32_16x16x32_bf16 v[64:67], v[36:39], v[192:195], v[64:67]\n\t"
    "s_add_u32 s85, s44, 0xd800\n\t"
    "buffer_load_dwordx4 v[212:215], v6, s[28:31], s85 offen offset:1024 sc1\n\t"
    "v_mfma_f32_16x16x32_bf16 v[68:71], v[36:39], v[196:199], v[68:71]\n\t"
    "ds_read_b64_tr_b16 v[36:37], v2 offset:49152\n\t"
    "s_setprio 1\n\t"
    "s_waitcnt lgkmcnt(5)\n\t"
    "v_mfma_f32_16x16x32_bf16 v[72:75], v[24:27], v[192:195], v[72:75]\n\t"
    "ds_read_b64_tr_b16 v[38:39], v2 offset:51200\n\t"
    "v_mfma_f32_16x16x32_bf16 v[76:79], v[24:27], v[196:199], v[76:79]\n\t"
    "v_add_u32 v1, 0xffffe000, v1\n\t"
    "ds_read_b64_tr_b16 v[24:25], v1 offset:0\n\t"
    "s_waitcnt lgkmcnt(5)\n\t"
    "v_mfma_f32_16x16x32_bf16 v[80:83], v[28:31], v[192:195], v[80:83]\n\t"
    "ds_read_b64_tr_b16 v[26:27], v1 offset:2048\n\t"
    "s_add_u32 m0, s36, 49152\n\t"
    "s_add_u32 s85, s41, 0x12000\n\t"
    "s_add_u32 s85, s85, s100\n\t"
    "buffer_load_dwordx4 v5, s[24:27], s85 offen lds\n\t"
    "v_mfma_f32_16x16x32_bf16 v[84:87], v[28:31], v[196:199], v[84:87]\n\t"
    "v_add_u32 v3, 0xffffe000, v3\n\t"
    "ds_read_b64_tr_b16 v[28:29], v3 offset:0\n\t"
    "s_setprio 0\n\t"
    "s_waitcnt lgkmcnt(5)\n\t"
    "v_mfma_f32_16x16x32_bf16 v[88:91], v[32:35], v[192:195], v[88:91]\n\t"
    "ds_read_b64_tr_b16 v[30:31], v3 offset:2048\n\t"
    "v_mfma_f32_16x16x32_bf16 v[92:95], v[32:35], v[196:199], v[92:95]\n\t"
    "v_add_u32 v0, 0xffffe000, v0\n\t"
    "ds_read_b64_tr_b16 v[32:33], v0 offset:0\n\t"
    "s_waitcnt vmcnt(8)\n\t"
    "v_add3_u32 v17, v200, v201, v17\n\t"
    "v_add3_u32 v17, v202, v203, v17\n\t"
    "s_waitcnt vmcnt(6)\n\t"
    "v_add3_u32 v17, v204, v205, v17\n\t"
    "v_add3_u32 v17, v206, v207, v17\n\t"
    "v_cmp_ne_u32 vcc, s48, v17\n\t"
    "s_cbranch_vccnz LB19_%=\n\t"
    "LB20_%=:\n\t"
    "s_waitcnt lgkmcnt(5)\n\t"
    "v_mfma_f32_16x16x32_bf16 v[96:99], v[36:39], v[192:195], v[96:99]\n\t"
    "ds_read_b64_tr_b16 v[34:35], v0 offset:2048\n\t"
    "s_add_u32 s85, s45, 0xd800\n\t"
    "buffer_load_dwordx4 v[216:219], v6, s[28:31], s85 offen sc1\n\t"
    "v_mfma_f32_16x16x32_bf16 v[100:103], v[36:39], v[196:199], v[100:103]\n\t"
    "v_add_u32 v2, 0xffffe000, v2\n\t"
    "ds_read_b64_tr_b16 v[36:37], v2 offset:0\n\t"
    "ds_read_b64_tr_b16 v[38:39], v2 offset:2048\n\t"
    "s_barrier\n\t"
    "s_setprio 3\n\t"
    "s_waitcnt lgkmcnt(6)\n\t"
    "v_mfma_f32_16x16x32_bf16 v[40:43], v[24:27], v[200:203], v[40:43]\n\t"
    "s_add_u32 s85, s45, 0xd800\n\t"
    "buffer_load_dword v19, v7, s[28:31], s85 offen offset:2048 sc1\n\t"
    "v_mfma_f32_16x16x32_bf16 v[44:47], v[24:27], v[204:207], v[44:47]\n\t"
    "v_add_u32 v1, 0x10000, v1\n\t"
    "ds_read_b64_tr_b16 v[24:25], v1 offset:24576\n\t"
    "s_waitcnt lgkmcnt(5)\n\t"
    "v_mfma_f32_16x16x32_bf16 v[48:51], v[28:31], v[200:203], v[48:51]\n\t"
    "ds_read_b64_tr_b16 v[26:27], v1 offset:26624\n\t"
    "s_add_u32 m0, s36, 98304\n\t"
    "s_add_u32 s85, s39, 0x24000\n\t"
    "s_add_u32 s85, s85, s100\n\t"
    "buffer_load_dwordx4 v5, s[24:27], s85 offen lds\n\t"
    "v_mfma_f32_16x16x32_bf16 v[52:55], v[28:31], v[204:207], v[52:55]\n\t"
    "v_add_u32 v3, 0x10000, v3\n\t"
    "ds_read_b64_tr_b16 v[28:29], v3 offset:24576\n\t"
    "s_waitcnt lgkmcnt(5)\n\t"
    "v_mfma_f32_16x16x32_bf16 v[56:59], v[32:35], v[200:203], v[56:59]\n\t"
    "ds_read_b64_tr_b16 v[30:31], v3 offset:26624\n\t"
    "v_mfma_f32_16x16x32_bf16 v[60:63], v[32:35], v[204:207], v[60:63]\n\t"
    "v_add_u32 v0, 0x10000, v0\n\t"
    "ds_read_b64_tr_b16 v[32:33], v0 offset:24576\n\t"
    "s_waitcnt lgkmcnt(5)\n\t"
    "v_mfma_f32_16x16x32_bf16 v[64:67], v[36:39], v[200:203], v[64:67]\n\t"
    "ds_read_b64_tr_b16 v[34:35], v0 offset:26624\n\t"
    "s_add_u32 s85, s45, 0xd800\n\t"
    "buffer_load_dwordx4 v[220:223], v6, s[28:31], s85 offen offset:1024 sc1\n\t"
    "v_mfma_f32_16x16x32_bf16 v[68:71], v[36:39], v[204:207], v[68:71]\n\t"
    "v_add_u32 v2, 0x10000, v2\n\t"
    "ds_read_b64_tr_b16 v[36:37], v2 offset:24576\n\t"
    "ds_read_b64_tr_b16 v[38:39], v2 offset:26624\n\t"
    "s_waitcnt lgkmcnt(6)\n\t"
    "v_mfma_f32_16x16x32_bf16 v[72:75], v[24:27], v[200:203], v[72:75]\n\t"
    "v_mfma_f32_16x16x32_bf16 v[76:79], v[24:27], v[204:207], v[76:79]\n\t"
    "ds_read_b64_tr_b16 v[24:25], v1 offset:16384\n\t"
    "s_xor_b32 s74, s91, 5\n\t"
    "v_readlane_b32 s0, v8, s74\n\t"
    "s_nop 0\n\t"
    "s_mul_i32 s0, s0, 0x9e3779b1\n\t"
    "s_add_u32 s0, s0, 0x7f4a7c15\n\t"
    "s_movk_i32 s3, 0x1ff\n\t"
    "s_mov_b32 s4, 0x38800100\n\t"
    "s_waitcnt lgkmcnt(5)\n\t"
    "v_mfma_f32_16x16x32_bf16 v[80:83], v[28:31], v[200:203], v[80:83]\n\t"
    "ds_read_b64_tr_b16 v[26:27], v1 offset:18432\n\t"
    "s_add_u32 m0, s36, 90112\n\t"
    "s_add_u32 s85, s40, 0x24000\n\t"
    "s_add_u32 s85, s85, s100\n\t"
    "buffer_load_dwordx4 v5, s[24:27], s85 offen lds\n\t"
    "v_mfma_f32_16x16x32_bf16 v[84:87], v[28:31], v[204:207], v[84:87]\n\t"
    "ds_read_b64_tr_b16 v[28:29], v3 offset:16384\n\t"
    "s_setprio 2\n\t"
    "s_waitcnt lgkmcnt(5)\n\t"
    "v_mfma_f32_16x16x32_bf16 v[88:91], v[32:35], v[200:203], v[88:91]\n\t"
    "ds_read_b64_tr_b16 v[30:31], v3 offset:18432\n\t"
    "v_mfma_f32_16x16x32_bf16 v[92:95], v[32:35], v[204:207], v[92:95]\n\t"
    "ds_read_b64_tr_b16 v[32:33], v0 offset:16384\n\t"
    "s_add_u32 s85, s43, 0x12000\n\t"
    "buffer_load_dwordx4 v[224:227], v6, s[28:31], s85 offen sc1\n\t"
    "s_waitcnt vmcnt(9)\n\t"
    "v_add3_u32 v18, v208, v209, v18\n\t"
    "v_add3_u32 v18, v210, v211, v18\n\t"
    "s_waitcnt vmcnt(7)\n\t"
    "v_add3_u32 v18, v212, v213, v18\n\t"
    "v_add3_u32 v18, v214, v215, v18\n\t"
    "v_cmp_ne_u32 vcc, s49, v18\n\t"
    "s_cbranch_vccnz LB21_%=\n\t"
    "LB22_%=:\n\t"
    "s_waitcnt lgkmcnt(5)\n\t"
    "v_mfma_f32_16x16x32_bf16 v[96:99], v[36:39], v[200:203], v[96:99]\n\t"
    "ds_read_b64_tr_b16 v[34:35], v0 offset:18432\n\t"
    "v_mfma_f32_16x16x32_bf16 v[100:103], v[36:39], v[204:207], v[100:103]\n\t"
    "s_add_u32 s85, s43, 0x12000\n\t"
    "buffer_load_dwordx4 v[228:231], v6, s[28:31], s85 offen offset:1024 sc1\n\t"
    "ds_read_b64_tr_b16 v[36:37], v2 offset:16384\n\t"
    "ds_read_b64_tr_b16 v[38:39], v2 offset:18432\n\t"
    "s_setprio 1\n\t"
    "s_waitcnt lgkmcnt(6)\n\t"
    "v_mfma_f32_16x16x32_bf16 v[40:43], v[24:27], v[208:211], v[40:43]\n\t"
    "s_add_u32 s85, s43, 0x12000\n\t"
    "buffer_load_dword v20, v7, s[28:31], s85 offen offset:2048 sc1\n\t"
    "v_mfma_f32_16x16x32_bf16 v[44:47], v[24:27], v[212:215], v[44:47]\n\t"
    "v_add_u32 v1, 0xffffc000, v1\n\t"
    "ds_read_b64_tr_b16 v[24:25], v1 offset:0\n\t"
    "s_xor_b32 s74, s91, 6\n\t"
    "v_readlane_b32 s1, v8, s74\n\t"
    "s_nop 0\n\t"
    "s_mul_i32 s1, s1, 0x9e3779b1\n\t"
    "s_add_u32 s1, s1, 0x7f4a7c15\n\t"
    "s_waitcnt lgkmcnt(5)\n\t"
    "v_mfma_f32_16x16x32_bf16 v[48:51], v[28:31], v[208:211], v[48:51]\n\t"
    "ds_read_b64_tr_b16 v[26:27], v1 offset:2048\n\t"
    "s_add_u32 m0, s36, 40960\n\t"
    "s_add_u32 s85, s41, 0x24000\n\t"
    "s_add_u32 s85, s85, s100\n\t"
    "buffer_load_dwordx4 v5, s[24:27], s85 offen lds\n\t"
    "v_mfma_f32_16x16x32_bf16 v[52:55], v[28:31], v[212:215], v[52:55]\n\t"
    "v_add_u32 v3, 0xffffc000, v3\n\t"
    "ds_read_b64_tr_b16 v[28:29], v3 offset:0\n\t"
    "s_setprio 0\n\t"
    "s_waitcnt lgkmcnt(5)\n\t"
    "v_mfma_f32_16x16x32_bf16 v[56:59], v[32:35], v[208:211], v[56:59]\n\t"
    "ds_read_b64_tr_b16 v[30:31], v3 offset:2048\n\t"
    "v_mfma_f32_16x16x32_bf16 v[60:63], v[32:35], v[212:215], v[60:63]\n\t"
    "v_add_u32 v0, 0xffffc000, v0\n\t"
    "ds_read_b64_tr_b16 v[32:33], v0 offset:0\n\t"
    "s_add_u32 s85, s44, 0x12000\n\t"
    "buffer_load_dwordx4 v[232:235], v6, s[28:31], s85 offen sc1\n\t"
    "s_waitcnt lgkmcnt(5)\n\t"
    "v_mfma_f32_16x16x32_bf16 v[64:67], v[36:39], v[208:211], v[64:67]\n\t"
    "ds_read_b64_tr_b16 v[34:35], v0 offset:2048\n\t"
    "v_mfma_f32_16x16x32_bf16 v[68:71], v[36:39], v[212:215], v[68:71]\n\t"
    "v_add_u32 v2, 0xffffc000, v2\n\t"
    "ds_read_b64_tr_b16 v[36:37], v2 offset:0\n\t"
    "s_add_u32 s85, s44, 0x12000\n\t"
    "buffer_load_dwordx4 v[236:239], v6, s[28:31], s85 offen offset:1024 sc1\n\t"
    "s_barrier\n\t"
    "s_setprio 3\n\t"
    "s_waitcnt lgkmcnt(5)\n\t"
    "v_mfma_f32_16x16x32_bf16 v[72:75], v[24:27], v[208:211], v[72:75]\n\t"
    "s_add_u32 s85, s44, 0x12000\n\t"
    "buffer_load_dword v21, v7, s[28:31], s85 offen offset:2048 sc1\n\t"
    "ds_read_b64_tr_b16 v[38:39], v2 offset:2048\n\t"
    "v_mfma_f32_16x16x32_bf16 v[76:79], v[24:27], v[212:215], v[76:79]\n\t"
    "s_xor_b32 s74, s91, 7\n\t"
    "v_readlane_b32 s2, v8, s74\n\t"
    "s_nop 0\n\t"
    "s_mul_i32 s2, s2, 0x9e3779b1\n\t"
    "s_add_u32 s2, s2, 0x7f4a7c15\n\t"
    "v_add_u32 v1, 0x4000, v1\n\t"
    "ds_read_b64_tr_b16 v[24:25], v1 offset:57344\n\t"
    "s_waitcnt lgkmcnt(5)\n\t"
    "v_mfma_f32_16x16x32_bf16 v[80:83], v[28:31], v[208:211], v[80:83]\n\t"
    "s_add_u32 m0, s36, 122880\n\t"
    "s_add_u32 s85, s39, 0x36000\n\t"
    "s_add_u32 s85, s85, s100\n\t"
    "buffer_load_dwordx4 v5, s[24:27], s85 offen lds\n\t"
    "ds_read_b64_tr_b16 v[26:27], v1 offset:59392\n\t"
    "v_mfma_f32_16x16x32_bf16 v[84:87], v[28:31], v[212:215], v[84:87]\n\t"
    "v_add_u32 v3, 0x4000, v3\n\t"
    "ds_read_b64_tr_b16 v[28:29], v3 offset:57344\n\t"
    "ds_read_b64_tr_b16 v[30:31], v3 offset:59392\n\t"
    "s_waitcnt lgkmcnt(6)\n\t"
    "v_mfma_f32_16x16x32_bf16 v[88:91], v[32:35], v[208:211], v[88:91]\n\t"
    "v_mfma_f32_16x16x32_bf16 v[92:95], v[32:35], v[212:215], v[92:95]\n\t"
    "s_waitcnt vmcnt(11)\n\t"
    "v_add3_u32 v19, v216, v217, v19\n\t"
    "v_add3_u32 v19, v218, v219, v19\n\t"
    "s_waitcnt vmcnt(9)\n\t"
    "v_add3_u32 v19, v220, v221, v19\n\t"
    "v_add3_u32 v19, v222, v223, v19\n\t"
    "v_cmp_ne_u32 vcc, s50, v19\n\t"
    "s_cbranch_vccnz LB23_%=\n\t"
    "LB24_%=:\n\t"
    "v_add_u32 v0, 0x4000, v0\n\t"
    "ds_read_b64_tr_b16 v[32:33], v0 offset:57344\n\t"
    "s_waitcnt lgkmcnt(5)\n\t"
    "v_mfma_f32_16x16x32_bf16 v[96:99], v[36:39], v[208:211], v[96:99]\n\t"
    "s_add_u32 s85, s45, 0x12000\n\t"
    "buffer_load_dwordx4 v[240:243], v6, s[28:31], s85 offen sc1\n\t"
    "LIPB3x0_%=:\n\t"
    "s_cbranch_execz LIPRB_%=\n\t"
    "ds_read_b64_tr_b16 v[34:35], v0 offset:59392\n\t"
    "v_mfma_f32_16x16x32_bf16 v[100:103], v[36:39], v[212:215], v[100:103]\n\t"
    "v_add_u32 v2, 0x4000, v2\n\t"
    "ds_read_b64_tr_b16 v[36:37], v2 offset:57344\n\t"
    "s_waitcnt lgkmcnt(5)\n\t"
    "v_mfma_f32_16x16x32_bf16 v[40:43], v[24:27], v[216:219], v[40:43]\n\t"
    "ds_read_b64_tr_b16 v[38:39], v2 offset:59392\n\t"
    "s_add_u32 s85, s45, 0x12000\n\t"
    "buffer_load_dword v22, v7, s[28:31], s85 offen offset:2048 sc1\n\t"
    "v_mfma_f32_16x16x32_bf16 v[44:47], v[24:27], v[220:223], v[44:47]\n\t"
    "ds_read_b64_tr_b16 v[24:25], v1 offset:49152\n\t"
    "ds_read_b64_tr_b16 v[26:27], v1 offset:51200\n\t"
    "s_waitcnt lgkmcnt(6)\n\t"
    "v_mfma_f32_16x16x32_bf16 v[48:51], v[28:31], v[216:219], v[48:51]\n\t"
    "s_add_u32 m0, s36, 114688\n\t"
    "s_add_u32 s85, s40, 0x36000\n\t"
    "s_add_u32 s85, s85, s100\n\t"
    "buffer_load_dwordx4 v5, s[24:27], s85 offen lds\n\t"
    "v_mfma_f32_16x16x32_bf16 v[52:55], v[28:31], v[220:223], v[52:55]\n\t"
    "ds_read_b64_tr_b16 v[28:29], v3 offset:49152\n\t"
    "ds_read_b64_tr_b16 v[30:31], v3 offset:51200\n\t"
    "s_setprio 2\n\t"
    "s_waitcnt lgkmcnt(6)\n\t"
    "v_mfma_f32_16x16x32_bf16 v[56:59], v[32:35], v[216:219], v[56:59]\n\t"
    "v_mfma_f32_16x16x32_bf16 v[60:63], v[32:35], v[220:223], v[60:63]\n\t"
    "ds_read_b64_tr_b16 v[32:33], v0 offset:49152\n\t"
    "s_waitcnt lgkmcnt(5)\n\t"
    "v_mfma_f32_16x16x32_bf16 v[64:67], v[36:39], v[216:219], v[64:67]\n\t"
    "ds_read_b64_tr_b16 v[34:35], v0 offset:51200\n\t"
    "s_add_u32 s85, s45, 0x12000\n\t"
    "buffer_load_dwordx4 v[244:247], v6, s[28:31], s85 offen offset:1024 sc1\n\t"
    "v_mfma_f32_16x16x32_bf16 v[68:71], v[36:39], v[220:223], v[68:71]\n\t"
    "ds_read_b64_tr_b16 v[36:37], v2 offset:49152\n\t"
    "s_setprio 1\n\t"
    "s_waitcnt lgkmcnt(5)\n\t"
    "v_mfma_f32_16x16x32_bf16 v[72:75], v[24:27], v[216:219], v[72:75]\n\t"
    "ds_read_b64_tr_b16 v[38:39], v2 offset:51200\n\t"
    "v_mfma_f32_16x16x32_bf16 v[76:79], v[24:27], v[220:223], v[76:79]\n\t"
    "ds_read_b64_tr_b16 v[24:25], v1 offset:8192\n\t"
    "ds_read_b64_tr_b16 v[26:27], v1 offset:10240\n\t"
    "s_waitcnt lgkmcnt(6)\n\t"
    "v_mfma_f32_16x16x32_bf16 v[80:83], v[28:31], v[216:219], v[80:83]\n\t"
    "s_add_u32 m0, s36, 32768\n\t"
    "s_add_u32 s85, s41, 0x36000\n\t"
    "s_add_u32 s85, s85, s100\n\t"
    "buffer_load_dwordx4 v5, s[24:27], s85 offen lds\n\t"
    "v_mfma_f32_16x16x32_bf16 v[84:87], v[28:31], v[220:223], v[84:87]\n\t"
    "ds_read_b64_tr_b16 v[28:29], v3 offset:8192\n\t"
    "ds_read_b64_tr_b16 v[30:31], v3 offset:10240\n\t"
    "s_setprio 0\n\t"
    "s_waitcnt lgkmcnt(6)\n\t"
    "v_mfma_f32_16x16x32_bf16 v[88:91], v[32:35], v[216:219], v[88:91]\n\t"
    "v_mfma_f32_16x16x32_bf16 v[92:95], v[32:35], v[220:223], v[92:95]\n\t"
    "s_waitcnt vmcnt(10)\n\t"
    "v_add3_u32 v20, v224, v225, v20\n\t"
    "v_add3_u32 v20, v226, v227, v20\n\t"
    "v_add3_u32 v20, v228, v229, v20\n\t"
    "v_add3_u32 v20, v230, v231, v20\n\t"
    "v_cmp_ne_u32 vcc, s48, v20\n\t"
    "s_cbranch_vccnz LB25_%=\n\t"
    "LB26_%=:\n\t"
    "ds_read_b64_tr_b16 v[32:33], v0 offset:8192\n\t"
    "s_waitcnt lgkmcnt(5)\n\t"
    "v_mfma_f32_16x16x32_bf16 v[96:99], v[36:39], v[216:219], v[96:99]\n\t"
    "ds_read_b64_tr_b16 v[34:35], v0 offset:10240\n\t"
    "v_mfma_f32_16x16x32_bf16 v[100:103], v[36:39], v[220:223], v[100:103]\n\t"
    "ds_read_b64_tr_b16 v[36:37], v2 offset:8192\n\t"
    "s_barrier\n\t"
    "s_setprio 3\n\t"
    "s_waitcnt lgkmcnt(5)\n\t"
    "v_mfma_f32_16x16x32_bf16 v[40:43], v[24:27], v[224:227], v[40:43]\n\t"
    "ds_read_b64_tr_b16 v[38:39], v2 offset:10240\n\t"
    "v_mfma_f32_16x16x32_bf16 v[44:47], v[24:27], v[228:231], v[44:47]\n\t"
    "ds_read_b64_tr_b16 v[24:25], v1 offset:40960\n\t"
    "s_waitcnt lgkmcnt(5)\n\t"
    "v_mfma_f32_16x16x32_bf16 v[48:51], v[28:31], v[224:227], v[48:51]\n\t"
    "ds_read_b64_tr_b16 v[26:27], v1 offset:43008\n\t"
    "s_add_u32 m0, s36, 81920\n\t"
    "s_add_u32 s85, s39, 0x48000\n\t"
    "s_add_u32 s85, s85, s100\n\t"
    "buffer_load_dwordx4 v5, s[24:27], s85 offen lds\n\t"
    "v_mfma_f32_16x16x32_bf16 v[52:55], v[28:31], v[228:231], v[52:55]\n\t"
    "ds_read_b64_tr_b16 v[28:29], v3 offset:40960\n\t"
    "s_waitcnt lgkmcnt(5)\n\t"
    "v_mfma_f32_16x16x32_bf16 v[56:59], v[32:35], v[224:227], v[56:59]\n\t"
    "ds_read_b64_tr_b16 v[30:31], v3 offset:43008\n\t"
    "v_mfma_f32_16x16x32_bf16 v[60:63], v[32:35], v[228:231], v[60:63]\n\t"
    "ds_read_b64_tr_b16 v[32:33], v0 offset:40960\n\t"
    "s_waitcnt vmcnt(7)\n\t"
    "v_add3_u32 v21, v232, v233, v21\n\t"
    "v_add3_u32 v21, v234, v235, v21\n\t"
    "v_add3_u32 v21, v236, v237, v21\n\t"
    "v_add3_u32 v21, v238, v239, v21\n\t"
    "v_cmp_ne_u32 vcc, s49, v21\n\t"
    "s_cbranch_vccnz LB27_%=\n\t"
    "LB28_%=:\n\t"
    "s_waitcnt lgkmcnt(5)\n\t"
    "v_mfma_f32_16x16x32_bf16 v[64:67], v[36:39], v[224:227], v[64:67]\n\t"
    "ds_read_b64_tr_b16 v[34:35], v0 offset:43008\n\t"
    "v_mfma_f32_16x16x32_bf16 v[68:71], v[36:39], v[228:231], v[68:71]\n\t"
    "ds_read_b64_tr_b16 v[36:37], v2 offset:40960\n\t"
    "s_waitcnt lgkmcnt(5)\n\t"
    "v_mfma_f32_16x16x32_bf16 v[40:43], v[24:27], v[232:235], v[40:43]\n\t"
    "ds_read_b64_tr_b16 v[38:39], v2 offset:43008\n\t"
    "v_mfma_f32_16x16x32_bf16 v[44:47], v[24:27], v[236:239], v[44:47]\n\t"
    "ds_read_b64_tr_b16 v[24:25], v1 offset:32768\n\t"
    "s_waitcnt lgkmcnt(5)\n\t"
    "v_mfma_f32_16x16x32_bf16 v[48:51], v[28:31], v[232:235], v[48:51]\n\t"
    "ds_read_b64_tr_b16 v[26:27], v1 offset:34816\n\t"
    "v_mfma_f32_16x16x32_bf16 v[52:55], v[28:31], v[236:239], v[52:55]\n\t"
    "ds_read_b64_tr_b16 v[28:29], v3 offset:32768\n\t"
    "s_setprio 2\n\t"
    "s_waitcnt lgkmcnt(5)\n\t"
    "v_mfma_f32_16x16x32_bf16 v[56:59], v[32:35], v[232:235], v[56:59]\n\t"
    "ds_read_b64_tr_b16 v[30:31], v3 offset:34816\n\t"
    "v_mfma_f32_16x16x32_bf16 v[60:63], v[32:35], v[236:239], v[60:63]\n\t"
    "ds_read_b64_tr_b16 v[32:33], v0 offset:32768\n\t"
    "s_waitcnt vmcnt(4)\n\t"
    "v_add3_u32 v22, v240, v241, v22\n\t"
    "v_add3_u32 v22, v242, v243, v22\n\t"
    "s_waitcnt vmcnt(2)\n\t"
    "v_add3_u32 v22, v244, v245, v22\n\t"
    "v_add3_u32 v22, v246, v247, v22\n\t"
    "v_cmp_ne_u32 vcc, s50, v22\n\t"
    "s_cbranch_vccnz LB29_%=\n\t"
    "LB30_%=:\n\t"
    "s_waitcnt lgkmcnt(5)\n\t"
    "v_mfma_f32_16x16x32_bf16 v[64:67], v[36:39], v[232:235], v[64:67]\n\t"
    "ds_read_b64_tr_b16 v[34:35], v0 offset:34816\n\t"
    "v_mfma_f32_16x16x32_bf16 v[68:71], v[36:39], v[236:239], v[68:71]\n\t"
    "ds_read_b64_tr_b16 v[36:37], v2 offset:32768\n\t"
    "s_setprio 1\n\t"
    "s_waitcnt lgkmcnt(5)\n\t"
    "v_mfma_f32_16x16x32_bf16 v[40:43], v[24:27], v[240:243], v[40:43]\n\t"
    "ds_read_b64_tr_b16 v[38:39], v2 offset:34816\n\t"
    "v_mfma_f32_16x16x32_bf16 v[44:47], v[24:27], v[244:247], v[44:47]\n\t"
    "v_add_u32 v1, 0xffffa000, v1\n\t"
    "ds_read_b64_tr_b16 v[24:25], v1 offset:0\n\t"
    "s_lshl_b32 s84, s89, 11\n\t"
    "s_add_u32 s84, s84, 98304\n\t"
    "v_add_u32 v4, s84, v6\n\t"
    "v_xor_b32 v8, 0x2000, v4\n\t"
    "v_mov_b32 v11, 0xffffff00\n\t"
    "s_waitcnt lgkmcnt(5)\n\t"
    "v_mfma_f32_16x16x32_bf16 v[48:51], v[28:31], v[240:243], v[48:51]\n\t"
    "ds_read_b64_tr_b16 v[26:27], v1 offset:2048\n\t"
    "v_mfma_f32_16x16x32_bf16 v[52:55], v[28:31], v[244:247], v[52:55]\n\t"
    "v_add_u32 v3, 0xffffa000, v3\n\t"
    "ds_read_b64_tr_b16 v[28:29], v3 offset:0\n\t"
    "s_setprio 0\n\t"
    "s_waitcnt lgkmcnt(5)\n\t"
    "v_mfma_f32_16x16x32_bf16 v[56:59], v[32:35], v[240:243], v[56:59]\n\t"
    "ds_read_b64_tr_b16 v[30:31], v3 offset:2048\n\t"
    "v_mfma_f32_16x16x32_bf16 v[60:63], v[32:35], v[244:247], v[60:63]\n\t"
    "v_add_u32 v0, 0xffffa000, v0\n\t"
    "ds_read_b64_tr_b16 v[32:33], v0 offset:0\n\t"
    "s_waitcnt lgkmcnt(5)\n\t"
    "v_mfma_f32_16x16x32_bf16 v[64:67], v[36:39], v[240:243], v[64:67]\n\t"
    "ds_read_b64_tr_b16 v[34:35], v0 offset:2048\n\t"
    "v_mfma_f32_16x16x32_bf16 v[68:71], v[36:39], v[244:247], v[68:71]\n\t"
    "v_add_u32 v2, 0xffffa000, v2\n\t"
    "ds_read_b64_tr_b16 v[36:37], v2 offset:0\n\t"
    "s_barrier\n\t"
    "s_setprio 1\n\t"
    "s_waitcnt lgkmcnt(5)\n\t"
    "v_mfma_f32_16x16x32_bf16 v[72:75], v[24:27], v[224:227], v[72:75]\n\t"
    "ds_read_b64_tr_b16 v[38:39], v2 offset:2048\n\t"
    "v_mfma_f32_16x16x32_bf16 v[76:79], v[24:27], v[228:231], v[76:79]\n\t"
    "v_add_u32 v1, 0xffffe000, v1\n\t"
    "ds_read_b64_tr_b16 v[24:25], v1 offset:0\n\t"
    "ds_read_b64_tr_b16 v[26:27], v1 offset:2048\n\t"
    "ds_write_b128 v4, v[40:43] offset:49152\n\t"
    "ds_write_b128 v4, v[48:51] offset:32768\n\t"
    "s_waitcnt lgkmcnt(8)\n\t"
    "v_mfma_f32_16x16x32_bf16 v[80:83], v[28:31], v[224:227], v[80:83]\n\t"
    "s_add_u32 m0, s36, 106496\n\t"
    "s_add_u32 s85, s40, 0x48000\n\t"
    "s_add_u32 s85, s85, s100\n\t"
    "buffer_load_dwordx4 v5, s[24:27], s85 offen lds\n\t"
    "v_mfma_f32_16x16x32_bf16 v[84:87], v[28:31], v[228:231], v[84:87]\n\t"
    "v_add_u32 v3, 0xffffe000, v3\n\t"
    "ds_read_b64_tr_b16 v[28:29], v3 offset:0\n\t"
    "ds_write_b128 v4, v[44:47] offset:50176\n\t"
    "ds_write_b128 v4, v[52:55] offset:33792\n\t"
    "s_setprio 0\n\t"
    "s_waitcnt lgkmcnt(9)\n\t"
    "v_mfma_f32_16x16x32_bf16 v[88:91], v[32:35], v[224:227], v[88:91]\n\t"
    "ds_read_b64_tr_b16 v[30:31], v3 offset:2048\n\t"
    "v_mfma_f32_16x16x32_bf16 v[92:95], v[32:35], v[228:231], v[92:95]\n\t"
    "v_add_u32 v0, 0xffffe000, v0\n\t"
    "ds_read_b64_tr_b16 v[32:33], v0 offset:0\n\t"
    "s_waitcnt lgkmcnt(9)\n\t"
    "v_mfma_f32_16x16x32_bf16 v[96:99], v[36:39], v[224:227], v[96:99]\n\t"
    "ds_read_b64_tr_b16 v[34:35], v0 offset:2048\n\t"
    "v_mfma_f32_16x16x32_bf16 v[100:103], v[36:39], v[228:231], v[100:103]\n\t"
    "v_add_u32 v2, 0xffffe000, v2\n\t"
    "ds_read_b64_tr_b16 v[36:37], v2 offset:0\n\t"
    "s_waitcnt lgkmcnt(4)\n\t"
    "s_barrier\n\t"
    "s_setprio 3\n\t"
    "v_mfma_f32_16x16x32_bf16 v[72:75], v[24:27], v[232:235], v[72:75]\n\t"
    "ds_read_b64_tr_b16 v[38:39], v2 offset:2048\n\t"
    "ds_read_b128 v[40:43], v8 offset:49152\n\t"
    "ds_read_b128 v[48:51], v8 offset:32768\n\t"
    "v_mfma_f32_16x16x32_bf16 v[76:79], v[24:27], v[236:239], v[76:79]\n\t"
    "v_add_u32 v1, 0xffffe000, v1\n\t"
    "ds_read_b64_tr_b16 v[24:25], v1 offset:0\n\t"
    "ds_read_b128 v[44:47], v8 offset:50176\n\t"
    "ds_read_b128 v[52:55], v8 offset:33792\n\t"
    "s_waitcnt lgkmcnt(9)\n\t"
    "v_mfma_f32_16x16x32_bf16 v[80:83], v[28:31], v[232:235], v[80:83]\n\t"
    "s_add_u32 m0, s36, 73728\n\t"
    "s_add_u32 s85, s41, 0x48000\n\t"
    "s_add_u32 s85, s85, s100\n\t"
    "buffer_load_dwordx4 v5, s[24:27], s85 offen lds\n\t"
    "s_waitcnt lgkmcnt(4)\n\t"
    "v_add_f32 v56, v56, v40\n\t"
    "v_add_f32 v57, v57, v41\n\t"
    "v_add_f32 v58, v58, v42\n\t"
    "v_add_f32 v59, v59, v43\n\t"
    "ds_read_b64_tr_b16 v[26:27], v1 offset:2048\n\t"
    "v_mfma_f32_16x16x32_bf16 v[84:87], v[28:31], v[236:239], v[84:87]\n\t"
    "v_add_u32 v3, 0xffffe000, v3\n\t"
    "ds_read_b64_tr_b16 v[28:29], v3 offset:0\n\t"
    "ds_read_b64_tr_b16 v[30:31], v3 offset:2048\n\t"
    "s_setprio 2\n\t"
    "v_mfma_f32_16x16x32_bf16 v[88:91], v[32:35], v[232:235], v[88:91]\n\t"
    "s_waitcnt lgkmcnt(4)\n\t"
    "v_add_f32 v60, v60, v44\n\t"
    "v_add_f32 v61, v61, v45\n\t"
    "v_add_f32 v62, v62, v46\n\t"
    "v_add_f32 v63, v63, v47\n\t"
    "v_and_or_b32 v60, v60, v11, s57\n\t"
    "v_and_or_b32 v61, v61, v11, s58\n\t"
    "v_and_or_b32 v62, v62, v11, s59\n\t"
    "v_and_or_b32 v63, v63, v11, s60\n\t"
    "buffer_store_dwordx4 v[60:63], v6, s[28:31], s70 offen offset:2048 sc1\n\t"
    "v_mfma_f32_16x16x32_bf16 v[92:95], v[32:35], v[236:239], v[92:95]\n\t"
    "v_add_u32 v0, 0xffffe000, v0\n\t"
    "ds_read_b64_tr_b16 v[32:33], v0 offset:0\n\t"
    "ds_read_b64_tr_b16 v[34:35], v0 offset:2048\n\t"
    "v_mfma_f32_16x16x32_bf16 v[96:99], v[36:39], v[232:235], v[96:99]\n\t"
    "v_add_f32 v64, v64, v48\n\t"
    "v_add_f32 v65, v65, v49\n\t"
    "v_add_f32 v66, v66, v50\n\t"
    "v_add_f32 v67, v67, v51\n\t"
    "v_and_or_b32 v64, v64, v11, s57\n\t"
    "v_and_or_b32 v65, v65, v11, s58\n\t"
    "v_and_or_b32 v66, v66, v11, s59\n\t"
    "v_and_or_b32 v67, v67, v11, s60\n\t"
    "buffer_store_dwordx4 v[64:67], v6, s[28:31], s69 offen offset:2048 sc1\n\t"
    "v_mfma_f32_16x16x32_bf16 v[100:103], v[36:39], v[236:239], v[100:103]\n\t"
    "v_add_u32 v2, 0xffffe000, v2\n\t"
    "ds_read_b64_tr_b16 v[36:37], v2 offset:0\n\t"
    "ds_read_b64_tr_b16 v[38:39], v2 offset:2048\n\t"
    "s_setprio 1\n\t"
    "s_waitcnt lgkmcnt(6)\n\t"
    "v_mfma_f32_16x16x32_bf16 v[72:75], v[24:27], v[240:243], v[72:75]\n\t"
    "v_add_f32 v68, v68, v52\n\t"
    "v_add_f32 v69, v69, v53\n\t"
    "v_add_f32 v70, v70, v54\n\t"
    "v_add_f32 v71, v71, v55\n\t"
    "v_and_or_b32 v68, v68, v11, s57\n\t"
    "v_and_or_b32 v69, v69, v11, s58\n\t"
    "v_and_or_b32 v70, v70, v11, s59\n\t"
    "v_and_or_b32 v71, v71, v11, s60\n\t"
    "buffer_store_dwordx4 v[68:71], v6, s[28:31], s71 offen offset:2048 sc1\n\t"
    "v_mfma_f32_16x16x32_bf16 v[76:79], v[24:27], v[244:247], v[76:79]\n\t"
    "v_add_u32 v3, 0xffff8000, v3\n\t"
    "ds_read_b64_tr_b16 v[24:25], v3 offset:0\n\t"
    "s_waitcnt lgkmcnt(5)\n\t"
    "v_mfma_f32_16x16x32_bf16 v[80:83], v[28:31], v[240:243], v[80:83]\n\t"
    "ds_read_b64_tr_b16 v[26:27], v3 offset:2048\n\t"
    "v_mfma_f32_16x16x32_bf16 v[84:87], v[28:31], v[244:247], v[84:87]\n\t"
    "v_add_u32 v2, 0xffff8000, v2\n\t"
    "ds_read_b64_tr_b16 v[28:29], v2 offset:0\n\t"
    "s_setprio 0\n\t"
    "s_waitcnt lgkmcnt(5)\n\t"
    "v_mfma_f32_16x16x32_bf16 v[88:91], v[32:35], v[240:243], v[88:91]\n\t"
    "ds_read_b64_tr_b16 v[30:31], v2 offset:2048\n\t"
    "v_mfma_f32_16x16x32_bf16 v[92:95], v[32:35], v[244:247], v[92:95]\n\t"
    "v_add_u32 v3, 0xffffe000, v3\n\t"
    "ds_read_b64_tr_b16 v[32:33], v3 offset:0\n\t"
    "s_waitcnt lgkmcnt(5)\n\t"
    "v_mfma_f32_16x16x32_bf16 v[96:99], v[36:39], v[240:243], v[96:99]\n\t"
    "ds_read_b64_tr_b16 v[34:35], v3 offset:2048\n\t"
    "v_mfma_f32_16x16x32_bf16 v[100:103], v[36:39], v[244:247], v[100:103]\n\t"
    "v_add_u32 v2, 0xffffe000, v2\n\t"
    "ds_read_b64_tr_b16 v[36:37], v2 offset:0\n\t"
    "s_barrier\n\t"
    "s_waitcnt lgkmcnt(5)\n\t"
    "v_mfma_f32_16x16x32_bf16 v[120:123], v[24:27], v[144:147], v[120:123]\n\t"
    "ds_read_b64_tr_b16 v[38:39], v2 offset:2048\n\t"
    "v_mfma_f32_16x16x32_bf16 v[124:127], v[24:27], v[148:151], v[124:127]\n\t"
    "v_add_u32 v3, 0xffffe000, v3\n\t"
    "ds_read_b64_tr_b16 v[24:25], v3 offset:0\n\t"
    "ds_write_b128 v4, v[72:75] offset:49152\n\t"
    "ds_write_b128 v4, v[80:83] offset:32768\n\t"
    "s_waitcnt lgkmcnt(7)\n\t"
    "v_mfma_f32_16x16x32_bf16 v[136:139], v[28:31], v[144:147], v[136:139]\n\t"
    "ds_read_b64_tr_b16 v[26:27], v3 offset:2048\n\t"
    "v_mfma_f32_16x16x32_bf16 v[140:143], v[28:31], v[148:151], v[140:143]\n\t"
    "v_add_u32 v2, 0xffffe000, v2\n\t"
    "ds_read_b64_tr_b16 v[28:29], v2 offset:0\n\t"
    "ds_write_b128 v4, v[76:79] offset:50176\n\t"
    "ds_write_b128 v4, v[84:87] offset:33792\n\t"
    "s_waitcnt lgkmcnt(9)\n\t"
    "v_mfma_f32_16x16x32_bf16 v[120:123], v[32:35], v[152:155], v[120:123]\n\t"
    "ds_read_b64_tr_b16 v[30:31], v2 offset:2048\n\t"
    "v_mfma_f32_16x16x32_bf16 v[124:127], v[32:35], v[156:159], v[124:127]\n\t"
    "v_add_u32 v3, 0xffffe000, v3\n\t"
    "ds_read_b64_tr_b16 v[32:33], v3 offset:0\n\t"
    "s_waitcnt lgkmcnt(9)\n\t"
    "v_mfma_f32_16x16x32_bf16 v[136:139], v[36:39], v[152:155], v[136:139]\n\t"
    "ds_read_b64_tr_b16 v[34:35], v3 offset:2048\n\t"
    "v_mfma_f32_16x16x32_bf16 v[140:143], v[36:39], v[156:159], v[140:143]\n\t"
    "v_add_u32 v2, 0xffffe000, v2\n\t"
    "ds_read_b64_tr_b16 v[36:37], v2 offset:0\n\t"
    "s_waitcnt lgkmcnt(4)\n\t"
    "s_barrier\n\t"
    "s_setprio 3\n\t"
    "v_mfma_f32_16x16x32_bf16 v[120:123], v[24:27], v[160:163], v[120:123]\n\t"
    "ds_read_b64_tr_b16 v[38:39], v2 offset:2048\n\t"
    "ds_read_b128 v[72:75], v8 offset:49152\n\t"
    "ds_read_b128 v[80:83], v8 offset:32768\n\t"
    "v_mfma_f32_16x16x32_bf16 v[124:127], v[24:27], v[164:167], v[124:127]\n\t"
    "ds_read_b64_tr_b16 v[24:25], v3 offset:49152\n\t"
    "ds_read_b128 v[76:79], v8 offset:50176\n\t"
    "ds_read_b128 v[84:87], v8 offset:33792\n\t"
    "s_waitcnt lgkmcnt(9)\n\t"
    "v_mfma_f32_16x16x32_bf16 v[136:139], v[28:31], v[160:163], v[136:139]\n\t"
    "ds_read_b64_tr_b16 v[26:27], v3 offset:51200\n\t"
    "s_waitcnt lgkmcnt(5)\n\t"
    "v_add_f32 v88, v88, v72\n\t"
    "v_add_f32 v89, v89, v73\n\t"
    "v_add_f32 v90, v90, v74\n\t"
    "v_add_f32 v91, v91, v75\n\t"
    "v_mfma_f32_16x16x32_bf16 v[140:143], v[28:31], v[164:167], v[140:143]\n\t"
    "ds_read_b64_tr_b16 v[28:29], v2 offset:49152\n\t"
    "s_waitcnt lgkmcnt(3)\n\t"
    "v_add_f32 v92, v92, v76\n\t"
    "v_add_f32 v93, v93, v77\n\t"
    "v_add_f32 v94, v94, v78\n\t"
    "v_add_f32 v95, v95, v79\n\t"
    "v_mfma_f32_16x16x32_bf16 v[120:123], v[32:35], v[168:171], v[120:123]\n\t"
    "v_maximum3_f32 v72, |v88|, |v89|, |v90|\n\t"
    "v_maximum3_f32 v73, |v91|, |v92|, |v93|\n\t"
    "v_maximum3_f32 v72, v72, |v94|, |v95|\n\t"
    "v_maximum3_f32 v72, v72, v73, v73\n\t"
    "v_bfe_u32 v73, v72, 23, 8\n\t"
    "ds_read_b64_tr_b16 v[30:31], v2 offset:51200\n\t"
    "v_mfma_f32_16x16x32_bf16 v[124:127], v[32:35], v[172:175], v[124:127]\n\t"
    "v_add_u32 v3, 0x18000, v3\n\t"
    "ds_read_b64_tr_b16 v[32:33], v3 offset:0\n\t"
    "v_sub_u32 v72, 0x7e, v73\n\t"
    "v_ldexp_f32 v88, v88, v72\n\t"
    "v_ldexp_f32 v89, v89, v72\n\t"
    "v_cvt_pknorm_i16_f32 v88, v88, v89\n\t"
    "v_ldexp_f32 v90, v90, v72\n\t"
    "v_mfma_f32_16x16x32_bf16 v[136:139], v[36:39], v[168:171], v[136:139]\n\t"
    "ds_read_b64_tr_b16 v[34:35], v3 offset:2048\n\t"
    "v_ldexp_f32 v91, v91, v72\n\t"
    "v_cvt_pknorm_i16_f32 v89, v90, v91\n\t"
    "v_ldexp_f32 v92, v92, v72\n\t"
    "v_ldexp_f32 v93, v93, v72\n\t"
    "v_cvt_pknorm_i16_f32 v90, v92, v93\n\t"
    "v_mfma_f32_16x16x32_bf16 v[140:143], v[36:39], v[172:175], v[140:143]\n\t"
    "v_ldexp_f32 v94, v94, v72\n\t"
    "v_ldexp_f32 v95, v95, v72\n\t"
    "v_cvt_pknorm_i16_f32 v91, v94, v95\n\t"
    "buffer_store_dwordx4 v[88:91], v6, s[28:31], s70 offen sc1\n\t"
    "v_add3_u32 v76, v88, v89, v90\n\t"
    "v_add_u32 v2, 0x18000, v2\n\t"
    "ds_read_b64_tr_b16 v[36:37], v2 offset:0\n\t"
    "s_waitcnt lgkmcnt(5)\n\t"
    "v_mfma_f32_16x16x32_bf16 v[120:123], v[24:27], v[104:107], v[120:123]\n\t"
    "ds_read_b64_tr_b16 v[38:39], v2 offset:2048\n\t"
    "v_add3_u32 v76, v76, v91, s55\n\t"
    "v_bfi_b32 v76, s3, v73, v76\n\t"
    "buffer_store_dword v76, v7, s[28:31], s70 offen offset:1024 sc1\n\t"
    "v_mfma_f32_16x16x32_bf16 v[124:127], v[24:27], v[108:111], v[124:127]\n\t"
    "v_add_u32 v3, 0xffffe000, v3\n\t"
    "ds_read_b64_tr_b16 v[24:25], v3 offset:0\n\t"
    "v_add_f32 v96, v96, v80\n\t"
    "v_add_f32 v97, v97, v81\n\t"
    "v_add_f32 v98, v98, v82\n\t"
    "v_add_f32 v99, v99, v83\n\t"
    "s_waitcnt lgkmcnt(5)\n\t"
    "v_mfma_f32_16x16x32_bf16 v[136:139], v[28:31], v[104:107], v[136:139]\n\t"
    "v_add_f32 v100, v100, v84\n\t"
    "v_add_f32 v101, v101, v85\n\t"
    "v_add_f32 v102, v102, v86\n\t"
    "v_add_f32 v103, v103, v87\n\t"
    "ds_read_b64_tr_b16 v[26:27], v3 offset:2048\n\t"
    "v_mfma_f32_16x16x32_bf16 v[140:143], v[28:31], v[108:111], v[140:143]\n\t"
    "v_add_u32 v2, 0xffffe000, v2\n\t"
    "ds_read_b64_tr_b16 v[28:29], v2 offset:0\n\t"
    "v_maximum3_f32 v80, |v96|, |v97|, |v98|\n\t"
    "v_maximum3_f32 v81, |v99|, |v100|, |v101|\n\t"
    "v_maximum3_f32 v80, v80, |v102|, |v103|\n\t"
    "v_maximum3_f32 v80, v80, v81, v81\n\t"
    "v_bfe_u32 v81, v80, 23, 8\n\t"
    "s_setprio 2\n\t"
    "s_waitcnt lgkmcnt(5)\n\t"
    "v_mfma_f32_16x16x32_bf16 v[120:123], v[32:35], v[176:179], v[120:123]\n\t"
    "v_sub_u32 v80, 0x7e, v81\n\t"
    "v_ldexp_f32 v96, v96, v80\n\t"
    "v_ldexp_f32 v97, v97, v80\n\t"
    "v_cvt_pknorm_i16_f32 v96, v96, v97\n\t"
    "v_ldexp_f32 v98, v98, v80\n\t"
    "ds_read_b64_tr_b16 v[30:31], v2 offset:2048\n\t"
    "v_mfma_f32_16x16x32_bf16 v[124:127], v[32:35], v[180:183], v[124:127]\n\t"
    "v_add_u32 v3, 0xffff4000, v3\n\t"
    "ds_read_b64_tr_b16 v[32:33], v3 offset:0\n\t"
    "v_ldexp_f32 v99, v99, v80\n\t"
    "v_cvt_pknorm_i16_f32 v97, v98, v99\n\t"
    "v_ldexp_f32 v100, v100, v80\n\t"
    "v_ldexp_f32 v101, v101, v80\n\t"
    "v_cvt_pknorm_i16_f32 v98, v100, v101\n\t"
    "s_waitcnt lgkmcnt(5)\n\t"
    "v_mfma_f32_16x16x32_bf16 v[136:139], v[36:39], v[176:179], v[136:139]\n\t"
    "ds_read_b64_tr_b16 v[34:35], v3 offset:2048\n\t"
    "v_ldexp_f32 v102, v102, v80\n\t"
    "v_ldexp_f32 v103, v103, v80\n\t"
    "v_cvt_pknorm_i16_f32 v99, v102, v103\n\t"
    "buffer_store_dwordx4 v[96:99], v6, s[28:31], s71 offen sc1\n\t"
    "v_add3_u32 v84, v96, v97, v98\n\t"
    "v_mfma_f32_16x16x32_bf16 v[140:143], v[36:39], v[180:183], v[140:143]\n\t"
    "v_add_u32 v2, 0xffff4000, v2\n\t"
    "ds_read_b64_tr_b16 v[36:37], v2 offset:0\n\t"
    "ds_read_b64_tr_b16 v[38:39], v2 offset:2048\n\t"
    "v_add3_u32 v84, v84, v99, s55\n\t"
    "v_bfi_b32 v84, s3, v81, v84\n\t"
    "buffer_store_dword v84, v7, s[28:31], s71 offen offset:1024 sc1\n\t"
    "s_setprio 1\n\t"
    "s_waitcnt lgkmcnt(6)\n\t"
    "v_mfma_f32_16x16x32_bf16 v[120:123], v[24:27], v[184:187], v[120:123]\n\t"
    "v_mfma_f32_16x16x32_bf16 v[124:127], v[24:27], v[188:191], v[124:127]\n\t"
    "v_add_u32 v3, 0xe000, v3\n\t"
    "ds_read_b64_tr_b16 v[24:25], v3 offset:24576\n\t"
    "s_waitcnt lgkmcnt(5)\n\t"
    "v_mfma_f32_16x16x32_bf16 v[136:139], v[28:31], v[184:187], v[136:139]\n\t"
    "ds_read_b64_tr_b16 v[26:27], v3 offset:26624\n\t"
    "v_mfma_f32_16x16x32_bf16 v[140:143], v[28:31], v[188:191], v[140:143]\n\t"
    "v_add_u32 v2, 0xe000, v2\n\t"
    "ds_read_b64_tr_b16 v[28:29], v2 offset:24576\n\t"
    "s_setprio 0\n\t"
    "s_waitcnt lgkmcnt(5)\n\t"
    "v_mfma_f32_16x16x32_bf16 v[120:123], v[32:35], v[192:195], v[120:123]\n\t"
    "ds_read_b64_tr_b16 v[30:31], v2 offset:26624\n\t"
    "v_mfma_f32_16x16x32_bf16 v[124:127], v[32:35], v[196:199], v[124:127]\n\t"
    "ds_read_b64_tr_b16 v[32:33], v3 offset:16384\n\t"
    "s_waitcnt lgkmcnt(5)\n\t"
    "v_mfma_f32_16x16x32_bf16 v[136:139], v[36:39], v[192:195], v[136:139]\n\t"
    "ds_read_b64_tr_b16 v[34:35], v3 offset:18432\n\t"
    "v_mfma_f32_16x16x32_bf16 v[140:143], v[36:39], v[196:199], v[140:143]\n\t"
    "ds_read_b64_tr_b16 v[36:37], v2 offset:16384\n\t"
    "s_waitcnt vmcnt(7)\n\t"
    "s_barrier\n\t"
    "s_setprio 3\n\t"
    "s_waitcnt lgkmcnt(5)\n\t"
    "v_mfma_f32_16x16x32_bf16 v[120:123], v[24:27], v[200:203], v[120:123]\n\t"
    "ds_read_b64_tr_b16 v[38:39], v2 offset:18432\n\t"
    "v_mfma_f32_16x16x32_bf16 v[124:127], v[24:27], v[204:207], v[124:127]\n\t"
    "v_add_u32 v3, 0xffff0000, v3\n\t"
    "ds_read_b64_tr_b16 v[24:25], v3 offset:0\n\t"
    "s_add_u32 s85, s64, 0\n\t"
    "buffer_load_dwordx4 v[40:43], v6, s[28:31], s85 offen offset:2048 sc1\n\t"
    "s_waitcnt lgkmcnt(5)\n\t"
    "v_mfma_f32_16x16x32_bf16 v[136:139], v[28:31], v[200:203], v[136:139]\n\t"
    "ds_read_b64_tr_b16 v[26:27], v3 offset:2048\n\t"
    "v_mfma_f32_16x16x32_bf16 v[140:143], v[28:31], v[204:207], v[140:143]\n\t"
    "ds_read_b64_tr_b16 v[28:29], v3 offset:49152\n\t"
    "s_add_u32 s85, s64, 24576\n\t"
    "buffer_load_dwordx4 v[44:47], v6, s[28:31], s85 offen offset:2048 sc1\n\t"
    "s_waitcnt lgkmcnt(5)\n\t"
    "v_mfma_f32_16x16x32_bf16 v[120:123], v[32:35], v[208:211], v[120:123]\n\t"
    "ds_read_b64_tr_b16 v[30:31], v3 offset:51200\n\t"
    "v_mfma_f32_16x16x32_bf16 v[124:127], v[32:35], v[212:215], v[124:127]\n\t"
    "v_add_u32 v3, 0x10000, v3\n\t"
    "ds_read_b64_tr_b16 v[32:33], v3 offset:8192\n\t"
    "s_add_u32 s85, s64, 49152\n\t"
    "buffer_load_dwordx4 v[48:51], v6, s[28:31], s85 offen offset:2048 sc1\n\t"
    "s_waitcnt lgkmcnt(5)\n\t"
    "v_mfma_f32_16x16x32_bf16 v[136:139], v[36:39], v[208:211], v[136:139]\n\t"
    "ds_read_b64_tr_b16 v[34:35], v3 offset:10240\n\t"
    "v_mfma_f32_16x16x32_bf16 v[140:143], v[36:39], v[212:215], v[140:143]\n\t"
    "v_add_u32 v3, 0xffffa000, v3\n\t"
    "ds_read_b64_tr_b16 v[36:37], v3 offset:0\n\t"
    "s_waitcnt lgkmcnt(5)\n\t"
    "v_mfma_f32_16x16x32_bf16 v[120:123], v[24:27], v[216:219], v[120:123]\n\t"
    "ds_read_b64_tr_b16 v[38:39], v3 offset:2048\n\t"
    "v_mfma_f32_16x16x32_bf16 v[124:127], v[24:27], v[220:223], v[124:127]\n\t"
    "v_add_u32 v2, 0xffff0000, v2\n\t"
    "ds_read_b64_tr_b16 v[24:25], v2 offset:0\n\t"
    "s_waitcnt lgkmcnt(5)\n\t"
    "v_mfma_f32_16x16x32_bf16 v[120:123], v[28:31], v[224:227], v[120:123]\n\t"
    "ds_read_b64_tr_b16 v[26:27], v2 offset:2048\n\t"
    "v_mfma_f32_16x16x32_bf16 v[124:127], v[28:31], v[228:231], v[124:127]\n\t"
    "ds_read_b64_tr_b16 v[28:29], v2 offset:49152\n\t"
    "s_setprio 2\n\t"
    "s_waitcnt lgkmcnt(5)\n\t"
    "v_mfma_f32_16x16x32_bf16 v[120:123], v[32:35], v[232:235], v[120:123]\n\t"
    "ds_read_b64_tr_b16 v[30:31], v2 offset:51200\n\t"
    "v_mfma_f32_16x16x32_bf16 v[124:127], v[32:35], v[236:239], v[124:127]\n\t"
    "v_add_u32 v2, 0x10000, v2\n\t"
    "ds_read_b64_tr_b16 v[32:33], v2 offset:8192\n\t"
    "s_waitcnt lgkmcnt(5)\n\t"
    "v_mfma_f32_16x16x32_bf16 v[120:123], v[36:39], v[240:243], v[120:123]\n\t"
    "ds_read_b64_tr_b16 v[34:35], v2 offset:10240\n\t"
    "v_mfma_f32_16x16x32_bf16 v[124:127], v[36:39], v[244:247], v[124:127]\n\t"
    "v_add_u32 v2, 0xffffa000, v2\n\t"
    "ds_read_b64_tr_b16 v[36:37], v2 offset:0\n\t"
    "s_setprio 1\n\t"
    "s_waitcnt lgkmcnt(5)\n\t"
    "v_mfma_f32_16x16x32_bf16 v[136:139], v[24:27], v[216:219], v[136:139]\n\t"
    "ds_read_b64_tr_b16 v[38:39], v2 offset:2048\n\t"
    "v_mfma_f32_16x16x32_bf16 v[140:143], v[24:27], v[220:223], v[140:143]\n\t"
    "v_add_u32 v1, 0xffff8000, v1\n\t"
    "ds_read_b64_tr_b16 v[24:25], v1 offset:0\n\t"
    "ds_write_b128 v4, v[120:123] offset:49152\n\t"
    "s_waitcnt lgkmcnt(6)\n\t"
    "v_mfma_f32_16x16x32_bf16 v[136:139], v[28:31], v[224:227], v[136:139]\n\t"
    "ds_read_b64_tr_b16 v[26:27], v1 offset:2048\n\t"
    "v_mfma_f32_16x16x32_bf16 v[140:143], v[28:31], v[228:231], v[140:143]\n\t"
    "v_add_u32 v1, 0xffffe000, v1\n\t"
    "ds_read_b64_tr_b16 v[28:29], v1 offset:0\n\t"
    "ds_write_b128 v4, v[124:127] offset:50176\n\t"
    "s_setprio 0\n\t"
    "s_waitcnt lgkmcnt(7)\n\t"
    "v_mfma_f32_16x16x32_bf16 v[136:139], v[32:35], v[232:235], v[136:139]\n\t"
    "ds_read_b64_tr_b16 v[30:31], v1 offset:2048\n\t"
    "v_mfma_f32_16x16x32_bf16 v[140:143], v[32:35], v[236:239], v[140:143]\n\t"
    "v_add_u32 v1, 0xffffe000, v1\n\t"
    "ds_read_b64_tr_b16 v[32:33], v1 offset:0\n\t"
    "s_waitcnt lgkmcnt(7)\n\t"
    "v_mfma_f32_16x16x32_bf16 v[136:139], v[36:39], v[240:243], v[136:139]\n\t"
    "ds_read_b64_tr_b16 v[34:35], v1 offset:2048\n\t"
    "v_mfma_f32_16x16x32_bf16 v[140:143], v[36:39], v[244:247], v[140:143]\n\t"
    "v_add_u32 v1, 0xffffe000, v1\n\t"
    "ds_read_b64_tr_b16 v[36:37], v1 offset:0\n\t"
    "ds_read_b64_tr_b16 v[38:39], v1 offset:2048\n\t"
    "s_waitcnt lgkmcnt(5)\n\t"
    "s_barrier\n\t"
    "s_setprio 3\n\t"
    "v_mfma_f32_16x16x32_bf16 v[112:115], v[24:27], v[144:147], v[112:115]\n\t"
    "ds_read_b128 v[120:123], v8 offset:49152\n\t"
    "v_mfma_f32_16x16x32_bf16 v[116:119], v[24:27], v[148:151], v[116:119]\n\t"
    "ds_read_b64_tr_b16 v[24:25], v1 offset:49152\n\t"
    "ds_read_b64_tr_b16 v[26:27], v1 offset:51200\n\t"
    "ds_read_b128 v[124:127], v8 offset:50176\n\t"
    "s_waitcnt lgkmcnt(8)\n\t"
    "v_mfma_f32_16x16x32_bf16 v[112:115], v[28:31], v[152:155], v[112:115]\n\t"
    "s_waitcnt lgkmcnt(3)\n\t"
    "v_add_f32 v136, v136, v120\n\t"
    "v_add_f32 v137, v137, v121\n\t"
    "v_add_f32 v138, v138, v122\n\t"
    "v_add_f32 v139, v139, v123\n\t"
    "v_mfma_f32_16x16x32_bf16 v[116:119], v[28:31], v[156:159], v[116:119]\n\t"
    "v_add_u32 v1, 0x18000, v1\n\t"
    "ds_read_b64_tr_b16 v[28:29], v1 offset:0\n\t"
    "s_waitcnt lgkmcnt(1)\n\t"
    "v_add_f32 v140, v140, v124\n\t"
    "v_add_f32 v141, v141, v125\n\t"
    "v_add_f32 v142, v142, v126\n\t"
    "v_add_f32 v143, v143, v127\n\t"
    "v_mfma_f32_16x16x32_bf16 v[112:115], v[32:35], v[160:163], v[112:115]\n\t"
    "ds_read_b64_tr_b16 v[30:31], v1 offset:2048\n\t"
    "v_maximum3_f32 v120, |v136|, |v137|, |v138|\n\t"
    "v_maximum3_f32 v121, |v139|, |v140|, |v141|\n\t"
    "v_maximum3_f32 v120, v120, |v142|, |v143|\n\t"
    "v_maximum3_f32 v120, v120, v121, v121\n\t"
    "v_bfe_u32 v121, v120, 23, 8\n\t"
    "v_mfma_f32_16x16x32_bf16 v[116:119], v[32:35], v[164:167], v[116:119]\n\t"
    "v_add_u32 v1, 0xffffe000, v1\n\t"
    "ds_read_b64_tr_b16 v[32:33], v1 offset:0\n\t"
    "v_sub_u32 v120, 0x7e, v121\n\t"
    "v_ldexp_f32 v136, v136, v120\n\t"
    "v_ldexp_f32 v137, v137, v120\n\t"
    "v_cvt_pknorm_i16_f32 v136, v136, v137\n\t"
    "v_ldexp_f32 v138, v138, v120\n\t"
    "v_mfma_f32_16x16x32_bf16 v[112:115], v[36:39], v[168:171], v[112:115]\n\t"
    "ds_read_b64_tr_b16 v[34:35], v1 offset:2048\n\t"
    "v_ldexp_f32 v139, v139, v120\n\t"
    "v_cvt_pknorm_i16_f32 v137, v138, v139\n\t"
    "v_ldexp_f32 v140, v140, v120\n\t"
    "v_ldexp_f32 v141, v141, v120\n\t"
    "v_cvt_pknorm_i16_f32 v138, v140, v141\n\t"
    "v_mfma_f32_16x16x32_bf16 v[116:119], v[36:39], v[172:175], v[116:119]\n\t"
    "v_add_u32 v1, 0xffff4000, v1\n\t"
    "LIPB4x0_%=:\n\t"
    "s_cbranch_execz LIPRB_%=\n\t"
    "ds_read_b64_tr_b16 v[36:37], v1 offset:0\n\t"
    "ds_read_b64_tr_b16 v[38:39], v1 offset:2048\n\t"
    "v_ldexp_f32 v142, v142, v120\n\t"
    "v_ldexp_f32 v143, v143, v120\n\t"
    "v_cvt_pknorm_i16_f32 v139, v142, v143\n\t"
    "buffer_store_dwordx4 v[136:139], v6, s[28:31], s69 offen sc1\n\t"
    "v_add3_u32 v124, v136, v137, v138\n\t"
    "v_mfma_f32_16x16x32_bf16 v[112:115], v[24:27], v[104:107], v[112:115]\n\t"
    "v_add3_u32 v124, v124, v139, s55\n\t"
    "v_bfi_b32 v124, s3, v121, v124\n\t"
    "buffer_store_dword v124, v7, s[28:31], s69 offen offset:1024 sc1\n\t"
    "v_mfma_f32_16x16x32_bf16 v[116:119], v[24:27], v[108:111], v[116:119]\n\t"
    "v_add_u32 v1, 0xe000, v1\n\t"
    "ds_read_b64_tr_b16 v[24:25], v1 offset:24576\n\t"
    "s_add_u32 s85, s64, 24576\n\t"
    "buffer_load_dwordx4 v[52:55], v6, s[28:31], s85 offen sc1\n\t"
    "s_waitcnt lgkmcnt(5)\n\t"
    "v_mfma_f32_16x16x32_bf16 v[112:115], v[28:31], v[176:179], v[112:115]\n\t"
    "ds_read_b64_tr_b16 v[26:27], v1 offset:26624\n\t"
    "v_mfma_f32_16x16x32_bf16 v[116:119], v[28:31], v[180:183], v[116:119]\n\t"
    "ds_read_b64_tr_b16 v[28:29], v1 offset:16384\n\t"
    "s_add_u32 s85, s64, 24576\n\t"
    "buffer_load_dword v64, v7, s[28:31], s85 offen offset:1024 sc1\n\t"
    "s_waitcnt lgkmcnt(5)\n\t"
    "v_mfma_f32_16x16x32_bf16 v[112:115], v[32:35], v[184:187], v[112:115]\n\t"
    "ds_read_b64_tr_b16 v[30:31], v1 offset:18432\n\t"
    "v_mfma_f32_16x16x32_bf16 v[116:119], v[32:35], v[188:191], v[116:119]\n\t"
    "v_add_u32 v1, 0xffff0000, v1\n\t"
    "ds_read_b64_tr_b16 v[32:33], v1 offset:0\n\t"
    "s_add_u32 s85, s64, 49152\n\t"
    "buffer_load_dwordx4 v[68:71], v6, s[28:31], s85 offen sc1\n\t"
    "s_waitcnt lgkmcnt(5)\n\t"
    "v_mfma_f32_16x16x32_bf16 v[112:115], v[36:39], v[192:195], v[112:115]\n\t"
    "ds_read_b64_tr_b16 v[34:35], v1 offset:2048\n\t"
    "v_mfma_f32_16x16x32_bf16 v[116:119], v[36:39], v[196:199], v[116:119]\n\t"
    "s_add_u32 s85, s64, 49152\n\t"
    "buffer_load_dword v72, v7, s[28:31], s85 offen offset:1024 sc1\n\t"
    "ds_read_b64_tr_b16 v[36:37], v1 offset:49152\n\t"
    "ds_read_b64_tr_b16 v[38:39], v1 offset:51200\n\t"
    "s_waitcnt lgkmcnt(6)\n\t"
    "v_mfma_f32_16x16x32_bf16 v[112:115], v[24:27], v[200:203], v[112:115]\n\t"
    "s_waitcnt vmcnt(8)\n\t"
    "v_perm_b32 v12, v41, v40, s72\n\t"
    "v_perm_b32 v13, v43, v42, s73\n\t"
    "v_or_b32 v12, v12, v13\n\t"
    "v_cmp_ne_u32 s[74:75], s52, v12\n\t"
    "s_waitcnt vmcnt(7)\n\t"
    "v_perm_b32 v12, v45, v44, s72\n\t"
    "v_perm_b32 v13, v47, v46, s73\n\t"
    "v_or_b32 v12, v12, v13\n\t"
    "v_cmp_ne_u32 s[76:77], s53, v12\n\t"
    "s_waitcnt vmcnt(6)\n\t"
    "v_perm_b32 v12, v49, v48, s72\n\t"
    "v_perm_b32 v13, v51, v50, s73\n\t"
    "v_or_b32 v12, v12, v13\n\t"
    "v_cmp_ne_u32 s[78:79], s54, v12\n\t"
    "s_nop 1\n\t"
    "s_or_b64 s[74:75], s[74:75], s[76:77]\n\t"
    "s_or_b64 s[74:75], s[74:75], s[78:79]\n\t"
    "s_cbranch_scc1 LB31_%=\n\t"
    "LB32_%=:\n\t"
    "v_mfma_f32_16x16x32_bf16 v[116:119], v[24:27], v[204:207], v[116:119]\n\t"
    "v_add_u32 v1, 0x10000, v1\n\t"
    "ds_read_b64_tr_b16 v[24:25], v1 offset:8192\n\t"
    "ds_read_b64_tr_b16 v[26:27], v1 offset:10240\n\t"
    "v_and_b32 v40, v40, v11\n\t"
    "v_and_b32 v41, v41, v11\n\t"
    "v_and_b32 v42, v42, v11\n\t"
    "v_and_b32 v43, v43, v11\n\t"
    "s_waitcnt lgkmcnt(6)\n\t"
    "v_mfma_f32_16x16x32_bf16 v[112:115], v[28:31], v[208:211], v[112:115]\n\t"
    "v_and_b32 v44, v44, v11\n\t"
    "v_and_b32 v45, v45, v11\n\t"
    "v_and_b32 v46, v46, v11\n\t"
    "v_and_b32 v47, v47, v11\n\t"
    "v_mfma_f32_16x16x32_bf16 v[116:119], v[28:31], v[212:215], v[116:119]\n\t"
    "v_add_u32 v1, 0xffffa000, v1\n\t"
    "ds_read_b64_tr_b16 v[28:29], v1 offset:0\n\t"
    "ds_read_b64_tr_b16 v[30:31], v1 offset:2048\n\t"
    "v_and_b32 v48, v48, v11\n\t"
    "v_and_b32 v49, v49, v11\n\t"
    "v_and_b32 v50, v50, v11\n\t"
    "v_and_b32 v51, v51, v11\n\t"
    "s_waitcnt lgkmcnt(6)\n\t"
    "v_mfma_f32_16x16x32_bf16 v[112:115], v[32:35], v[216:219], v[112:115]\n\t"
    "v_add_f32 v56, v56, v40\n\t"
    "v_add_f32 v57, v57, v41\n\t"
    "v_add_f32 v58, v58, v42\n\t"
    "v_add_f32 v59, v59, v43\n\t"
    "v_mfma_f32_16x16x32_bf16 v[116:119], v[32:35], v[220:223], v[116:119]\n\t"
    "v_add_u32 v0, 0xffff8000, v0\n\t"
    "ds_read_b64_tr_b16 v[32:33], v0 offset:0\n\t"
    "ds_read_b64_tr_b16 v[34:35], v0 offset:2048\n\t"
    "v_add_f32 v44, v44, v48\n\t"
    "v_add_f32 v45, v45, v49\n\t"
    "v_add_f32 v46, v46, v50\n\t"
    "v_add_f32 v47, v47, v51\n\t"
    "s_waitcnt lgkmcnt(6)\n\t"
    "v_mfma_f32_16x16x32_bf16 v[112:115], v[36:39], v[224:227], v[112:115]\n\t"
    "v_add_f32 v56, v56, v44\n\t"
    "v_add_f32 v57, v57, v45\n\t"
    "v_add_f32 v58, v58, v46\n\t"
    "v_add_f32 v59, v59, v47\n\t"
    "v_mfma_f32_16x16x32_bf16 v[116:119], v[36:39], v[228:231], v[116:119]\n\t"
    "v_add_u32 v0, 0xffffe000, v0\n\t"
    "ds_read_b64_tr_b16 v[36:37], v0 offset:0\n\t"
    "v_cvt_pk_bf16_f32 v56, v56, v57\n\t"
    "v_cvt_pk_bf16_f32 v57, v58, v59\n\t"
    "buffer_store_dwordx2 v[56:57], v9, s[32:35], s68 offen\n\t"
    "s_waitcnt lgkmcnt(5)\n\t"
    "v_mfma_f32_16x16x32_bf16 v[112:115], v[24:27], v[232:235], v[112:115]\n\t"
    "ds_read_b64_tr_b16 v[38:39], v0 offset:2048\n\t"
    "v_mfma_f32_16x16x32_bf16 v[116:119], v[24:27], v[236:239], v[116:119]\n\t"
    "v_add_u32 v0, 0xffffe000, v0\n\t"
    "ds_read_b64_tr_b16 v[24:25], v0 offset:0\n\t"
    "s_waitcnt lgkmcnt(5)\n\t"
    "v_mfma_f32_16x16x32_bf16 v[112:115], v[28:31], v[240:243], v[112:115]\n\t"
    "ds_read_b64_tr_b16 v[26:27], v0 offset:2048\n\t"
    "v_mfma_f32_16x16x32_bf16 v[116:119], v[28:31], v[244:247], v[116:119]\n\t"
    "v_add_u32 v0, 0xffffe000, v0\n\t"
    "ds_read_b64_tr_b16 v[28:29], v0 offset:0\n\t"
    "s_setprio 2\n\t"
    "s_waitcnt lgkmcnt(5)\n\t"
    "v_mfma_f32_16x16x32_bf16 v[128:131], v[32:35], v[144:147], v[128:131]\n\t"
    "ds_read_b64_tr_b16 v[30:31], v0 offset:2048\n\t"
    "v_mfma_f32_16x16x32_bf16 v[132:135], v[32:35], v[148:151], v[132:135]\n\t"
    "ds_read_b64_tr_b16 v[32:33], v0 offset:49152\n\t"
    "s_waitcnt lgkmcnt(5)\n\t"
    "v_mfma_f32_16x16x32_bf16 v[128:131], v[36:39], v[152:155], v[128:131]\n\t"
    "ds_read_b64_tr_b16 v[34:35], v0 offset:51200\n\t"
    "v_mfma_f32_16x16x32_bf16 v[132:135], v[36:39], v[156:159], v[132:135]\n\t"
    "v_add_u32 v0, 0x18000, v0\n\t"
    "ds_read_b64_tr_b16 v[36:37], v0 offset:0\n\t"
    "s_setprio 1\n\t"
    "s_waitcnt lgkmcnt(5)\n\t"
    "v_mfma_f32_16x16x32_bf16 v[128:131], v[24:27], v[160:163], v[128:131]\n\t"
    "ds_read_b64_tr_b16 v[38:39], v0 offset:2048\n\t"
    "v_mfma_f32_16x16x32_bf16 v[132:135], v[24:27], v[164:167], v[132:135]\n\t"
    "v_add_u32 v0, 0xffffe000, v0\n\t"
    "ds_read_b64_tr_b16 v[24:25], v0 offset:0\n\t"
    "ds_write_b128 v4, v[112:115] offset:32768\n\t"
    "s_waitcnt lgkmcnt(6)\n\t"
    "v_mfma_f32_16x16x32_bf16 v[128:131], v[28:31], v[168:171], v[128:131]\n\t"
    "ds_read_b64_tr_b16 v[26:27], v0 offset:2048\n\t"
    "v_mfma_f32_16x16x32_bf16 v[132:135], v[28:31], v[172:175], v[132:135]\n\t"
    "v_add_u32 v0, 0xffff4000, v0\n\t"
    "ds_read_b64_tr_b16 v[28:29], v0 offset:0\n\t"
    "ds_write_b128 v4, v[116:119] offset:33792\n\t"
    "s_setprio 0\n\t"
    "s_waitcnt lgkmcnt(7)\n\t"
    "v_mfma_f32_16x16x32_bf16 v[128:131], v[32:35], v[104:107], v[128:131]\n\t"
    "ds_read_b64_tr_b16 v[30:31], v0 offset:2048\n\t"
    "v_mfma_f32_16x16x32_bf16 v[132:135], v[32:35], v[108:111], v[132:135]\n\t"
    "v_add_u32 v0, 0xe000, v0\n\t"
    "ds_read_b64_tr_b16 v[32:33], v0 offset:24576\n\t"
    "s_waitcnt lgkmcnt(7)\n\t"
    "v_mfma_f32_16x16x32_bf16 v[128:131], v[36:39], v[176:179], v[128:131]\n\t"
    "ds_read_b64_tr_b16 v[34:35], v0 offset:26624\n\t"
    "v_mfma_f32_16x16x32_bf16 v[132:135], v[36:39], v[180:183], v[132:135]\n\t"
    "ds_read_b64_tr_b16 v[36:37], v0 offset:16384\n\t"
    "s_waitcnt lgkmcnt(4)\n\t"
    "s_barrier\n\t"
    "s_setprio 3\n\t"
    "v_mfma_f32_16x16x32_bf16 v[128:131], v[24:27], v[184:187], v[128:131]\n\t"
    "ds_read_b128 v[112:115], v8 offset:32768\n\t"
    "s_waitcnt vmcnt(4)\n\t"
    "v_add3_u32 v65, v52, v53, v54\n\t"
    "v_add3_u32 v65, v65, v55, s1\n\t"
    "s_waitcnt vmcnt(3)\n\t"
    "v_xor_b32 v12, v65, v64\n\t"
    "s_waitcnt vmcnt(2)\n\t"
    "v_add3_u32 v73, v68, v69, v70\n\t"
    "v_add3_u32 v73, v73, v71, s2\n\t"
    "s_waitcnt vmcnt(1)\n\t"
    "v_xor_b32 v73, v73, v72\n\t"
    "v_or_b32 v12, v12, v73\n\t"
    "v_cmp_lt_u32 vcc, s3, v12\n\t"
    "s_cbranch_vccnz LB33_%=\n\t"
    "LB34_%=:\n\t"
    "ds_read_b64_tr_b16 v[38:39], v0 offset:18432\n\t"
    "v_mfma_f32_16x16x32_bf16 v[132:135], v[24:27], v[188:191], v[132:135]\n\t"
    "ds_read_b128 v[116:119], v8 offset:33792\n\t"
    "s_add_u32 s85, s64, 0\n\t"
    "buffer_load_dwordx4 v[76:79], v6, s[28:31], s85 offen sc1\n\t"
    "v_add_u32 v0, 0xffff0000, v0\n\t"
    "ds_read_b64_tr_b16 v[24:25], v0 offset:0\n\t"
    "s_waitcnt lgkmcnt(7)\n\t"
    "v_mfma_f32_16x16x32_bf16 v[128:131], v[28:31], v[192:195], v[128:131]\n\t"
    "ds_read_b64_tr_b16 v[26:27], v0 offset:2048\n\t"
    "v_mfma_f32_16x16x32_bf16 v[132:135], v[28:31], v[196:199], v[132:135]\n\t"
    "v_lshlrev_b32 v64, 23, v64\n\t"
    "v_mul_f32 v64, s4, v64\n\t"
    "v_cvt_f32_i32_sdwa v66, sext(v52) dst_sel:DWORD dst_unused:UNUSED_PAD src0_sel:WORD_0\n\t"
    "v_cvt_f32_i32_sdwa v67, sext(v52) dst_sel:DWORD dst_unused:UNUSED_PAD src0_sel:WORD_1\n\t"
    "s_add_u32 s85, s64, 0\n\t"
    "buffer_load_dword v80, v7, s[28:31], s85 offen offset:1024 sc1\n\t"
    "ds_read_b64_tr_b16 v[28:29], v0 offset:49152\n\t"
    "s_setprio 2\n\t"
    "s_waitcnt lgkmcnt(7)\n\t"
    "v_mfma_f32_16x16x32_bf16 v[128:131], v[32:35], v[200:203], v[128:131]\n\t"
    "s_waitcnt lgkmcnt(3)\n\t"
    "v_fmac_f32 v116, v66, v64\n\t"
    "v_fmac_f32 v117, v67, v64\n\t"
    "v_cvt_f32_i32_sdwa v66, sext(v53) dst_sel:DWORD dst_unused:UNUSED_PAD src0_sel:WORD_0\n\t"
    "v_cvt_f32_i32_sdwa v67, sext(v53) dst_sel:DWORD dst_unused:UNUSED_PAD src0_sel:WORD_1\n\t"
    "ds_read_b64_tr_b16 v[30:31], v0 offset:51200\n\t"
    "v_mfma_f32_16x16x32_bf16 v[132:135], v[32:35], v[204:207], v[132:135]\n\t"
    "v_fmac_f32 v118, v66, v64\n\t"
    "v_fmac_f32 v119, v67, v64\n\t"
    "v_cvt_f32_i32_sdwa v66, sext(v54) dst_sel:DWORD dst_unused:UNUSED_PAD src0_sel:WORD_0\n\t"
    "v_cvt_f32_i32_sdwa v67, sext(v54) dst_sel:DWORD dst_unused:UNUSED_PAD src0_sel:WORD_1\n\t"
    "v_add_u32 v0, 0x10000, v0\n\t"
    "ds_read_b64_tr_b16 v[32:33], v0 offset:8192\n\t"
    "v_mfma_f32_16x16x32_bf16 v[128:131], v[36:39], v[208:211], v[128:131]\n\t"
    "v_fmac_f32 v112, v66, v64\n\t"
    "v_fmac_f32 v113, v67, v64\n\t"
    "v_cvt_f32_i32_sdwa v66, sext(v55) dst_sel:DWORD dst_unused:UNUSED_PAD src0_sel:WORD_0\n\t"
    "v_cvt_f32_i32_sdwa v67, sext(v55) dst_sel:DWORD dst_unused:UNUSED_PAD src0_sel:WORD_1\n\t"
    "ds_read_b64_tr_b16 v[34:35], v0 offset:10240\n\t"
    "v_mfma_f32_16x16x32_bf16 v[132:135], v[36:39], v[212:215], v[132:135]\n\t"
    "v_fmac_f32 v114, v66, v64\n\t"
    "v_fmac_f32 v115, v67, v64\n\t"
    "v_lshlrev_b32 v72, 23, v72\n\t"
    "v_mul_f32 v72, s4, v72\n\t"
    "v_add_u32 v0, 0xffffa000, v0\n\t"
    "ds_read_b64_tr_b16 v[36:37], v0 offset:0\n\t"
    "s_setprio 1\n\t"
    "s_waitcnt lgkmcnt(5)\n\t"
    "v_mfma_f32_16x16x32_bf16 v[128:131], v[24:27], v[216:219], v[128:131]\n\t"
    "v_cvt_f32_i32_sdwa v74, sext(v68) dst_sel:DWORD dst_unused:UNUSED_PAD src0_sel:WORD_0\n\t"
    "v_cvt_f32_i32_sdwa v75, sext(v68) dst_sel:DWORD dst_unused:UNUSED_PAD src0_sel:WORD_1\n\t"
    "v_fmac_f32 v116, v74, v72\n\t"
    "v_fmac_f32 v117, v75, v72\n\t"
    "ds_read_b64_tr_b16 v[38:39], v0 offset:2048\n\t"
    "v_mfma_f32_16x16x32_bf16 v[132:135], v[24:27], v[220:223], v[132:135]\n\t"
    "v_cvt_f32_i32_sdwa v74, sext(v69) dst_sel:DWORD dst_unused:UNUSED_PAD src0_sel:WORD_0\n\t"
    "v_cvt_f32_i32_sdwa v75, sext(v69) dst_sel:DWORD dst_unused:UNUSED_PAD src0_sel:WORD_1\n\t"
    "v_fmac_f32 v118, v74, v72\n\t"
    "v_fmac_f32 v119, v75, v72\n\t"
    "s_waitcnt lgkmcnt(4)\n\t"
    "v_mfma_f32_16x16x32_bf16 v[128:131], v[28:31], v[224:227], v[128:131]\n\t"
    "v_cvt_f32_i32_sdwa v74, sext(v70) dst_sel:DWORD dst_unused:UNUSED_PAD src0_sel:WORD_0\n\t"
    "v_cvt_f32_i32_sdwa v75, sext(v70) dst_sel:DWORD dst_unused:UNUSED_PAD src0_sel:WORD_1\n\t"
    "v_fmac_f32 v112, v74, v72\n\t"
    "v_fmac_f32 v113, v75, v72\n\t"
    "v_mfma_f32_16x16x32_bf16 v[132:135], v[28:31], v[228:231], v[132:135]\n\t"
    "v_cvt_f32_i32_sdwa v74, sext(v71) dst_sel:DWORD dst_unused:UNUSED_PAD src0_sel:WORD_0\n\t"
    "v_cvt_f32_i32_sdwa v75, sext(v71) dst_sel:DWORD dst_unused:UNUSED_PAD src0_sel:WORD_1\n\t"
    "v_fmac_f32 v114, v74, v72\n\t"
    "v_fmac_f32 v115, v75, v72\n\t"
    "s_setprio 0\n\t"
    "s_waitcnt lgkmcnt(2)\n\t"
    "v_mfma_f32_16x16x32_bf16 v[128:131], v[32:35], v[232:235], v[128:131]\n\t"
    "v_mfma_f32_16x16x32_bf16 v[132:135], v[32:35], v[236:239], v[132:135]\n\t"
    "s_waitcnt lgkmcnt(0)\n\t"
    "v_mfma_f32_16x16x32_bf16 v[128:131], v[36:39], v[240:243], v[128:131]\n\t"
    "v_mfma_f32_16x16x32_bf16 v[132:135], v[36:39], v[244:247], v[132:135]\n\t"
    "s_nop 6\n\t"
    "v_add_f32 v112, v112, v128\n\t"
    "v_add_f32 v113, v113, v129\n\t"
    "v_add_f32 v114, v114, v130\n\t"
    "v_add_f32 v115, v115, v131\n\t"
    "v_add_f32 v116, v116, v132\n\t"
    "v_add_f32 v117, v117, v133\n\t"
    "v_add_f32 v118, v118, v134\n\t"
    "v_add_f32 v119, v119, v135\n\t"
    "s_waitcnt vmcnt(1)\n\t"
    "v_add3_u32 v81, v76, v77, v78\n\t"
    "v_add3_u32 v81, v81, v79, s0\n\t"
    "s_waitcnt vmcnt(0)\n\t"
    "v_xor_b32 v12, v81, v80\n\t"
    "v_cmp_lt_u32 vcc, s3, v12\n\t"
    "s_cbranch_vccnz LB35_%=\n\t"
    "LB36_%=:\n\t"
    "v_lshlrev_b32 v80, 23, v80\n\t"
    "v_mul_f32 v80, s4, v80\n\t"
    "v_cvt_f32_i32_sdwa v82, sext(v76) dst_sel:DWORD dst_unused:UNUSED_PAD src0_sel:WORD_0\n\t"
    "v_cvt_f32_i32_sdwa v83, sext(v76) dst_sel:DWORD dst_unused:UNUSED_PAD src0_sel:WORD_1\n\t"
    "v_fmac_f32 v112, v82, v80\n\t"
    "v_fmac_f32 v113, v83, v80\n\t"
    "v_cvt_f32_i32_sdwa v82, sext(v77) dst_sel:DWORD dst_unused:UNUSED_PAD src0_sel:WORD_0\n\t"
    "v_cvt_f32_i32_sdwa v83, sext(v77) dst_sel:DWORD dst_unused:UNUSED_PAD src0_sel:WORD_1\n\t"
    "v_fmac_f32 v114, v82, v80\n\t"
    "v_fmac_f32 v115, v83, v80\n\t"
    "v_cvt_f32_i32_sdwa v82, sext(v78) dst_sel:DWORD dst_unused:UNUSED_PAD src0_sel:WORD_0\n\t"
    "v_cvt_f32_i32_sdwa v83, sext(v78) dst_sel:DWORD dst_unused:UNUSED_PAD src0_sel:WORD_1\n\t"
    "v_fmac_f32 v116, v82, v80\n\t"
    "v_fmac_f32 v117, v83, v80\n\t"
    "v_cvt_f32_i32_sdwa v82, sext(v79) dst_sel:DWORD dst_unused:UNUSED_PAD src0_sel:WORD_0\n\t"
    "v_cvt_f32_i32_sdwa v83, sext(v79) dst_sel:DWORD dst_unused:UNUSED_PAD src0_sel:WORD_1\n\t"
    "v_fmac_f32 v118, v82, v80\n\t"
    "v_fmac_f32 v119, v83, v80\n\t"
    "v_cvt_pk_bf16_f32 v112, v112, v113\n\t"
    "v_cvt_pk_bf16_f32 v113, v114, v115\n\t"
    "buffer_store_dwordx2 v[112:113], v9, s[32:35], s62 offen\n\t"
    "v_cvt_pk_bf16_f32 v116, v116, v117\n\t"
    "v_cvt_pk_bf16_f32 v117, v118, v119\n\t"
    "buffer_store_dwordx2 v[116:117], v9, s[32:35], s67 offen\n\t"
    "s_cmp_lg_u32 s89, 0\n\t"
    "s_cbranch_scc1 LB37_%=\n\t"
    "s_add_u32 s84, s56, 1\n\t"
    "v_mov_b32 v12, s84\n\t"
    "s_mov_b64 exec, 1\n\t"
    "buffer_store_dword v12, off, s[28:31], s95 sc1\n\t"
    "s_mov_b64 exec, -1\n\t"
    "LB37_%=:\n\t"
    "s_branch LEND_%=\n\t"
    "LB1_%=:\n\t"
    "s_cmp_eq_u32 s63, 0\n\t"
    "s_cbranch_scc1 LB2_%=\n\t"
    "s_sub_u32 s63, s63, 1\n\t"
    "s_sleep 1\n\t"
    "s_add_u32 s85, s43, 0x0\n\t"
    "buffer_load_dwordx4 v[104:107], v6, s[28:31], s85 offen sc1\n\t"
    "buffer_load_dwordx4 v[108:111], v6, s[28:31], s85 offen offset:1024 sc1\n\t"
    "buffer_load_dword v16, v7, s[28:31], s85 offen offset:2048 sc1\n\t"
    "s_waitcnt vmcnt(0)\n\t"
    "v_add3_u32 v16, v104, v105, v16\n\t"
    "v_add3_u32 v16, v106, v107, v16\n\t"
    "v_add3_u32 v16, v108, v109, v16\n\t"
    "v_add3_u32 v16, v110, v111, v16\n\t"
    "v_cmp_ne_u32 vcc, s48, v16\n\t"
    "s_cbranch_vccnz LB1_%=\n\t"
    "s_nop 4\n\t"
    "s_branch LB2_%=\n\t"
    "LB3_%=:\n\t"
    "s_cmp_eq_u32 s63, 0\n\t"
    "s_cbranch_scc1 LB4_%=\n\t"
    "s_sub_u32 s63, s63, 1\n\t"
    "s_sleep 1\n\t"
    "s_add_u32 s85, s44, 0x0\n\t"
    "buffer_load_dwordx4 v[144:147], v6, s[28:31], s85 offen sc1\n\t"
    "buffer_load_dwordx4 v[148:151], v6, s[28:31], s85 offen offset:1024 sc1\n\t"
    "buffer_load_dword v17, v7, s[28:31], s85 offen offset:2048 sc1\n\t"
    "s_waitcnt vmcnt(0)\n\t"
    "v_add3_u32 v17, v144, v145, v17\n\t"
    "v_add3_u32 v17, v146, v147, v17\n\t"
    "v_add3_u32 v17, v148, v149, v17\n\t"
    "v_add3_u32 v17, v150, v151, v17\n\t"
    "v_cmp_ne_u32 vcc, s49, v17\n\t"
    "s_cbranch_vccnz LB3_%=\n\t"
    "s_nop 4\n\t"
    "s_branch LB4_%=\n\t"
    "LB5_%=:\n\t"
    "s_cmp_eq_u32 s63, 0\n\t"
    "s_cbranch_scc1 LB6_%=\n\t"
    "s_sub_u32 s63, s63, 1\n\t"
    "s_sleep 1\n\t"
    "s_add_u32 s85, s45, 0x0\n\t"
    "buffer_load_dwordx4 v[152:155], v6, s[28:31], s85 offen sc1\n\t"
    "buffer_load_dwordx4 v[156:159], v6, s[28:31], s85 offen offset:1024 sc1\n\t"
    "buffer_load_dword v18, v7, s[28:31], s85 offen offset:2048 sc1\n\t"
    "s_waitcnt vmcnt(0)\n\t"
    "v_add3_u32 v18, v152, v153, v18\n\t"
    "v_add3_u32 v18, v154, v155, v18\n\t"
    "v_add3_u32 v18, v156, v157, v18\n\t"
    "v_add3_u32 v18, v158, v159, v18\n\t"
    "v_cmp_ne_u32 vcc, s50, v18\n\t"
    "s_cbranch_vccnz LB5_%=\n\t"
    "s_nop 4\n\t"
    "s_branch LB6_%=\n\t"
    "LB7_%=:\n\t"
    "s_cmp_eq_u32 s63, 0\n\t"
    "s_cbranch_scc1 LB8_%=\n\t"
    "s_sub_u32 s63, s63, 1\n\t"
    "s_sleep 1\n\t"
    "s_add_u32 s85, s43, 0x4800\n\t"
    "buffer_load_dwordx4 v[160:163], v6, s[28:31], s85 offen sc1\n\t"
    "buffer_load_dwordx4 v[164:167], v6, s[28:31], s85 offen offset:1024 sc1\n\t"
    "buffer_load_dword v19, v7, s[28:31], s85 offen offset:2048 sc1\n\t"
    "s_waitcnt vmcnt(0)\n\t"
    "v_add3_u32 v19, v160, v161, v19\n\t"
    "v_add3_u32 v19, v162, v163, v19\n\t"
    "v_add3_u32 v19, v164, v165, v19\n\t"
    "v_add3_u32 v19, v166, v167, v19\n\t"
    "v_cmp_ne_u32 vcc, s48, v19\n\t"
    "s_cbranch_vccnz LB7_%=\n\t"
    "s_nop 4\n\t"
    "s_branch LB8_%=\n\t"
    "LB9_%=:\n\t"
    "s_cmp_eq_u32 s63, 0\n\t"
    "s_cbranch_scc1 LB10_%=\n\t"
    "s_sub_u32 s63, s63, 1\n\t"
    "s_sleep 1\n\t"
    "s_add_u32 s85, s44, 0x4800\n\t"
    "buffer_load_dwordx4 v[168:171], v6, s[28:31], s85 offen sc1\n\t"
    "buffer_load_dwordx4 v[172:175], v6, s[28:31], s85 offen offset:1024 sc1\n\t"
    "buffer_load_dword v20, v7, s[28:31], s85 offen offset:2048 sc1\n\t"
    "s_waitcnt vmcnt(0)\n\t"
    "v_add3_u32 v20, v168, v169, v20\n\t"
    "v_add3_u32 v20, v170, v171, v20\n\t"
    "v_add3_u32 v20, v172, v173, v20\n\t"
    "v_add3_u32 v20, v174, v175, v20\n\t"
    "v_cmp_ne_u32 vcc, s49, v20\n\t"
    "s_cbranch_vccnz LB9_%=\n\t"
    "s_nop 4\n\t"
    "s_branch LB10_%=\n\t"
    "LB11_%=:\n\t"
    "s_cmp_eq_u32 s63, 0\n\t"
    "s_cbranch_scc1 LB12_%=\n\t"
    "s_sub_u32 s63, s63, 1\n\t"
    "s_sleep 1\n\t"
    "s_add_u32 s85, s45, 0x4800\n\t"
    "buffer_load_dwordx4 v[104:107], v6, s[28:31], s85 offen sc1\n\t"
    "buffer_load_dwordx4 v[108:111], v6, s[28:31], s85 offen offset:1024 sc1\n\t"
    "buffer_load_dword v21, v7, s[28:31], s85 offen offset:2048 sc1\n\t"
    "s_waitcnt vmcnt(0)\n\t"
    "v_add3_u32 v21, v104, v105, v21\n\t"
    "v_add3_u32 v21, v106, v107, v21\n\t"
    "v_add3_u32 v21, v108, v109, v21\n\t"
    "v_add3_u32 v21, v110, v111, v21\n\t"
    "v_cmp_ne_u32 vcc, s50, v21\n\t"
    "s_cbranch_vccnz LB11_%=\n\t"
    "s_nop 4\n\t"
    "s_branch LB12_%=\n\t"
    "LB13_%=:\n\t"
    "s_cmp_eq_u32 s63, 0\n\t"
    "s_cbranch_scc1 LB14_%=\n\t"
    "s_sub_u32 s63, s63, 1\n\t"
    "s_sleep 1\n\t"
    "s_add_u32 s85, s43, 0x9000\n\t"
    "buffer_load_dwordx4 v[176:179], v6, s[28:31], s85 offen sc1\n\t"
    "buffer_load_dwordx4 v[180:183], v6, s[28:31], s85 offen offset:1024 sc1\n\t"
    "buffer_load_dword v22, v7, s[28:31], s85 offen offset:2048 sc1\n\t"
    "s_waitcnt vmcnt(0)\n\t"
    "v_add3_u32 v22, v176, v177, v22\n\t"
    "v_add3_u32 v22, v178, v179, v22\n\t"
    "v_add3_u32 v22, v180, v181, v22\n\t"
    "v_add3_u32 v22, v182, v183, v22\n\t"
    "v_cmp_ne_u32 vcc, s48, v22\n\t"
    "s_cbranch_vccnz LB13_%=\n\t"
    "s_nop 4\n\t"
    "s_branch LB14_%=\n\t"
    "LB15_%=:\n\t"
    "s_cmp_eq_u32 s63, 0\n\t"
    "s_cbranch_scc1 LB16_%=\n\t"
    "s_sub_u32 s63, s63, 1\n\t"
    "s_sleep 1\n\t"
    "s_add_u32 s85, s44, 0x9000\n\t"
    "buffer_load_dwordx4 v[184:187], v6, s[28:31], s85 offen sc1\n\t"
    "buffer_load_dwordx4 v[188:191], v6, s[28:31], s85 offen offset:1024 sc1\n\t"
    "buffer_load_dword v23, v7, s[28:31], s85 offen offset:2048 sc1\n\t"
    "s_waitcnt vmcnt(0)\n\t"
    "v_add3_u32 v23, v184, v185, v23\n\t"
    "v_add3_u32 v23, v186, v187, v23\n\t"
    "v_add3_u32 v23, v188, v189, v23\n\t"
    "v_add3_u32 v23, v190, v191, v23\n\t"
    "v_cmp_ne_u32 vcc, s49, v23\n\t"
    "s_cbranch_vccnz LB15_%=\n\t"
    "s_nop 4\n\t"
    "s_branch LB16_%=\n\t"
    "LB17_%=:\n\t"
    "s_cmp_eq_u32 s63, 0\n\t"
    "s_cbranch_scc1 LB18_%=\n\t"
    "s_sub_u32 s63, s63, 1\n\t"
    "s_sleep 1\n\t"
    "s_add_u32 s85, s45, 0x9000\n\t"
    "buffer_load_dwordx4 v[192:195], v6, s[28:31], s85 offen sc1\n\t"
    "buffer_load_dwordx4 v[196:199], v6, s[28:31], s85 offen offset:1024 sc1\n\t"
    "buffer_load_dword v16, v7, s[28:31], s85 offen offset:2048 sc1\n\t"
    "s_waitcnt vmcnt(0)\n\t"
    "v_add3_u32 v16, v192, v193, v16\n\t"
    "v_add3_u32 v16, v194, v195, v16\n\t"
    "v_add3_u32 v16, v196, v197, v16\n\t"
    "v_add3_u32 v16, v198, v199, v16\n\t"
    "v_cmp_ne_u32 vcc, s50, v16\n\t"
    "s_cbranch_vccnz LB17_%=\n\t"
    "s_nop 4\n\t"
    "s_branch LB18_%=\n\t"
    "LB19_%=:\n\t"
    "s_cmp_eq_u32 s63, 0\n\t"
    "s_cbranch_scc1 LB20_%=\n\t"
    "s_sub_u32 s63, s63, 1\n\t"
    "s_sleep 1\n\t"
    "s_add_u32 s85, s43, 0xd800\n\t"
    "buffer_load_dwordx4 v[200:203], v6, s[28:31], s85 offen sc1\n\t"
    "buffer_load_dwordx4 v[204:207], v6, s[28:31], s85 offen offset:1024 sc1\n\t"
    "buffer_load_dword v17, v7, s[28:31], s85 offen offset:2048 sc1\n\t"
    "s_waitcnt vmcnt(0)\n\t"
    "v_add3_u32 v17, v200, v201, v17\n\t"
    "v_add3_u32 v17, v202, v203, v17\n\t"
    "v_add3_u32 v17, v204, v205, v17\n\t"
    "v_add3_u32 v17, v206, v207, v17\n\t"
    "v_cmp_ne_u32 vcc, s48, v17\n\t"
    "s_cbranch_vccnz LB19_%=\n\t"
    "s_nop 4\n\t"
    "s_branch LB20_%=\n\t"
    "LB21_%=:\n\t"
    "s_cmp_eq_u32 s63, 0\n\t"
    "s_cbranch_scc1 LB22_%=\n\t"
    "s_sub_u32 s63, s63, 1\n\t"
    "s_sleep 1\n\t"
    "s_add_u32 s85, s44, 0xd800\n\t"
    "buffer_load_dwordx4 v[208:211], v6, s[28:31], s85 offen sc1\n\t"
    "buffer_load_dwordx4 v[212:215], v6, s[28:31], s85 offen offset:1024 sc1\n\t"
    "buffer_load_dword v18, v7, s[28:31], s85 offen offset:2048 sc1\n\t"
    "s_waitcnt vmcnt(0)\n\t"
    "v_add3_u32 v18, v208, v209, v18\n\t"
    "v_add3_u32 v18, v210, v211, v18\n\t"
    "v_add3_u32 v18, v212, v213, v18\n\t"
    "v_add3_u32 v18, v214, v215, v18\n\t"
    "v_cmp_ne_u32 vcc, s49, v18\n\t"
    "s_cbranch_vccnz LB21_%=\n\t"
    "s_nop 4\n\t"
    "s_branch LB22_%=\n\t"
    "LB23_%=:\n\t"
    "s_cmp_eq_u32 s63, 0\n\t"
    "s_cbranch_scc1 LB24_%=\n\t"
    "s_sub_u32 s63, s63, 1\n\t"
    "s_sleep 1\n\t"
    "s_add_u32 s85, s45, 0xd800\n\t"
    "buffer_load_dwordx4 v[216:219], v6, s[28:31], s85 offen sc1\n\t"
    "buffer_load_dwordx4 v[220:223], v6, s[28:31], s85 offen offset:1024 sc1\n\t"
    "buffer_load_dword v19, v7, s[28:31], s85 offen offset:2048 sc1\n\t"
    "s_waitcnt vmcnt(0)\n\t"
    "v_add3_u32 v19, v216, v217, v19\n\t"
    "v_add3_u32 v19, v218, v219, v19\n\t"
    "v_add3_u32 v19, v220, v221, v19\n\t"
    "v_add3_u32 v19, v222, v223, v19\n\t"
    "v_cmp_ne_u32 vcc, s50, v19\n\t"
    "s_cbranch_vccnz LB23_%=\n\t"
    "s_nop 4\n\t"
    "s_branch LB24_%=\n\t"
    "LB25_%=:\n\t"
    "s_cmp_eq_u32 s63, 0\n\t"
    "s_cbranch_scc1 LB26_%=\n\t"
    "s_sub_u32 s63, s63, 1\n\t"
    "s_sleep 1\n\t"
    "s_add_u32 s85, s43, 0x12000\n\t"
    "buffer_load_dwordx4 v[224:227], v6, s[28:31], s85 offen sc1\n\t"
    "buffer_load_dwordx4 v[228:231], v6, s[28:31], s85 offen offset:1024 sc1\n\t"
    "buffer_load_dword v20, v7, s[28:31], s85 offen offset:2048 sc1\n\t"
    "s_waitcnt vmcnt(0)\n\t"
    "v_add3_u32 v20, v224, v225, v20\n\t"
    "v_add3_u32 v20, v226, v227, v20\n\t"
    "v_add3_u32 v20, v228, v229, v20\n\t"
    "v_add3_u32 v20, v230, v231, v20\n\t"
    "v_cmp_ne_u32 vcc, s48, v20\n\t"
    "s_cbranch_vccnz LB25_%=\n\t"
    "s_nop 4\n\t"
    "s_branch LB26_%=\n\t"
    "LB27_%=:\n\t"
    "s_cmp_eq_u32 s63, 0\n\t"
    "s_cbranch_scc1 LB28_%=\n\t"
    "s_sub_u32 s63, s63, 1\n\t"
    "s_sleep 1\n\t"
    "s_add_u32 s85, s44, 0x12000\n\t"
    "buffer_load_dwordx4 v[232:235], v6, s[28:31], s85 offen sc1\n\t"
    "buffer_load_dwordx4 v[236:239], v6, s[28:31], s85 offen offset:1024 sc1\n\t"
    "buffer_load_dword v21, v7, s[28:31], s85 offen offset:2048 sc1\n\t"
    "s_waitcnt vmcnt(0)\n\t"
    "v_add3_u32 v21, v232, v233, v21\n\t"
    "v_add3_u32 v21, v234, v235, v21\n\t"
    "v_add3_u32 v21, v236, v237, v21\n\t"
    "v_add3_u32 v21, v238, v239, v21\n\t"
    "v_cmp_ne_u32 vcc, s49, v21\n\t"
    "s_cbranch_vccnz LB27_%=\n\t"
    "s_nop 4\n\t"
    "s_branch LB28_%=\n\t"
    "LB29_%=:\n\t"
    "s_cmp_eq_u32 s63, 0\n\t"
    "s_cbranch_scc1 LB30_%=\n\t"
    "s_sub_u32 s63, s63, 1\n\t"
    "s_sleep 1\n\t"
    "s_add_u32 s85, s45, 0x12000\n\t"
    "buffer_load_dwordx4 v[240:243], v6, s[28:31], s85 offen sc1\n\t"
    "buffer_load_dwordx4 v[244:247], v6, s[28:31], s85 offen offset:1024 sc1\n\t"
    "buffer_load_dword v22, v7, s[28:31], s85 offen offset:2048 sc1\n\t"
    "s_waitcnt vmcnt(0)\n\t"
    "v_add3_u32 v22, v240, v241, v22\n\t"
    "v_add3_u32 v22, v242, v243, v22\n\t"
    "v_add3_u32 v22, v244, v245, v22\n\t"
    "v_add3_u32 v22, v246, v247, v22\n\t"
    "v_cmp_ne_u32 vcc, s50, v22\n\t"
    "s_cbranch_vccnz LB29_%=\n\t"
    "s_nop 4\n\t"
    "s_branch LB30_%=\n\t"
    "LB31_%=:\n\t"
    "s_cmp_eq_u32 s63, 0\n\t"
    "s_cbranch_scc1 LB32_%=\n\t"
    "s_sub_u32 s63, s63, 1\n\t"
    "s_sleep 1\n\t"
    "s_add_u32 s85, s64, 0\n\t"
    "buffer_load_dwordx4 v[40:43], v6, s[28:31], s85 offen offset:2048 sc1\n\t"
    "s_add_u32 s85, s64, 24576\n\t"
    "buffer_load_dwordx4 v[44:47], v6, s[28:31], s85 offen offset:2048 sc1\n\t"
    "s_add_u32 s85, s64, 49152\n\t"
    "buffer_load_dwordx4 v[48:51], v6, s[28:31], s85 offen offset:2048 sc1\n\t"
    "s_waitcnt vmcnt(0)\n\t"
    "v_perm_b32 v12, v41, v40, s72\n\t"
    "v_perm_b32 v13, v43, v42, s73\n\t"
    "v_or_b32 v12, v12, v13\n\t"
    "v_cmp_ne_u32 s[74:75], s52, v12\n\t"
    "v_perm_b32 v12, v45, v44, s72\n\t"
    "v_perm_b32 v13, v47, v46, s73\n\t"
    "v_or_b32 v12, v12, v13\n\t"
    "v_cmp_ne_u32 s[76:77], s53, v12\n\t"
    "v_perm_b32 v12, v49, v48, s72\n\t"
    "v_perm_b32 v13, v51, v50, s73\n\t"
    "v_or_b32 v12, v12, v13\n\t"
    "v_cmp_ne_u32 s[78:79], s54, v12\n\t"
    "s_nop 1\n\t"
    "s_or_b64 s[74:75], s[74:75], s[76:77]\n\t"
    "s_or_b64 s[74:75], s[74:75], s[78:79]\n\t"
    "s_cbranch_scc1 LB31_%=\n\t"
    "s_nop 4\n\t"
    "s_branch LB32_%=\n\t"
    "LB33_%=:\n\t"
    "s_cmp_eq_u32 s63, 0\n\t"
    "s_cbranch_scc1 LB34_%=\n\t"
    "s_sub_u32 s63, s63, 1\n\t"
    "s_sleep 1\n\t"
    "s_add_u32 s85, s64, 24576\n\t"
    "buffer_load_dwordx4 v[52:55], v6, s[28:31], s85 offen sc1\n\t"
    "buffer_load_dword v64, v7, s[28:31], s85 offen offset:1024 sc1\n\t"
    "s_add_u32 s85, s64, 49152\n\t"
    "buffer_load_dwordx4 v[68:71], v6, s[28:31], s85 offen sc1\n\t"
    "buffer_load_dword v72, v7, s[28:31], s85 offen offset:1024 sc1\n\t"
    "s_waitcnt vmcnt(0)\n\t"
    "v_add3_u32 v65, v52, v53, v54\n\t"
    "v_add3_u32 v65, v65, v55, s1\n\t"
    "v_xor_b32 v12, v65, v64\n\t"
    "v_add3_u32 v73, v68, v69, v70\n\t"
    "v_add3_u32 v73, v73, v71, s2\n\t"
    "v_xor_b32 v73, v73, v72\n\t"
    "v_or_b32 v12, v12, v73\n\t"
    "v_cmp_lt_u32 vcc, s3, v12\n\t"
    "s_cbranch_vccnz LB33_%=\n\t"
    "s_nop 4\n\t"
    "s_branch LB34_%=\n\t"
    "LB35_%=:\n\t"
    "s_cmp_eq_u32 s63, 0\n\t"
    "s_cbranch_scc1 LB36_%=\n\t"
    "s_sub_u32 s63, s63, 1\n\t"
    "s_sleep 1\n\t"
    "s_add_u32 s85, s64, 0\n\t"
    "buffer_load_dwordx4 v[76:79], v6, s[28:31], s85 offen sc1\n\t"
    "buffer_load_dword v80, v7, s[28:31], s85 offen offset:1024 sc1\n\t"
    "s_waitcnt vmcnt(0)\n\t"
    "v_add3_u32 v81, v76, v77, v78\n\t"
    "v_add3_u32 v81, v81, v79, s0\n\t"
    "v_xor_b32 v12, v81, v80\n\t"
    "v_cmp_lt_u32 vcc, s3, v12\n\t"
    "s_cbranch_vccnz LB35_%=\n\t"
    "s_nop 4\n\t"
    "s_branch LB36_%=\n\t"
    "LEND_%=:\n\t"
    : "+v"(wv)
    : "s"(__builtin_amdgcn_kernarg_segment_ptr()), "s"(bid)
    : "v1", "v2", "v3", "v4", "v5", "v6", "v7", "v8", "v9", "v10", "v11", "v12", "v13", "v14", "v15", "v16", "v17", "v18", "v19", "v20", "v21", "v22", "v23", "v24", "v25", "v26", "v27", "v28", "v29", "v30", "v31", "v32", "v33", "v34", "v35", "v36", "v37", "v38", "v39", "v40", "v41", "v42", "v43", "v44", "v45", "v46", "v47", "v48", "v49", "v50", "v51", "v52", "v53", "v54", "v55", "v56", "v57", "v58", "v59", "v60", "v61", "v62", "v63", "v64", "v65", "v66", "v67", "v68", "v69", "v70", "v71", "v72", "v73", "v74", "v75", "v76", "v77", "v78", "v79", "v80", "v81", "v82", "v83", "v84", "v85", "v86", "v87", "v88", "v89", "v90", "v91", "v92", "v93", "v94", "v95", "v96", "v97", "v98", "v99", "v100", "v101", "v102", "v103", "v104", "v105", "v106", "v107", "v108", "v109", "v110", "v111", "v112", "v113", "v114", "v115", "v116", "v117", "v118", "v119", "v120", "v121", "v122", "v123", "v124", "v125", "v126", "v127", "v128", "v129", "v130", "v131", "v132", "v133", "v134", "v135", "v136", "v137", "v138", "v139", "v140", "v141", "v142", "v143", "v144", "v145", "v146", "v147", "v148", "v149", "v150", "v151", "v152", "v153", "v154", "v155", "v156", "v157", "v158", "v159", "v160", "v161", "v162", "v163", "v164", "v165", "v166", "v167", "v168", "v169", "v170", "v171", "v172", "v173", "v174", "v175", "v176", "v177", "v178", "v179", "v180", "v181", "v182", "v183", "v184", "v185", "v186", "v187", "v188", "v189", "v190", "v191", "v192", "v193", "v194", "v195", "v196", "v197", "v198", "v199", "v200", "v201", "v202", "v203", "v204", "v205", "v206", "v207", "v208", "v209", "v210", "v211", "v212", "v213", "v214", "v215", "v216", "v217", "v218", "v219", "v220", "v221", "v222", "v223", "v224", "v225", "v226", "v227", "v228", "v229", "v230", "v231", "v232", "v233", "v234", "v235", "v236", "v237", "v238", "v239", "v240", "v241", "v242", "v243", "v244", "v245", "v246", "v247", "v248", "v249", "v250", "v251", "v252", "v253", "v254", "v255", "s16", "s17", "s18", "s19", "s20", "s21", "s22", "s23", "s24", "s25", "s26", "s27", "s28", "s29", "s30", "s31", "s32", "s33", "s34", "s35", "s36", "s37", "s38", "s39", "s40", "s41", "s42", "s43", "s44", "s45", "s46", "s47", "s48", "s49", "s50", "s51", "s52", "s53", "s54", "s55", "s56", "s57", "s58", "s59", "s60", "s61", "s62", "s63", "s64", "s65", "s66", "s67", "s68", "s69", "s70", "s71", "s72", "s73", "s74", "s75", "s76", "s77", "s78", "s79", "s80", "s81", "s82", "s83", "s84", "s85", "s86", "s87", "s88", "s89", "s90", "s91", "s92", "s93", "s94", "s95", "s96", "s97", "s98", "s99", "s100", "s101", "vcc", "scc", "m0", "memory");
}

extern "C" void kernel_launch(void* const* d_in, const int* in_sizes, int n_in,
                              void* d_out, int out_size, void* d_ws, size_t ws_size,
                              hipStream_t stream) {
  (void)in_sizes; (void)n_in; (void)out_size; (void)ws_size;
  (void)hipFuncSetAttribute((const void*)fused_mlp_kernel, hipFuncAttributeMaxDynamicSharedMemorySize, 163840);
  fused_mlp_kernel<<<256, 512, 163840, stream>>>(d_in[0], d_in[1], d_in[2], d_out, (char*)d_ws + ((ws_size - 46137344) & ~(size_t)0x1fffff));
}
```
